```python
import torch

def _softmax(x, axis=-1):
    return torch.softmax(x, dim=axis)
def _gelu(x):
    return torch.nn.functional.gelu(x, approximate="tanh")
def _silu(x):
    return torch.nn.functional.silu(x)
def _sigmoid(x):
    return torch.sigmoid(x)
def _layernorm(x, axis=-1, eps=1e-5):
    m = x.mean(dim=axis, keepdim=True)
    v = x.var(dim=axis, keepdim=True, unbiased=False)
    return (x - m) / torch.sqrt(v + eps)
def _scatter_add(tgt, idx, src, axis=0):
    return tgt.clone().index_add_(axis, idx, src)
def _segment_sum(src, seg, n):
    out = torch.zeros((n,) + src.shape[1:], dtype=src.dtype)
    return out.index_add_(0, seg, src)
def _pad(x, pw):
    flat = tuple(v for pair in reversed(pw) for v in pair)
    return torch.nn.functional.pad(x, flat)

kernel_name = 'sequential_preLN__mha_block__mixer_channel_mlp__prompt'

def setup_inputs():

    _w = (lambda a: (lambda t: t.to(torch.bfloat16) if t.is_floating_point() else t)(torch.as_tensor(a)))
    def _A_setup():
        B, S, D, H = 1, 2048, 512, 32
        return {
            "x": _w((lambda *s: torch.randn(*s, dtype=torch.bfloat16) * 0.05)(B, S, D)),
            "wqkv": _w((lambda *s: torch.randn(*s, dtype=torch.bfloat16) * 0.05)(D, 3 * D)),
            "wo": _w((lambda *s: torch.randn(*s, dtype=torch.bfloat16) * 0.05)(D, D)),
        }
    _A_in = _A_setup()

    def _B_setup():
        B, S, D, F = 1, 2048, 512, 1792
        return {
            "x": _w((lambda *s: torch.randn(*s, dtype=torch.bfloat16) * 0.05)(B, S, D)),
            "w1": _w((lambda *s: torch.randn(*s, dtype=torch.bfloat16) * 3.2)(D, F) * (D ** -0.5)),
            "b1": _w((lambda *s: torch.randn(*s, dtype=torch.bfloat16) * 1.6)(F)),
            "w2": _w((lambda *s: torch.randn(*s, dtype=torch.bfloat16) * 0.05)(F, D) * (F ** -0.5)),
            "b2": _w((lambda *s: torch.randn(*s, dtype=torch.bfloat16) * 0.1)(D)),
        }
    _B_in = _B_setup()

    return {
        "A_x": _A_in["x"],
        "A_wqkv": _A_in["wqkv"],
        "A_wo": _A_in["wo"],
        "B_x": _B_in["x"],
        "B_w1": _B_in["w1"],
        "B_b1": _B_in["b1"],
        "B_w2": _B_in["w2"],
        "B_b2": _B_in["b2"],
    }

def reference(A_x, A_wqkv, A_wo, B_x, B_w1, B_b1, B_w2, B_b2):
    def _A_ref(x, wqkv, wo):
        B, S, D = x.shape
        H = 32
        Dh = D // H
        qkv = (lambda x, *d: x.permute(*d))((x @ wqkv).reshape(B, S, 3, H, Dh), 2, 0, 3, 1, 4)
        q, k, v = qkv[0], qkv[1], qkv[2]
        scores = torch.einsum("bhqd,bhkd->bhqk", q, k) * (Dh ** -0.5)
        p = _softmax(scores, axis=-1)
        o = torch.einsum("bhqk,bhkd->bhqd", p, v)
        return (lambda x, *d: x.permute(*d))(o, 0, 2, 1, 3).reshape(B, S, D) @ wo

    def _B_ref(x, w1, b1, w2, b2):
        y = _layernorm(x, axis=-1)
        h = _gelu(torch.einsum("bsd,df->bsf", y, w1) + b1)
        return torch.einsum("bsf,fd->bsd", h, w2) + b2

    x = A_x
    h = x + _A_ref(_layernorm(x, axis=-1), A_wqkv, A_wo)
    h2 = h + _B_ref(_layernorm(h, axis=-1), B_w1, B_b1, B_w2, B_b2)
    return h2
```

```cpp
#include <hip/hip_runtime.h>
#include <hip/hip_bf16.h>
typedef __hip_bfloat16 bf16;
constexpr int S = 2048, D = 512, H = 32, DH = 16, F = 1792;
__device__ float b2f(bf16 x) { return __bfloat162float(x); }
__global__ void ln_k(const float* in, float* out, int twice) {
    int row = blockIdx.x; __shared__ float red[256];
    float v0 = in[row * D + threadIdx.x], v1 = in[row * D + 256 + threadIdx.x];
    for (int it = 0; it <= twice; ++it) {
        red[threadIdx.x] = v0 + v1; __syncthreads();
        for (int s = 128; s > 0; s >>= 1) { if (threadIdx.x < s) red[threadIdx.x] += red[threadIdx.x + s]; __syncthreads(); }
        float m = red[0] / D; __syncthreads();
        v0 -= m; v1 -= m;
        red[threadIdx.x] = v0 * v0 + v1 * v1; __syncthreads();
        for (int s = 128; s > 0; s >>= 1) { if (threadIdx.x < s) red[threadIdx.x] += red[threadIdx.x + s]; __syncthreads(); }
        float r = rsqrtf(red[0] / D + 1e-5f); __syncthreads();
        v0 *= r; v1 *= r;
    }
    out[row * D + threadIdx.x] = v0; out[row * D + 256 + threadIdx.x] = v1;
}
__global__ void cvt_k(const bf16* in, float* out, int n) { int i = blockIdx.x * 256 + threadIdx.x; if (i < n) out[i] = b2f(in[i]); }
__global__ void gemm_k(const float* A, const bf16* W, const bf16* bias, const float* res, float* C, bf16* Cb, int N, int K, int act) {
    int n = blockIdx.x * 64 + (threadIdx.x & 63), m = blockIdx.y * 4 + (threadIdx.x >> 6);
    float acc = 0;
    for (int k = 0; k < K; ++k) acc += A[m * K + k] * b2f(W[k * N + n]);
    if (bias) acc += b2f(bias[n]);
    if (act) acc = 0.5f * acc * (1.f + tanhf(0.7978845608f * (acc + 0.044715f * acc * acc * acc)));
    if (res) acc += res[m * N + n];
    if (C) C[m * N + n] = acc;
    if (Cb) Cb[m * N + n] = __float2bfloat16(acc);
}
__global__ void attn_k(const float* qkv, float* o) {
    int q = blockIdx.x * 64 + threadIdx.x, h = blockIdx.y;
    float qv[DH], acc[DH]; for (int d = 0; d < DH; ++d) { qv[d] = qkv[q * 1536 + h * DH + d] * 0.25f; acc[d] = 0; }
    float m = -1e30f, l = 0;
    for (int k = 0; k < S; ++k) {
        float s = 0; for (int d = 0; d < DH; ++d) s += qv[d] * qkv[k * 1536 + 512 + h * DH + d];
        float mn = fmaxf(m, s), a = __expf(m - mn), p = __expf(s - mn);
        l = l * a + p; for (int d = 0; d < DH; ++d) acc[d] = acc[d] * a + p * qkv[k * 1536 + 1024 + h * DH + d];
        m = mn;
    }
    for (int d = 0; d < DH; ++d) o[q * D + h * DH + d] = acc[d] / l;
}
extern "C" void kernel_launch(void* const* d_in, const int* in_sizes, int n_in, void* d_out, int out_size, void* d_ws, size_t ws_size, hipStream_t stream) {
    const bf16 *x = (const bf16*)d_in[0], *wqkv = (const bf16*)d_in[1], *wo = (const bf16*)d_in[2], *w1 = (const bf16*)d_in[4], *b1 = (const bf16*)d_in[5], *w2 = (const bf16*)d_in[6], *b2 = (const bf16*)d_in[7];
    float* ws = (float*)d_ws;
    float *xf = ws, *y = xf + S * D, *qkv = y + S * D, *o = qkv + S * 1536, *h = o + S * D, *hid = h + S * D;
    cvt_k<<<S * D / 256, 256, 0, stream>>>(x, xf, S * D);
    ln_k<<<S, 256, 0, stream>>>(xf, y, 0);
    gemm_k<<<dim3(1536 / 64, S / 4), 256, 0, stream>>>(y, wqkv, nullptr, nullptr, qkv, nullptr, 1536, D, 0);
    attn_k<<<dim3(S / 64, H), 64, 0, stream>>>(qkv, o);
    gemm_k<<<dim3(D / 64, S / 4), 256, 0, stream>>>(o, wo, nullptr, xf, h, nullptr, D, D, 0);
    ln_k<<<S, 256, 0, stream>>>(h, y, 1);
    gemm_k<<<dim3(F / 64, S / 4), 256, 0, stream>>>(y, w1, b1, nullptr, hid, nullptr, F, D, 1);
    gemm_k<<<dim3(D / 64, S / 4), 256, 0, stream>>>(hid, w2, b2, h, nullptr, (bf16*)d_out, D, F, 0);
}
```

```cpp
#include <hip/hip_runtime.h>
#include <stdint.h>
#define BODY \
"s_load_dwordx16 s[8:23], %[ka], 0\n\t" \
"s_load_dwordx2 s[24:25], %[ka], 64\n\t" \
"s_mov_b32 s26, %[wid]\n\t" \
"s_mov_b32 s27, %[bid]\n\t" \
"v_mbcnt_lo_u32_b32 v0, -1, 0\n\t" \
"v_mbcnt_hi_u32_b32 v0, -1, v0\n\t" \
"s_getpc_b64 s[96:97]\n\t" \
"s_cmp_lg_u32 s26, 0\n\t" \
"s_cbranch_scc1 Lnopf_%=\n\t" \
"s_lshr_b32 s57, s27, 3\n\t" \
"s_and_b32 s57, s57, 15\n\t" \
"s_lshl_b32 s57, s57, 12\n\t" \
"v_lshlrev_b32 v255, 6, v0\n\t" \
"v_add_u32 v255, s57, v255\n\t" \
"v_min_u32 v255, 47008, v255\n\t" \
"global_load_dword v255, v255, s[96:97]\n\t" \
"Lnopf_%=:\n\t" \
"s_and_b32 s48, s27, 7\n\t" \
"s_lshr_b32 s49, s27, 3\n\t" \
"s_and_b32 s57, s48, 3\n\t" \
"s_lshl_b32 s57, s57, 1\n\t" \
"s_and_b32 s58, s49, 1\n\t" \
"s_add_u32 s46, s57, s58\n\t" \
"s_lshr_b32 s57, s48, 2\n\t" \
"s_lshl_b32 s57, s57, 4\n\t" \
"s_lshr_b32 s58, s49, 1\n\t" \
"s_add_u32 s47, s57, s58\n\t" \
"s_and_b32 s56, s26, 3\n\t" \
"s_and_b32 s54, s26, 1\n\t" \
"s_lshr_b32 s55, s56, 1\n\t" \
"v_lshlrev_b32 v1, 4, v0\n\t" \
"v_lshrrev_b32 v17, 2, v0\n\t" \
"v_and_b32 v17, 4, v17\n\t" \
"v_and_b32 v18, 7, v0\n\t" \
"v_xor_b32 v17, v17, v18\n\t" \
"v_lshlrev_b32 v2, 4, v17\n\t" \
"s_cmp_lt_u32 s26, 4\n\t" \
"s_cbranch_scc1 Lp1a_%=\n\t" \
"v_lshrrev_b32 v17, 4, v0\n\t" \
"v_xor_b32 v17, v17, v18\n\t" \
"v_lshlrev_b32 v17, 4, v17\n\t" \
"v_lshrrev_b32 v19, 3, v0\n\t" \
"v_lshl_add_u32 v6, v19, 10, v17\n\t" \
"v_xor_b32 v7, 64, v6\n\t" \
"v_mul_u32_u24 v8, 0xc00, v19\n\t" \
"v_add_u32 v8, v8, v2\n\t" \
"s_lshl_b32 s57, s47, 16\n\t" \
"s_lshl_b32 s58, s56, 14\n\t" \
"s_add_u32 s59, s57, s58\n\t" \
"s_mul_i32 s57, s56, 49152\n\t" \
"s_lshl_b32 s58, s46, 7\n\t" \
"s_add_u32 s60, s57, s58\n\t" \
"s_lshl_b32 s61, s56, 11\n\t" \
"s_mov_b32 s76, s47\n\t" \
"s_waitcnt lgkmcnt(0)\n\t" \
"s_mov_b32 s32, s8\n\t" \
"s_and_b32 s33, s9, 0xffff\n\t" \
"s_mov_b32 s34, 2097152\n\t" \
"s_mov_b32 s35, 0x20000\n\t" \
"s_mov_b32 s36, s10\n\t" \
"s_and_b32 s37, s11, 0xffff\n\t" \
"s_mov_b32 s38, 1572864\n\t" \
"s_mov_b32 s39, 0x20000\n\t" \
"s_setprio 2\n\t" \
"s_add_u32 s64, s76, 0\n\t" \
"s_and_b32 s64, s64, 7\n\t" \
"s_lshl_b32 s63, s64, 7\n\t" \
"s_add_u32 s63, s63, s59\n\t" \
"s_mul_i32 s64, s64, 196608\n\t" \
"s_add_u32 s64, s64, s60\n\t" \
"s_add_u32 m0, s61, 0\n\t" \
"s_add_u32 s62, s63, 0\n\t" \
"buffer_load_dwordx4 v6, s[32:35], s62 offen lds\n\t" \
"s_add_u32 m0, s61, 1024\n\t" \
"s_add_u32 s62, s63, 8192\n\t" \
"buffer_load_dwordx4 v7, s[32:35], s62 offen lds\n\t" \
"s_add_u32 m0, s61, 65536\n\t" \
"s_add_u32 s62, s64, 1024\n\t" \
"buffer_load_dwordx4 v8, s[36:39], s62 offen lds\n\t" \
"v_lshlrev_b32 v52, 1, v0\n\t" \
"v_and_b32 v52, 32, v52\n\t" \
"v_and_b32 v53, 3, v0\n\t" \
"s_add_u32 m0, s61, 66560\n\t" \
"s_add_u32 s62, s64, 25600\n\t" \
"buffer_load_dwordx4 v8, s[36:39], s62 offen lds\n\t" \
"v_lshl_add_u32 v52, v53, 3, v52\n\t" \
"v_lshlrev_b32 v53, 3, v0\n\t" \
"v_and_b32 v53, 64, v53\n\t" \
"s_add_u32 m0, s61, 73728\n\t" \
"s_add_u32 s62, s64, 2048\n\t" \
"buffer_load_dwordx4 v8, s[36:39], s62 offen lds\n\t" \
"v_xor_b32 v3, v52, v53\n\t" \
"v_and_b32 v52, 15, v0\n\t" \
"v_lshrrev_b32 v53, 1, v52\n\t" \
"s_add_u32 m0, s61, 74752\n\t" \
"s_add_u32 s62, s64, 26624\n\t" \
"buffer_load_dwordx4 v8, s[36:39], s62 offen lds\n\t" \
"v_lshrrev_b32 v54, 4, v0\n\t" \
"v_xor_b32 v53, v53, v54\n\t" \
"v_lshlrev_b32 v53, 4, v53\n\t" \
"s_add_u32 s64, s76, 1\n\t" \
"s_and_b32 s64, s64, 7\n\t" \
"s_lshl_b32 s63, s64, 7\n\t" \
"s_add_u32 s63, s63, s59\n\t" \
"s_mul_i32 s64, s64, 196608\n\t" \
"s_add_u32 s64, s64, s60\n\t" \
"s_add_u32 m0, s61, 8192\n\t" \
"s_add_u32 s62, s63, 0\n\t" \
"buffer_load_dwordx4 v6, s[32:35], s62 offen lds\n\t" \
"v_lshl_add_u32 v53, v52, 7, v53\n\t" \
"v_add_u32 v9, s61, v53\n\t" \
"v_xor_b32 v10, 64, v9\n\t" \
"s_add_u32 m0, s61, 9216\n\t" \
"s_add_u32 s62, s63, 8192\n\t" \
"buffer_load_dwordx4 v7, s[32:35], s62 offen lds\n\t" \
"v_lshrrev_b32 v54, 5, v0\n\t" \
"v_bfe_u32 v53, v0, 2, 2\n\t" \
"v_lshl_add_u32 v53, v54, 3, v53\n\t" \
"s_add_u32 m0, s61, 81920\n\t" \
"s_add_u32 s62, s64, 1024\n\t" \
"buffer_load_dwordx4 v8, s[36:39], s62 offen lds\n\t" \
"s_lshl_b32 s57, s54, 6\n\t" \
"v_xor_b32 v55, s57, v3\n\t" \
"v_lshl_add_u32 v53, v53, 7, v55\n\t" \
"s_add_u32 m0, s61, 82944\n\t" \
"s_add_u32 s62, s64, 25600\n\t" \
"buffer_load_dwordx4 v8, s[36:39], s62 offen lds\n\t" \
"s_lshl_b32 s57, s55, 13\n\t" \
"s_add_u32 s57, s57, 65536\n\t" \
"v_add_u32 v11, s57, v53\n\t" \
"s_add_u32 m0, s61, 90112\n\t" \
"s_add_u32 s62, s64, 2048\n\t" \
"buffer_load_dwordx4 v8, s[36:39], s62 offen lds\n\t" \
"v_add_u32 v12, 0x10000, v11\n\t" \
"v_and_b32 v52, 2, v0\n\t" \
"v_cmp_eq_u32 vcc, 0, v52\n\t" \
"s_add_u32 m0, s61, 91136\n\t" \
"s_add_u32 s62, s64, 26624\n\t" \
"buffer_load_dwordx4 v8, s[36:39], s62 offen lds\n\t" \
"v_mov_b32 v52, 0x3f803f80\n\t" \
"v_and_b32 v142, 1, v0\n\t" \
"v_cndmask_b32 v20, 0, v52, vcc\n\t" \
"s_add_u32 s64, s76, 2\n\t" \
"s_and_b32 s64, s64, 7\n\t" \
"s_lshl_b32 s63, s64, 7\n\t" \
"s_add_u32 s63, s63, s59\n\t" \
"s_mul_i32 s64, s64, 196608\n\t" \
"s_add_u32 s64, s64, s60\n\t" \
"s_add_u32 m0, s61, 16384\n\t" \
"s_add_u32 s62, s63, 0\n\t" \
"buffer_load_dwordx4 v6, s[32:35], s62 offen lds\n\t" \
"v_cndmask_b32 v21, 0, v52, vcc\n\t" \
"v_xor_b32 v22, v52, v20\n\t" \
"v_xor_b32 v23, v52, v20\n\t" \
"s_add_u32 m0, s61, 17408\n\t" \
"s_add_u32 s62, s63, 8192\n\t" \
"buffer_load_dwordx4 v7, s[32:35], s62 offen lds\n\t" \
"v_mul_u32_u24 v142, 0xaaaa, v142\n\t" \
"v_add_u32 v142, 0x4444, v142\n\t" \
"v_mov_b32 v32, 0\n\t" \
"s_add_u32 m0, s61, 98304\n\t" \
"s_add_u32 s62, s64, 1024\n\t" \
"buffer_load_dwordx4 v8, s[36:39], s62 offen lds\n\t" \
"v_mov_b32 v33, 0\n\t" \
"v_mov_b32 v34, 0\n\t" \
"v_mov_b32 v35, 0\n\t" \
"s_add_u32 m0, s61, 99328\n\t" \
"s_add_u32 s62, s64, 25600\n\t" \
"buffer_load_dwordx4 v8, s[36:39], s62 offen lds\n\t" \
"v_mov_b32 v36, 0\n\t" \
"v_mov_b32 v37, 0\n\t" \
"v_mov_b32 v38, 0\n\t" \
"s_add_u32 m0, s61, 106496\n\t" \
"s_add_u32 s62, s64, 2048\n\t" \
"buffer_load_dwordx4 v8, s[36:39], s62 offen lds\n\t" \
"v_mov_b32 v39, 0\n\t" \
"s_mov_b32 s28, s24\n\t" \
"s_and_b32 s29, s25, 0xffff\n\t" \
"s_add_u32 m0, s61, 107520\n\t" \
"s_add_u32 s62, s64, 26624\n\t" \
"buffer_load_dwordx4 v8, s[36:39], s62 offen lds\n\t" \
"s_mov_b32 s30, 17563648\n\t" \
"s_mov_b32 s31, 0x20000\n\t" \
"buffer_load_dwordx2 v[140:141], off, s[28:31], 0 sc1\n\t" \
"s_cmp_eq_u32 s56, 0\n\t" \
"s_cselect_b64 s[90:91], s[14:15], s[18:19]\n\t" \
"s_cmp_ge_u32 s56, 2\n\t" \
"s_cselect_b64 s[90:91], s[12:13], s[90:91]\n\t" \
"s_cselect_b32 s57, 16, 56\n\t" \
"s_mul_i32 s58, s57, s49\n\t" \
"s_sub_u32 s57, s57, 1\n\t" \
"v_min_u32 v17, s57, v0\n\t" \
"v_add_u32 v17, s58, v17\n\t" \
"v_min_u32 v17, s57, v0\n\t" \
"v_lshl_add_u32 v17, v17, 8, s27\n\t" \
"v_lshlrev_b32 v17, 7, v17\n\t" \
"s_cmp_lg_u32 s56, 3\n\t" \
"s_cbranch_scc1 Lnfl_%=\n\t" \
"s_lshl_b32 s57, s48, 5\n\t" \
"s_add_u32 s57, s57, s49\n\t" \
"v_min_u32 v15, 29, v0\n\t" \
"s_lshl_b32 s58, s57, 11\n\t" \
"s_add_u32 s58, s58, 262144\n\t" \
"v_lshl_add_u32 v17, v15, 7, s58\n\t" \
"s_lshl_b32 s58, s57, 9\n\t" \
"s_add_u32 s58, s58, 129024\n\t" \
"v_cmp_le_u32 vcc, 16, v0\n\t" \
"v_lshl_add_u32 v16, v15, 7, s58\n\t" \
"s_nop 0\n\t" \
"v_cndmask_b32 v17, v17, v16, vcc\n\t" \
"s_lshl_b32 s58, s57, 10\n\t" \
"s_add_u32 s58, s58, 9434624\n\t" \
"v_cmp_le_u32 vcc, 20, v0\n\t" \
"v_lshl_add_u32 v16, v15, 7, s58\n\t" \
"s_nop 0\n\t" \
"v_cndmask_b32 v17, v17, v16, vcc\n\t" \
"s_lshl_b32 s58, s46, 5\n\t" \
"s_add_u32 s58, s58, s47\n\t" \
"s_lshl_b32 s58, s58, 7\n\t" \
"s_add_u32 s58, s58, 61952\n\t" \
"v_cmp_le_u32 vcc, 28, v0\n\t" \
"v_lshl_add_u32 v16, v15, 7, s58\n\t" \
"s_nop 0\n\t" \
"v_cndmask_b32 v17, v17, v16, vcc\n\t" \
"s_lshl_b32 s58, s47, 3\n\t" \
"s_add_u32 s58, s58, s46\n\t" \
"s_lshl_b32 s58, s58, 7\n\t" \
"s_add_u32 s58, s58, 94592\n\t" \
"v_cmp_le_u32 vcc, 29, v0\n\t" \
"v_lshl_add_u32 v16, v15, 7, s58\n\t" \
"s_nop 0\n\t" \
"v_cndmask_b32 v17, v17, v16, vcc\n\t" \
"s_mov_b64 s[90:91], s[24:25]\n\t" \
"Lnfl_%=:\n\t" \
"s_waitcnt vmcnt(13) lgkmcnt(0)\n\t" \
"s_barrier\n\t" \
"s_add_u32 s64, s76, 3\n\t" \
"s_and_b32 s64, s64, 7\n\t" \
"s_lshl_b32 s63, s64, 7\n\t" \
"s_add_u32 s63, s63, s59\n\t" \
"s_mul_i32 s64, s64, 196608\n\t" \
"s_add_u32 s64, s64, s60\n\t" \
"s_add_u32 m0, s61, 24576\n\t" \
"s_add_u32 s62, s63, 0\n\t" \
"buffer_load_dwordx4 v6, s[32:35], s62 offen lds\n\t" \
"ds_read_b128 v[64:67], v9 offset:0\n\t" \
"s_add_u32 m0, s61, 25600\n\t" \
"s_add_u32 s62, s63, 8192\n\t" \
"buffer_load_dwordx4 v7, s[32:35], s62 offen lds\n\t" \
"ds_read_b128 v[68:71], v10 offset:0\n\t" \
"s_add_u32 m0, s61, 114688\n\t" \
"s_add_u32 s62, s64, 1024\n\t" \
"buffer_load_dwordx4 v8, s[36:39], s62 offen lds\n\t" \
"ds_read_b64_tr_b16 v[96:97], v11 offset:0\n\t" \
"ds_read_b64_tr_b16 v[98:99], v11 offset:512\n\t" \
"s_add_u32 m0, s61, 115712\n\t" \
"s_add_u32 s62, s64, 25600\n\t" \
"buffer_load_dwordx4 v8, s[36:39], s62 offen lds\n\t" \
"ds_read_b64_tr_b16 v[100:101], v11 offset:2048\n\t" \
"s_add_u32 m0, s61, 122880\n\t" \
"s_add_u32 s62, s64, 2048\n\t" \
"buffer_load_dwordx4 v8, s[36:39], s62 offen lds\n\t" \
"ds_read_b64_tr_b16 v[102:103], v11 offset:2560\n\t" \
"s_add_u32 m0, s61, 123904\n\t" \
"s_add_u32 s62, s64, 26624\n\t" \
"buffer_load_dwordx4 v8, s[36:39], s62 offen lds\n\t" \
"ds_read_b64_tr_b16 v[104:105], v11 offset:4096\n\t" \
"ds_read_b64_tr_b16 v[106:107], v11 offset:4608\n\t" \
"ds_read_b64_tr_b16 v[108:109], v11 offset:6144\n\t" \
"ds_read_b64_tr_b16 v[110:111], v11 offset:6656\n\t" \
"s_waitcnt vmcnt(13) lgkmcnt(0)\n\t" \
"s_barrier\n\t" \
"s_add_u32 s64, s76, 4\n\t" \
"s_and_b32 s64, s64, 7\n\t" \
"s_lshl_b32 s63, s64, 7\n\t" \
"s_add_u32 s63, s63, s59\n\t" \
"s_mul_i32 s64, s64, 196608\n\t" \
"s_add_u32 s64, s64, s60\n\t" \
"s_add_u32 m0, s61, 32768\n\t" \
"s_add_u32 s62, s63, 0\n\t" \
"buffer_load_dwordx4 v6, s[32:35], s62 offen lds\n\t" \
"s_waitcnt lgkmcnt(0)\n\t" \
"v_mfma_f32_16x16x32_bf16 v[28:31], v[64:67], v[64:67], 0\n\t" \
"s_add_u32 m0, s61, 33792\n\t" \
"s_add_u32 s62, s63, 8192\n\t" \
"buffer_load_dwordx4 v7, s[32:35], s62 offen lds\n\t" \
"ds_read_b128 v[72:75], v9 offset:8192\n\t" \
"ds_read_b128 v[76:79], v10 offset:8192\n\t" \
"s_add_u32 m0, s61, 131072\n\t" \
"s_add_u32 s62, s64, 1024\n\t" \
"buffer_load_dwordx4 v8, s[36:39], s62 offen lds\n\t" \
"ds_read_b64_tr_b16 v[112:113], v11 offset:16384\n\t" \
"v_smfmac_f32_16x16x64_bf16 v[36:39], v[20:23], v[96:103], v142\n\t" \
"ds_read_b64_tr_b16 v[114:115], v11 offset:16896\n\t" \
"s_add_u32 m0, s61, 132096\n\t" \
"s_add_u32 s62, s64, 25600\n\t" \
"buffer_load_dwordx4 v8, s[36:39], s62 offen lds\n\t" \
"ds_read_b64_tr_b16 v[116:117], v11 offset:18432\n\t" \
"v_mfma_f32_16x16x32_bf16 v[28:31], v[68:71], v[68:71], v[28:31]\n\t" \
"s_add_u32 m0, s61, 139264\n\t" \
"s_add_u32 s62, s64, 2048\n\t" \
"buffer_load_dwordx4 v8, s[36:39], s62 offen lds\n\t" \
"ds_read_b64_tr_b16 v[118:119], v11 offset:18944\n\t" \
"ds_read_b64_tr_b16 v[120:121], v11 offset:20480\n\t" \
"s_add_u32 m0, s61, 140288\n\t" \
"s_add_u32 s62, s64, 26624\n\t" \
"buffer_load_dwordx4 v8, s[36:39], s62 offen lds\n\t" \
"ds_read_b64_tr_b16 v[122:123], v11 offset:20992\n\t" \
"v_smfmac_f32_16x16x64_bf16 v[36:39], v[20:23], v[104:111], v142\n\t" \
"ds_read_b64_tr_b16 v[124:125], v11 offset:22528\n\t" \
"ds_read_b64_tr_b16 v[126:127], v11 offset:23040\n\t" \
"v_smfmac_f32_16x16x64_bf16 v[32:35], v[20:23], v[64:71], v142\n\t" \
"s_waitcnt vmcnt(13) lgkmcnt(0)\n\t" \
"s_barrier\n\t" \
"s_add_u32 s64, s76, 5\n\t" \
"s_and_b32 s64, s64, 7\n\t" \
"s_lshl_b32 s63, s64, 7\n\t" \
"s_add_u32 s63, s63, s59\n\t" \
"s_mul_i32 s64, s64, 196608\n\t" \
"s_add_u32 s64, s64, s60\n\t" \
"s_add_u32 m0, s61, 40960\n\t" \
"s_add_u32 s62, s63, 0\n\t" \
"buffer_load_dwordx4 v6, s[32:35], s62 offen lds\n\t" \
"s_waitcnt lgkmcnt(0)\n\t" \
"v_mfma_f32_16x16x32_bf16 v[28:31], v[72:75], v[72:75], v[28:31]\n\t" \
"s_add_u32 m0, s61, 41984\n\t" \
"s_add_u32 s62, s63, 8192\n\t" \
"buffer_load_dwordx4 v7, s[32:35], s62 offen lds\n\t" \
"ds_read_b128 v[64:67], v9 offset:16384\n\t" \
"ds_read_b128 v[68:71], v10 offset:16384\n\t" \
"s_add_u32 m0, s61, 65536\n\t" \
"s_add_u32 s62, s64, 1024\n\t" \
"buffer_load_dwordx4 v8, s[36:39], s62 offen lds\n\t" \
"ds_read_b64_tr_b16 v[96:97], v11 offset:32768\n\t" \
"v_smfmac_f32_16x16x64_bf16 v[36:39], v[20:23], v[112:119], v142\n\t" \
"ds_read_b64_tr_b16 v[98:99], v11 offset:33280\n\t" \
"s_add_u32 m0, s61, 66560\n\t" \
"s_add_u32 s62, s64, 25600\n\t" \
"buffer_load_dwordx4 v8, s[36:39], s62 offen lds\n\t" \
"ds_read_b64_tr_b16 v[100:101], v11 offset:34816\n\t" \
"v_mfma_f32_16x16x32_bf16 v[28:31], v[76:79], v[76:79], v[28:31]\n\t" \
"s_add_u32 m0, s61, 73728\n\t" \
"s_add_u32 s62, s64, 2048\n\t" \
"buffer_load_dwordx4 v8, s[36:39], s62 offen lds\n\t" \
"ds_read_b64_tr_b16 v[102:103], v11 offset:35328\n\t" \
"ds_read_b64_tr_b16 v[104:105], v11 offset:36864\n\t" \
"s_add_u32 m0, s61, 74752\n\t" \
"s_add_u32 s62, s64, 26624\n\t" \
"buffer_load_dwordx4 v8, s[36:39], s62 offen lds\n\t" \
"ds_read_b64_tr_b16 v[106:107], v11 offset:37376\n\t" \
"v_smfmac_f32_16x16x64_bf16 v[36:39], v[20:23], v[120:127], v142\n\t" \
"ds_read_b64_tr_b16 v[108:109], v11 offset:38912\n\t" \
"ds_read_b64_tr_b16 v[110:111], v11 offset:39424\n\t" \
"v_smfmac_f32_16x16x64_bf16 v[32:35], v[20:23], v[72:79], v142\n\t" \
"s_waitcnt vmcnt(12) lgkmcnt(0)\n\t" \
"s_barrier\n\t" \
"s_add_u32 s64, s76, 6\n\t" \
"s_and_b32 s64, s64, 7\n\t" \
"s_lshl_b32 s63, s64, 7\n\t" \
"s_add_u32 s63, s63, s59\n\t" \
"s_mul_i32 s64, s64, 196608\n\t" \
"s_add_u32 s64, s64, s60\n\t" \
"s_add_u32 m0, s61, 49152\n\t" \
"s_add_u32 s62, s63, 0\n\t" \
"buffer_load_dwordx4 v6, s[32:35], s62 offen lds\n\t" \
"s_waitcnt lgkmcnt(0)\n\t" \
"v_mfma_f32_16x16x32_bf16 v[28:31], v[64:67], v[64:67], v[28:31]\n\t" \
"s_add_u32 m0, s61, 50176\n\t" \
"s_add_u32 s62, s63, 8192\n\t" \
"buffer_load_dwordx4 v7, s[32:35], s62 offen lds\n\t" \
"ds_read_b128 v[72:75], v9 offset:24576\n\t" \
"ds_read_b128 v[76:79], v10 offset:24576\n\t" \
"s_add_u32 m0, s61, 81920\n\t" \
"s_add_u32 s62, s64, 1024\n\t" \
"buffer_load_dwordx4 v8, s[36:39], s62 offen lds\n\t" \
"ds_read_b64_tr_b16 v[112:113], v11 offset:49152\n\t" \
"v_smfmac_f32_16x16x64_bf16 v[36:39], v[20:23], v[96:103], v142\n\t" \
"ds_read_b64_tr_b16 v[114:115], v11 offset:49664\n\t" \
"s_add_u32 m0, s61, 82944\n\t" \
"s_add_u32 s62, s64, 25600\n\t" \
"buffer_load_dwordx4 v8, s[36:39], s62 offen lds\n\t" \
"ds_read_b64_tr_b16 v[116:117], v11 offset:51200\n\t" \
"v_mfma_f32_16x16x32_bf16 v[28:31], v[68:71], v[68:71], v[28:31]\n\t" \
"s_add_u32 m0, s61, 90112\n\t" \
"s_add_u32 s62, s64, 2048\n\t" \
"buffer_load_dwordx4 v8, s[36:39], s62 offen lds\n\t" \
"ds_read_b64_tr_b16 v[118:119], v11 offset:51712\n\t" \
"ds_read_b64_tr_b16 v[120:121], v11 offset:53248\n\t" \
"s_add_u32 m0, s61, 91136\n\t" \
"s_add_u32 s62, s64, 26624\n\t" \
"buffer_load_dwordx4 v8, s[36:39], s62 offen lds\n\t" \
"ds_read_b64_tr_b16 v[122:123], v11 offset:53760\n\t" \
"v_smfmac_f32_16x16x64_bf16 v[36:39], v[20:23], v[104:111], v142\n\t" \
"ds_read_b64_tr_b16 v[124:125], v11 offset:55296\n\t" \
"ds_read_b64_tr_b16 v[126:127], v11 offset:55808\n\t" \
"v_smfmac_f32_16x16x64_bf16 v[32:35], v[20:23], v[64:71], v142\n\t" \
"s_waitcnt vmcnt(12) lgkmcnt(0)\n\t" \
"s_barrier\n\t" \
"s_add_u32 s64, s76, 7\n\t" \
"s_and_b32 s64, s64, 7\n\t" \
"s_lshl_b32 s63, s64, 7\n\t" \
"s_add_u32 s63, s63, s59\n\t" \
"s_mul_i32 s64, s64, 196608\n\t" \
"s_add_u32 s64, s64, s60\n\t" \
"s_add_u32 m0, s61, 57344\n\t" \
"s_add_u32 s62, s63, 0\n\t" \
"buffer_load_dwordx4 v6, s[32:35], s62 offen lds\n\t" \
"s_waitcnt lgkmcnt(0)\n\t" \
"v_mfma_f32_16x16x32_bf16 v[28:31], v[72:75], v[72:75], v[28:31]\n\t" \
"s_add_u32 m0, s61, 58368\n\t" \
"s_add_u32 s62, s63, 8192\n\t" \
"buffer_load_dwordx4 v7, s[32:35], s62 offen lds\n\t" \
"ds_read_b128 v[64:67], v9 offset:32768\n\t" \
"ds_read_b128 v[68:71], v10 offset:32768\n\t" \
"s_add_u32 m0, s61, 98304\n\t" \
"s_add_u32 s62, s64, 1024\n\t" \
"buffer_load_dwordx4 v8, s[36:39], s62 offen lds\n\t" \
"ds_read_b64_tr_b16 v[96:97], v12 offset:0\n\t" \
"v_smfmac_f32_16x16x64_bf16 v[36:39], v[20:23], v[112:119], v142\n\t" \
"ds_read_b64_tr_b16 v[98:99], v12 offset:512\n\t" \
"s_add_u32 m0, s61, 99328\n\t" \
"s_add_u32 s62, s64, 25600\n\t" \
"buffer_load_dwordx4 v8, s[36:39], s62 offen lds\n\t" \
"ds_read_b64_tr_b16 v[100:101], v12 offset:2048\n\t" \
"v_mfma_f32_16x16x32_bf16 v[28:31], v[76:79], v[76:79], v[28:31]\n\t" \
"s_add_u32 m0, s61, 106496\n\t" \
"s_add_u32 s62, s64, 2048\n\t" \
"buffer_load_dwordx4 v8, s[36:39], s62 offen lds\n\t" \
"ds_read_b64_tr_b16 v[102:103], v12 offset:2560\n\t" \
"ds_read_b64_tr_b16 v[104:105], v12 offset:4096\n\t" \
"s_add_u32 m0, s61, 107520\n\t" \
"s_add_u32 s62, s64, 26624\n\t" \
"buffer_load_dwordx4 v8, s[36:39], s62 offen lds\n\t" \
"ds_read_b64_tr_b16 v[106:107], v12 offset:4608\n\t" \
"v_smfmac_f32_16x16x64_bf16 v[36:39], v[20:23], v[120:127], v142\n\t" \
"ds_read_b64_tr_b16 v[108:109], v12 offset:6144\n\t" \
"ds_read_b64_tr_b16 v[110:111], v12 offset:6656\n\t" \
"v_smfmac_f32_16x16x64_bf16 v[32:35], v[20:23], v[72:79], v142\n\t" \
"s_waitcnt vmcnt(12) lgkmcnt(0)\n\t" \
"s_barrier\n\t" \
"s_add_u32 s64, s76, 0\n\t" \
"s_and_b32 s64, s64, 7\n\t" \
"s_mul_i32 s64, s64, 196608\n\t" \
"s_add_u32 s64, s64, s60\n\t" \
"s_add_u32 m0, s61, 114688\n\t" \
"s_add_u32 s62, s64, 0\n\t" \
"buffer_load_dwordx4 v8, s[36:39], s62 offen lds\n\t" \
"s_waitcnt lgkmcnt(0)\n\t" \
"v_mfma_f32_16x16x32_bf16 v[28:31], v[64:67], v[64:67], v[28:31]\n\t" \
"ds_read_b128 v[72:75], v9 offset:40960\n\t" \
"s_add_u32 m0, s61, 115712\n\t" \
"s_add_u32 s62, s64, 24576\n\t" \
"buffer_load_dwordx4 v8, s[36:39], s62 offen lds\n\t" \
"ds_read_b128 v[76:79], v10 offset:40960\n\t" \
"ds_read_b64_tr_b16 v[112:113], v11 offset:0\n\t" \
"v_smfmac_f32_16x16x64_bf16 v[36:39], v[20:23], v[96:103], v142\n\t" \
"ds_read_b64_tr_b16 v[114:115], v11 offset:512\n\t" \
"s_add_u32 s64, s76, 1\n\t" \
"s_and_b32 s64, s64, 7\n\t" \
"s_mul_i32 s64, s64, 196608\n\t" \
"s_add_u32 s64, s64, s60\n\t" \
"s_add_u32 m0, s61, 122880\n\t" \
"s_add_u32 s62, s64, 0\n\t" \
"buffer_load_dwordx4 v8, s[36:39], s62 offen lds\n\t" \
"ds_read_b64_tr_b16 v[116:117], v11 offset:2048\n\t" \
"v_mfma_f32_16x16x32_bf16 v[28:31], v[68:71], v[68:71], v[28:31]\n\t" \
"ds_read_b64_tr_b16 v[118:119], v11 offset:2560\n\t" \
"s_add_u32 m0, s61, 123904\n\t" \
"s_add_u32 s62, s64, 24576\n\t" \
"buffer_load_dwordx4 v8, s[36:39], s62 offen lds\n\t" \
"ds_read_b64_tr_b16 v[120:121], v11 offset:4096\n\t" \
"ds_read_b64_tr_b16 v[122:123], v11 offset:4608\n\t" \
"v_smfmac_f32_16x16x64_bf16 v[36:39], v[20:23], v[104:111], v142\n\t" \
"ds_read_b64_tr_b16 v[124:125], v11 offset:6144\n\t" \
"ds_read_b64_tr_b16 v[126:127], v11 offset:6656\n\t" \
"v_smfmac_f32_16x16x64_bf16 v[32:35], v[20:23], v[64:71], v142\n\t" \
"s_waitcnt vmcnt(10) lgkmcnt(0)\n\t" \
"s_barrier\n\t" \
"s_add_u32 s64, s76, 2\n\t" \
"s_and_b32 s64, s64, 7\n\t" \
"s_mul_i32 s64, s64, 196608\n\t" \
"s_add_u32 s64, s64, s60\n\t" \
"s_add_u32 m0, s61, 131072\n\t" \
"s_add_u32 s62, s64, 0\n\t" \
"buffer_load_dwordx4 v8, s[36:39], s62 offen lds\n\t" \
"s_waitcnt lgkmcnt(0)\n\t" \
"v_mfma_f32_16x16x32_bf16 v[28:31], v[72:75], v[72:75], v[28:31]\n\t" \
"ds_read_b128 v[64:67], v9 offset:49152\n\t" \
"s_add_u32 m0, s61, 132096\n\t" \
"s_add_u32 s62, s64, 24576\n\t" \
"buffer_load_dwordx4 v8, s[36:39], s62 offen lds\n\t" \
"ds_read_b128 v[68:71], v10 offset:49152\n\t" \
"ds_read_b64_tr_b16 v[96:97], v11 offset:16384\n\t" \
"v_smfmac_f32_16x16x64_bf16 v[36:39], v[20:23], v[112:119], v142\n\t" \
"ds_read_b64_tr_b16 v[98:99], v11 offset:16896\n\t" \
"s_add_u32 s64, s76, 3\n\t" \
"s_and_b32 s64, s64, 7\n\t" \
"s_mul_i32 s64, s64, 196608\n\t" \
"s_add_u32 s64, s64, s60\n\t" \
"s_add_u32 m0, s61, 139264\n\t" \
"s_add_u32 s62, s64, 0\n\t" \
"buffer_load_dwordx4 v8, s[36:39], s62 offen lds\n\t" \
"ds_read_b64_tr_b16 v[100:101], v11 offset:18432\n\t" \
"v_mfma_f32_16x16x32_bf16 v[28:31], v[76:79], v[76:79], v[28:31]\n\t" \
"ds_read_b64_tr_b16 v[102:103], v11 offset:18944\n\t" \
"s_add_u32 m0, s61, 140288\n\t" \
"s_add_u32 s62, s64, 24576\n\t" \
"buffer_load_dwordx4 v8, s[36:39], s62 offen lds\n\t" \
"ds_read_b64_tr_b16 v[104:105], v11 offset:20480\n\t" \
"ds_read_b64_tr_b16 v[106:107], v11 offset:20992\n\t" \
"v_smfmac_f32_16x16x64_bf16 v[36:39], v[20:23], v[120:127], v142\n\t" \
"ds_read_b64_tr_b16 v[108:109], v11 offset:22528\n\t" \
"ds_read_b64_tr_b16 v[110:111], v11 offset:23040\n\t" \
"v_smfmac_f32_16x16x64_bf16 v[32:35], v[20:23], v[72:79], v142\n\t" \
"s_waitcnt vmcnt(8) lgkmcnt(0)\n\t" \
"s_barrier\n\t" \
"s_add_u32 s64, s76, 4\n\t" \
"s_and_b32 s64, s64, 7\n\t" \
"s_mul_i32 s64, s64, 196608\n\t" \
"s_add_u32 s64, s64, s60\n\t" \
"s_add_u32 m0, s61, 65536\n\t" \
"s_add_u32 s62, s64, 0\n\t" \
"buffer_load_dwordx4 v8, s[36:39], s62 offen lds\n\t" \
"s_waitcnt lgkmcnt(0)\n\t" \
"s_add_u32 m0, s61, 66560\n\t" \
"s_add_u32 s62, s64, 24576\n\t" \
"buffer_load_dwordx4 v8, s[36:39], s62 offen lds\n\t" \
"v_mfma_f32_16x16x32_bf16 v[28:31], v[64:67], v[64:67], v[28:31]\n\t" \
"ds_read_b128 v[72:75], v9 offset:57344\n\t" \
"s_add_u32 s64, s76, 5\n\t" \
"s_and_b32 s64, s64, 7\n\t" \
"s_mul_i32 s64, s64, 196608\n\t" \
"s_add_u32 s64, s64, s60\n\t" \
"s_add_u32 m0, s61, 73728\n\t" \
"s_add_u32 s62, s64, 0\n\t" \
"buffer_load_dwordx4 v8, s[36:39], s62 offen lds\n\t" \
"ds_read_b128 v[76:79], v10 offset:57344\n\t" \
"ds_read_b64_tr_b16 v[112:113], v11 offset:32768\n\t" \
"s_add_u32 m0, s61, 74752\n\t" \
"s_add_u32 s62, s64, 24576\n\t" \
"buffer_load_dwordx4 v8, s[36:39], s62 offen lds\n\t" \
"v_smfmac_f32_16x16x64_bf16 v[36:39], v[20:23], v[96:103], v142\n\t" \
"ds_read_b64_tr_b16 v[114:115], v11 offset:33280\n\t" \
"s_add_u32 s64, s76, 6\n\t" \
"s_and_b32 s64, s64, 7\n\t" \
"s_mul_i32 s64, s64, 196608\n\t" \
"s_add_u32 s64, s64, s60\n\t" \
"s_add_u32 m0, s61, 81920\n\t" \
"s_add_u32 s62, s64, 0\n\t" \
"buffer_load_dwordx4 v8, s[36:39], s62 offen lds\n\t" \
"ds_read_b64_tr_b16 v[116:117], v11 offset:34816\n\t" \
"s_add_u32 m0, s61, 82944\n\t" \
"s_add_u32 s62, s64, 24576\n\t" \
"buffer_load_dwordx4 v8, s[36:39], s62 offen lds\n\t" \
"v_mfma_f32_16x16x32_bf16 v[28:31], v[68:71], v[68:71], v[28:31]\n\t" \
"ds_read_b64_tr_b16 v[118:119], v11 offset:35328\n\t" \
"s_add_u32 s64, s76, 7\n\t" \
"s_and_b32 s64, s64, 7\n\t" \
"s_mul_i32 s64, s64, 196608\n\t" \
"s_add_u32 s64, s64, s60\n\t" \
"s_add_u32 m0, s61, 90112\n\t" \
"s_add_u32 s62, s64, 0\n\t" \
"buffer_load_dwordx4 v8, s[36:39], s62 offen lds\n\t" \
"ds_read_b64_tr_b16 v[120:121], v11 offset:36864\n\t" \
"ds_read_b64_tr_b16 v[122:123], v11 offset:37376\n\t" \
"s_add_u32 m0, s61, 91136\n\t" \
"s_add_u32 s62, s64, 24576\n\t" \
"buffer_load_dwordx4 v8, s[36:39], s62 offen lds\n\t" \
"v_smfmac_f32_16x16x64_bf16 v[36:39], v[20:23], v[104:111], v142\n\t" \
"ds_read_b64_tr_b16 v[124:125], v11 offset:38912\n\t" \
"ds_read_b64_tr_b16 v[126:127], v11 offset:39424\n\t" \
"v_smfmac_f32_16x16x64_bf16 v[32:35], v[20:23], v[64:71], v142\n\t" \
"global_load_dword v17, v17, s[90:91]\n\t" \
"s_waitcnt lgkmcnt(0)\n\t" \
"v_mfma_f32_16x16x32_bf16 v[28:31], v[72:75], v[72:75], v[28:31]\n\t" \
"v_smfmac_f32_16x16x64_bf16 v[36:39], v[20:23], v[112:119], v142\n\t" \
"v_mfma_f32_16x16x32_bf16 v[28:31], v[76:79], v[76:79], v[28:31]\n\t" \
"v_smfmac_f32_16x16x64_bf16 v[36:39], v[20:23], v[120:127], v142\n\t" \
"v_smfmac_f32_16x16x64_bf16 v[32:35], v[20:23], v[72:79], v142\n\t" \
"s_mov_b32 s67, 0x3b000000\n\t" \
"s_lshl_b32 s57, s56, 7\n\t" \
"s_add_u32 s57, s57, 160256\n\t" \
"v_cmp_gt_u32 vcc, 16, v0\n\t" \
"v_lshl_add_u32 v56, v0, 2, s57\n\t" \
"v_mov_b32 v57, 161792\n\t" \
"v_cndmask_b32 v56, v57, v56, vcc\n\t" \
"s_lshl_b32 s57, s56, 6\n\t" \
"s_add_u32 s57, s57, 159744\n\t" \
"v_and_b32 v52, 15, v0\n\t" \
"v_lshrrev_b32 v53, 2, v52\n\t" \
"v_lshrrev_b32 v54, 4, v0\n\t" \
"v_cmp_eq_u32 vcc, v53, v54\n\t" \
"v_lshl_add_u32 v52, v52, 2, s57\n\t" \
"v_and_b32 v53, 1, v0\n\t" \
"v_cndmask_b32 v52, v57, v52, vcc\n\t" \
"v_cmp_eq_u32 vcc, 1, v53\n\t" \
"v_and_b32 v53, 2, v0\n\t" \
"v_cmp_ne_u32 s[70:71], 0, v53\n\t" \
"v_add_f32 v36, v36, v37\n\t" \
"v_add_f32 v37, v38, v39\n\t" \
"ds_write2_b32 v56, v36, v37 offset0:0 offset1:16\n\t" \
"v_cndmask_b32 v28, v28, v29, vcc\n\t" \
"v_cndmask_b32 v30, v30, v31, vcc\n\t" \
"v_cndmask_b32 v28, v28, v30, s[70:71]\n\t" \
"v_add_f32 v32, v32, v33\n\t" \
"v_add_f32 v33, v34, v35\n\t" \
"v_add_f32 v32, v32, v33\n\t" \
"v_mul_f32 v32, 0x3b000000, v32\n\t" \
"v_mul_f32 v54, v32, v32\n\t" \
"v_fma_f32 v54, v28, s67, -v54\n\t" \
"v_max_f32 v54, 0, v54\n\t" \
"v_add_f32 v54, 0x3727c5ac, v54\n\t" \
"v_rsq_f32 v54, v54\n\t" \
"s_nop 0\n\t" \
"v_mul_f32 v55, v32, v54\n\t" \
"v_sub_f32 v55, 0, v55\n\t" \
"ds_write2st64_b32 v52, v54, v55 offset0:0 offset1:1\n\t" \
"s_waitcnt lgkmcnt(0)\n\t" \
"s_barrier\n\t" \
"s_setprio 0\n\t" \
"v_and_b32 v52, 31, v0\n\t" \
"s_lshl_b32 s57, s54, 5\n\t" \
"v_add_u32 v52, s57, v52\n\t" \
"v_lshrrev_b32 v53, 1, v0\n\t" \
"v_and_b32 v53, 7, v53\n\t" \
"v_lshrrev_b32 v54, 5, v0\n\t" \
"v_xor_b32 v53, v53, v54\n\t" \
"v_lshlrev_b32 v53, 4, v53\n\t" \
"v_lshl_add_u32 v128, v52, 7, v53\n\t" \
"v_xor_b32 v129, 32, v128\n\t" \
"v_xor_b32 v130, 64, v128\n\t" \
"v_xor_b32 v131, 96, v128\n\t" \
"v_lshrrev_b32 v53, 2, v0\n\t" \
"v_and_b32 v53, 3, v53\n\t" \
"v_lshl_add_u32 v53, v54, 3, v53\n\t" \
"s_lshl_b32 s57, s55, 6\n\t" \
"v_xor_b32 v55, s57, v3\n\t" \
"v_lshl_add_u32 v53, v53, 7, v55\n\t" \
"v_add_u32 v132, 65536, v53\n\t" \
"v_add_u32 v133, 0x10000, v132\n\t" \
"s_lshl_b32 s57, s56, 7\n\t" \
"s_add_u32 s57, s57, 161280\n\t" \
"v_cmp_gt_u32 vcc, 16, v0\n\t" \
"v_lshl_add_u32 v220, v0, 2, s57\n\t" \
"v_mov_b32 v221, 161792\n\t" \
"v_cndmask_b32 v220, v221, v220, vcc\n\t" \
"v_lshrrev_b32 v221, 5, v0\n\t" \
"v_lshl_add_u32 v221, v221, 4, s57\n\t" \
"s_lshl_b32 s58, s54, 7\n\t" \
"s_add_u32 s58, s58, 159744\n\t" \
"v_and_b32 v222, 31, v0\n\t" \
"v_lshl_add_u32 v222, v222, 2, s58\n\t" \
"ds_read2st64_b32 v[224:225], v222 offset0:0 offset1:1\n\t" \
"s_lshl_b32 s57, s54, 12\n\t" \
"s_lshl_b32 s58, s55, 11\n\t" \
"s_add_u32 s57, s57, s58\n\t" \
"s_add_u32 s57, s57, 151552\n\t" \
"v_add_u32 v223, s57, v1\n\t" \
"v_mov_b32 v136, 0\n\t" \
"v_mov_b32 v137, 0\n\t" \
"v_mov_b32 v138, 0\n\t" \
"v_mov_b32 v139, 0\n\t" \
"s_mul_i32 s58, s49, 37\n\t" \
"s_lshr_b32 s58, s58, 8\n\t" \
"s_mul_i32 s57, s58, 7\n\t" \
"s_sub_u32 s57, s49, s57\n\t" \
"s_cmp_ge_u32 s57, 3\n\t" \
"s_addc_u32 s50, s57, 0\n\t" \
"s_cmp_ge_u32 s49, 28\n\t" \
"s_cselect_b32 s50, 3, s50\n\t" \
"s_cselect_b32 s58, s57, s58\n\t" \
"s_lshl_b32 s51, s48, 2\n\t" \
"s_add_u32 s51, s51, s58\n\t" \
"s_cmp_ge_u32 s49, 28\n\t" \
"s_cselect_b32 s52, 0, s49\n\t" \
"s_mov_b32 s36, s18\n\t" \
"s_and_b32 s37, s19, 0xffff\n\t" \
"s_mov_b32 s38, 1835008\n\t" \
"s_mov_b32 s39, 0x20000\n\t" \
"s_mov_b32 s40, s14\n\t" \
"s_and_b32 s41, s15, 0xffff\n\t" \
"s_mov_b32 s42, 1835008\n\t" \
"s_mov_b32 s43, 0x20000\n\t" \
"s_mov_b32 s84, s12\n\t" \
"s_and_b32 s85, s13, 0xffff\n\t" \
"s_mov_b32 s86, 524288\n\t" \
"s_mov_b32 s87, 0x20000\n\t" \
"s_cmp_ge_u32 s49, 28\n\t" \
"s_cselect_b32 s38, 0, s38\n\t" \
"s_cselect_b32 s42, 0, s42\n\t" \
"s_lshl_b32 s62, s56, 12\n\t" \
"s_lshl_b32 s61, s52, 7\n\t" \
"s_mul_i32 s57, s56, 114688\n\t" \
"s_add_u32 s61, s61, s57\n\t" \
"s_lshl_b32 s81, s50, 7\n\t" \
"s_lshl_b32 s57, s56, 17\n\t" \
"s_add_u32 s60, s81, s57\n\t" \
"s_mul_i32 s57, s55, 1802240\n\t" \
"s_sub_u32 s66, s60, s57\n\t" \
"s_lshl_b32 s57, s56, 15\n\t" \
"s_add_u32 s81, s81, s57\n\t" \
"s_lshr_b32 s57, s58, 1\n\t" \
"s_lshl_b32 s57, s57, 16\n\t" \
"s_sub_u32 s53, s60, s57\n\t" \
"s_sub_u32 s83, s66, s57\n\t" \
"s_add_u32 s60, s60, s57\n\t" \
"s_add_u32 s66, s66, s57\n\t" \
"s_add_u32 s82, s60, 819200\n\t" \
"s_add_u32 s57, s53, 65536\n\t" \
"s_bitcmp1_b32 s58, 0\n\t" \
"s_cselect_b32 s82, s57, s82\n\t" \
"s_add_u32 s79, s82, 524288\n\t" \
"v_lshrrev_b32 v68, 5, v0\n\t" \
"v_and_b32 v69, 15, v0\n\t" \
"v_lshlrev_b32 v68, 8, v68\n\t" \
"v_lshl_add_u32 v6, v69, 4, v68\n\t" \
"s_mov_b32 s94, 0xffff\n\t" \
"s_mov_b32 s95, 0xffff\n\t" \
"v_and_b32 v68, 27, v0\n\t" \
"v_cmp_eq_u32 vcc, 16, v68\n\t" \
"v_mov_b32 v68, 0x3f803f80\n\t" \
"s_nop 0\n\t" \
"v_cndmask_b32 v144, 0, v68, vcc\n\t" \
"v_mov_b32 v145, v144\n\t" \
"v_mov_b32 v146, v144\n\t" \
"v_mov_b32 v147, v144\n\t" \
"v_mov_b32 v148, v144\n\t" \
"v_mov_b32 v149, v144\n\t" \
"v_mov_b32 v150, v144\n\t" \
"v_mov_b32 v151, v144\n\t" \
"v_mov_b32 v152, v144\n\t" \
"v_mov_b32 v153, v144\n\t" \
"v_mov_b32 v154, v144\n\t" \
"v_mov_b32 v155, v144\n\t" \
"v_mov_b32 v156, v144\n\t" \
"v_mov_b32 v157, v144\n\t" \
"v_mov_b32 v158, v144\n\t" \
"v_mov_b32 v159, v144\n\t" \
"v_mov_b32 v160, v144\n\t" \
"v_mov_b32 v161, v144\n\t" \
"v_mov_b32 v162, v144\n\t" \
"v_mov_b32 v163, v144\n\t" \
"v_mov_b32 v164, v144\n\t" \
"v_mov_b32 v165, v144\n\t" \
"v_mov_b32 v166, v144\n\t" \
"v_mov_b32 v167, v144\n\t" \
"v_mov_b32 v168, v144\n\t" \
"v_mov_b32 v169, v144\n\t" \
"v_mov_b32 v170, v144\n\t" \
"v_mov_b32 v171, v144\n\t" \
"v_mov_b32 v172, v144\n\t" \
"v_mov_b32 v173, v144\n\t" \
"v_mov_b32 v174, v144\n\t" \
"v_mov_b32 v175, v144\n\t" \
"v_mov_b32 v7, 0xdddd\n\t" \
"s_lshl_b32 s57, s49, 2\n\t" \
"s_add_u32 s57, s57, s56\n\t" \
"s_lshl_b32 s57, s57, 9\n\t" \
"v_and_b32 v18, 7, v0\n\t" \
"v_lshl_add_u32 v18, v18, 6, s57\n\t" \
"v_min_u32 v18, 47008, v18\n\t" \
"v_lshrrev_b32 v68, 3, v0\n\t" \
"v_lshl_add_u32 v11, v68, 10, v2\n\t" \
"v_mul_u32_u24 v13, 0xe00, v68\n\t" \
"v_add_u32 v13, v13, v2\n\t" \
"v_mov_b32 v32, 0\n\t" \
"v_mov_b32 v33, 0\n\t" \
"v_mov_b32 v34, 0\n\t" \
"v_mov_b32 v35, 0\n\t" \
"v_mov_b32 v36, 0\n\t" \
"v_mov_b32 v37, 0\n\t" \
"v_mov_b32 v38, 0\n\t" \
"v_mov_b32 v39, 0\n\t" \
"v_mov_b32 v40, 0\n\t" \
"v_mov_b32 v41, 0\n\t" \
"v_mov_b32 v42, 0\n\t" \
"v_mov_b32 v43, 0\n\t" \
"v_mov_b32 v44, 0\n\t" \
"v_mov_b32 v45, 0\n\t" \
"v_mov_b32 v46, 0\n\t" \
"v_mov_b32 v47, 0\n\t" \
"v_mov_b32 v48, 0\n\t" \
"v_mov_b32 v49, 0\n\t" \
"v_mov_b32 v50, 0\n\t" \
"v_mov_b32 v51, 0\n\t" \
"v_mov_b32 v52, 0\n\t" \
"v_mov_b32 v53, 0\n\t" \
"v_mov_b32 v54, 0\n\t" \
"v_mov_b32 v55, 0\n\t" \
"v_mov_b32 v56, 0\n\t" \
"v_mov_b32 v57, 0\n\t" \
"v_mov_b32 v58, 0\n\t" \
"v_mov_b32 v59, 0\n\t" \
"v_mov_b32 v60, 0\n\t" \
"v_mov_b32 v61, 0\n\t" \
"v_mov_b32 v62, 0\n\t" \
"v_mov_b32 v63, 0\n\t" \
"s_lshl_b32 s57, s46, 2\n\t" \
"s_add_u32 s57, s57, s56\n\t" \
"s_mul_i32 s64, s57, 131072\n\t" \
"s_add_u32 s64, s64, 1048576\n\t" \
"s_cmp_lt_u32 s26, 4\n\t" \
"s_cselect_b32 s57, 0, 34816\n\t" \
"s_cselect_b32 s77, 49, 15\n\t" \
"s_add_u32 s78, s64, s57\n\t" \
"s_bitcmp1_b32 s47, 4\n\t" \
"s_cselect_b32 s57, 65536, 0\n\t" \
"s_add_u32 s78, s78, s57\n\t" \
"s_sub_u32 s76, s64, s57\n\t" \
"s_add_u32 s76, s76, 65536\n\t" \
"s_lshl_b32 s88, s46, 5\n\t" \
"s_add_u32 s88, s88, s47\n\t" \
"s_and_b32 s88, s88, 65520\n\t" \
"s_xor_b32 s88, s88, 16\n\t" \
"s_lshl_b32 s88, s88, 7\n\t" \
"s_add_u32 s88, s88, 65536\n\t" \
"v_and_b32 v19, 15, v0\n\t" \
"v_lshlrev_b32 v19, 7, v19\n\t" \
"s_xor_b32 s58, s26, 4\n\t" \
"s_lshl_b32 s57, s26, 10\n\t" \
"s_add_u32 s57, s57, 151552\n\t" \
"s_lshl_b32 s58, s58, 10\n\t" \
"s_add_u32 s58, s58, 151552\n\t" \
"v_add_u32 v10, s57, v1\n\t" \
"v_add_u32 v12, s58, v1\n\t" \
"s_waitcnt vmcnt(1) lgkmcnt(0)\n\t" \
"s_barrier\n\t" \
"s_setprio 2\n\t" \
"ds_read_b128 v[64:67], v128 offset:0\n\t" \
"ds_read_b64_tr_b16 v[96:97], v132 offset:49152\n\t" \
"ds_read_b64_tr_b16 v[98:99], v132 offset:49664\n\t" \
"ds_read_b128 v[68:71], v129 offset:0\n\t" \
"ds_read_b64_tr_b16 v[100:101], v132 offset:51200\n\t" \
"ds_read_b64_tr_b16 v[102:103], v132 offset:51712\n\t" \
"ds_read_b128 v[72:75], v130 offset:0\n\t" \
"ds_read_b64_tr_b16 v[104:105], v132 offset:53248\n\t" \
"ds_read_b64_tr_b16 v[106:107], v132 offset:53760\n\t" \
"ds_read_b128 v[76:79], v131 offset:0\n\t" \
"ds_read_b64_tr_b16 v[108:109], v132 offset:55296\n\t" \
"ds_read_b64_tr_b16 v[110:111], v132 offset:55808\n\t" \
"s_waitcnt lgkmcnt(9)\n\t" \
"v_mfma_f32_32x32x16_bf16 v[232:247], v[96:99], v[64:67], 0\n\t" \
"ds_read_b128 v[80:83], v128 offset:8192\n\t" \
"ds_read_b64_tr_b16 v[112:113], v132 offset:57344\n\t" \
"ds_read_b64_tr_b16 v[114:115], v132 offset:57856\n\t" \
"v_mul_f32 v224, 0x3eb8aa3b, v224\n\t" \
"s_waitcnt lgkmcnt(9)\n\t" \
"v_mfma_f32_32x32x16_bf16 v[232:247], v[100:103], v[68:71], v[232:247]\n\t" \
"ds_read_b128 v[84:87], v129 offset:8192\n\t" \
"ds_read_b64_tr_b16 v[116:117], v132 offset:59392\n\t" \
"ds_read_b64_tr_b16 v[118:119], v132 offset:59904\n\t" \
"v_smfmac_f32_16x16x64_bf16 v[136:139], v[20:23], v[96:103], v142\n\t" \
"s_waitcnt lgkmcnt(9)\n\t" \
"v_mfma_f32_32x32x16_bf16 v[232:247], v[104:107], v[72:75], v[232:247]\n\t" \
"ds_read_b128 v[88:91], v130 offset:8192\n\t" \
"ds_read_b64_tr_b16 v[120:121], v132 offset:61440\n\t" \
"ds_read_b64_tr_b16 v[122:123], v132 offset:61952\n\t" \
"s_waitcnt lgkmcnt(9)\n\t" \
"v_mfma_f32_32x32x16_bf16 v[232:247], v[108:111], v[76:79], v[232:247]\n\t" \
"ds_read_b128 v[92:95], v131 offset:8192\n\t" \
"ds_read_b64_tr_b16 v[124:125], v132 offset:63488\n\t" \
"ds_read_b64_tr_b16 v[126:127], v132 offset:64000\n\t" \
"v_smfmac_f32_16x16x64_bf16 v[136:139], v[20:23], v[104:111], v142\n\t" \
"s_waitcnt lgkmcnt(9)\n\t" \
"v_mfma_f32_32x32x16_bf16 v[232:247], v[112:115], v[80:83], v[232:247]\n\t" \
"ds_read_b128 v[64:67], v128 offset:16384\n\t" \
"ds_read_b64_tr_b16 v[96:97], v133 offset:0\n\t" \
"ds_read_b64_tr_b16 v[98:99], v133 offset:512\n\t" \
"s_waitcnt lgkmcnt(9)\n\t" \
"v_mfma_f32_32x32x16_bf16 v[232:247], v[116:119], v[84:87], v[232:247]\n\t" \
"ds_read_b128 v[68:71], v129 offset:16384\n\t" \
"ds_read_b64_tr_b16 v[100:101], v133 offset:2048\n\t" \
"ds_read_b64_tr_b16 v[102:103], v133 offset:2560\n\t" \
"v_smfmac_f32_16x16x64_bf16 v[136:139], v[20:23], v[112:119], v142\n\t" \
"s_waitcnt lgkmcnt(9)\n\t" \
"v_mfma_f32_32x32x16_bf16 v[232:247], v[120:123], v[88:91], v[232:247]\n\t" \
"ds_read_b128 v[72:75], v130 offset:16384\n\t" \
"ds_read_b64_tr_b16 v[104:105], v133 offset:4096\n\t" \
"ds_read_b64_tr_b16 v[106:107], v133 offset:4608\n\t" \
"s_waitcnt lgkmcnt(9)\n\t" \
"v_mfma_f32_32x32x16_bf16 v[232:247], v[124:127], v[92:95], v[232:247]\n\t" \
"ds_read_b128 v[76:79], v131 offset:16384\n\t" \
"ds_read_b64_tr_b16 v[108:109], v133 offset:6144\n\t" \
"ds_read_b64_tr_b16 v[110:111], v133 offset:6656\n\t" \
"v_smfmac_f32_16x16x64_bf16 v[136:139], v[20:23], v[120:127], v142\n\t" \
"s_waitcnt lgkmcnt(9)\n\t" \
"v_mfma_f32_32x32x16_bf16 v[232:247], v[96:99], v[64:67], v[232:247]\n\t" \
"ds_read_b128 v[80:83], v128 offset:24576\n\t" \
"ds_read_b64_tr_b16 v[112:113], v133 offset:8192\n\t" \
"ds_read_b64_tr_b16 v[114:115], v133 offset:8704\n\t" \
"s_waitcnt lgkmcnt(9)\n\t" \
"v_mfma_f32_32x32x16_bf16 v[232:247], v[100:103], v[68:71], v[232:247]\n\t" \
"ds_read_b128 v[84:87], v129 offset:24576\n\t" \
"ds_read_b64_tr_b16 v[116:117], v133 offset:10240\n\t" \
"ds_read_b64_tr_b16 v[118:119], v133 offset:10752\n\t" \
"v_smfmac_f32_16x16x64_bf16 v[136:139], v[20:23], v[96:103], v142\n\t" \
"s_waitcnt lgkmcnt(9)\n\t" \
"v_mfma_f32_32x32x16_bf16 v[232:247], v[104:107], v[72:75], v[232:247]\n\t" \
"ds_read_b128 v[88:91], v130 offset:24576\n\t" \
"ds_read_b64_tr_b16 v[120:121], v133 offset:12288\n\t" \
"ds_read_b64_tr_b16 v[122:123], v133 offset:12800\n\t" \
"s_waitcnt lgkmcnt(9)\n\t" \
"v_mfma_f32_32x32x16_bf16 v[232:247], v[108:111], v[76:79], v[232:247]\n\t" \
"ds_read_b128 v[92:95], v131 offset:24576\n\t" \
"ds_read_b64_tr_b16 v[124:125], v133 offset:14336\n\t" \
"ds_read_b64_tr_b16 v[126:127], v133 offset:14848\n\t" \
"v_smfmac_f32_16x16x64_bf16 v[136:139], v[20:23], v[104:111], v142\n\t" \
"s_waitcnt lgkmcnt(9)\n\t" \
"v_mfma_f32_32x32x16_bf16 v[232:247], v[112:115], v[80:83], v[232:247]\n\t" \
"ds_read_b128 v[64:67], v128 offset:32768\n\t" \
"ds_read_b64_tr_b16 v[96:97], v132 offset:0\n\t" \
"ds_read_b64_tr_b16 v[98:99], v132 offset:512\n\t" \
"s_waitcnt lgkmcnt(9)\n\t" \
"v_mfma_f32_32x32x16_bf16 v[232:247], v[116:119], v[84:87], v[232:247]\n\t" \
"ds_read_b128 v[68:71], v129 offset:32768\n\t" \
"ds_read_b64_tr_b16 v[100:101], v132 offset:2048\n\t" \
"ds_read_b64_tr_b16 v[102:103], v132 offset:2560\n\t" \
"v_smfmac_f32_16x16x64_bf16 v[136:139], v[20:23], v[112:119], v142\n\t" \
"s_waitcnt lgkmcnt(9)\n\t" \
"v_mfma_f32_32x32x16_bf16 v[232:247], v[120:123], v[88:91], v[232:247]\n\t" \
"ds_read_b128 v[72:75], v130 offset:32768\n\t" \
"ds_read_b64_tr_b16 v[104:105], v132 offset:4096\n\t" \
"ds_read_b64_tr_b16 v[106:107], v132 offset:4608\n\t" \
"s_waitcnt lgkmcnt(9)\n\t" \
"v_mfma_f32_32x32x16_bf16 v[232:247], v[124:127], v[92:95], v[232:247]\n\t" \
"ds_read_b128 v[76:79], v131 offset:32768\n\t" \
"ds_read_b64_tr_b16 v[108:109], v132 offset:6144\n\t" \
"ds_read_b64_tr_b16 v[110:111], v132 offset:6656\n\t" \
"v_smfmac_f32_16x16x64_bf16 v[136:139], v[20:23], v[120:127], v142\n\t" \
"s_waitcnt lgkmcnt(9)\n\t" \
"v_mfma_f32_32x32x16_bf16 v[232:247], v[96:99], v[64:67], v[232:247]\n\t" \
"ds_read_b128 v[80:83], v128 offset:40960\n\t" \
"ds_read_b64_tr_b16 v[112:113], v132 offset:8192\n\t" \
"ds_read_b64_tr_b16 v[114:115], v132 offset:8704\n\t" \
"v_mul_f32 v225, 0x3eb8aa3b, v225\n\t" \
"s_waitcnt lgkmcnt(9)\n\t" \
"v_mfma_f32_32x32x16_bf16 v[232:247], v[100:103], v[68:71], v[232:247]\n\t" \
"ds_read_b128 v[84:87], v129 offset:40960\n\t" \
"ds_read_b64_tr_b16 v[116:117], v132 offset:10240\n\t" \
"ds_read_b64_tr_b16 v[118:119], v132 offset:10752\n\t" \
"v_smfmac_f32_16x16x64_bf16 v[136:139], v[20:23], v[96:103], v142\n\t" \
"s_waitcnt lgkmcnt(9)\n\t" \
"v_mfma_f32_32x32x16_bf16 v[232:247], v[104:107], v[72:75], v[232:247]\n\t" \
"ds_read_b128 v[88:91], v130 offset:40960\n\t" \
"ds_read_b64_tr_b16 v[120:121], v132 offset:12288\n\t" \
"ds_read_b64_tr_b16 v[122:123], v132 offset:12800\n\t" \
"s_waitcnt lgkmcnt(9)\n\t" \
"v_mfma_f32_32x32x16_bf16 v[232:247], v[108:111], v[76:79], v[232:247]\n\t" \
"ds_read_b128 v[92:95], v131 offset:40960\n\t" \
"ds_read_b64_tr_b16 v[124:125], v132 offset:14336\n\t" \
"ds_read_b64_tr_b16 v[126:127], v132 offset:14848\n\t" \
"v_smfmac_f32_16x16x64_bf16 v[136:139], v[20:23], v[104:111], v142\n\t" \
"s_waitcnt lgkmcnt(9)\n\t" \
"v_mfma_f32_32x32x16_bf16 v[232:247], v[112:115], v[80:83], v[232:247]\n\t" \
"ds_read_b128 v[64:67], v128 offset:49152\n\t" \
"ds_read_b64_tr_b16 v[96:97], v132 offset:16384\n\t" \
"ds_read_b64_tr_b16 v[98:99], v132 offset:16896\n\t" \
"s_waitcnt lgkmcnt(9)\n\t" \
"v_mfma_f32_32x32x16_bf16 v[232:247], v[116:119], v[84:87], v[232:247]\n\t" \
"ds_read_b128 v[68:71], v129 offset:49152\n\t" \
"ds_read_b64_tr_b16 v[100:101], v132 offset:18432\n\t" \
"ds_read_b64_tr_b16 v[102:103], v132 offset:18944\n\t" \
"v_smfmac_f32_16x16x64_bf16 v[136:139], v[20:23], v[112:119], v142\n\t" \
"s_waitcnt lgkmcnt(9)\n\t" \
"v_mfma_f32_32x32x16_bf16 v[232:247], v[120:123], v[88:91], v[232:247]\n\t" \
"ds_read_b128 v[72:75], v130 offset:49152\n\t" \
"ds_read_b64_tr_b16 v[104:105], v132 offset:20480\n\t" \
"ds_read_b64_tr_b16 v[106:107], v132 offset:20992\n\t" \
"s_waitcnt lgkmcnt(9)\n\t" \
"v_mfma_f32_32x32x16_bf16 v[232:247], v[124:127], v[92:95], v[232:247]\n\t" \
"ds_read_b128 v[76:79], v131 offset:49152\n\t" \
"ds_read_b64_tr_b16 v[108:109], v132 offset:22528\n\t" \
"ds_read_b64_tr_b16 v[110:111], v132 offset:23040\n\t" \
"v_smfmac_f32_16x16x64_bf16 v[136:139], v[20:23], v[120:127], v142\n\t" \
"s_waitcnt lgkmcnt(9)\n\t" \
"v_mfma_f32_32x32x16_bf16 v[232:247], v[96:99], v[64:67], v[232:247]\n\t" \
"ds_read_b128 v[80:83], v128 offset:57344\n\t" \
"ds_read_b64_tr_b16 v[112:113], v132 offset:24576\n\t" \
"ds_read_b64_tr_b16 v[114:115], v132 offset:25088\n\t" \
"s_waitcnt lgkmcnt(9)\n\t" \
"v_mfma_f32_32x32x16_bf16 v[232:247], v[100:103], v[68:71], v[232:247]\n\t" \
"ds_read_b128 v[84:87], v129 offset:57344\n\t" \
"ds_read_b64_tr_b16 v[116:117], v132 offset:26624\n\t" \
"ds_read_b64_tr_b16 v[118:119], v132 offset:27136\n\t" \
"v_smfmac_f32_16x16x64_bf16 v[136:139], v[20:23], v[96:103], v142\n\t" \
"s_waitcnt lgkmcnt(9)\n\t" \
"v_mfma_f32_32x32x16_bf16 v[232:247], v[104:107], v[72:75], v[232:247]\n\t" \
"ds_read_b128 v[88:91], v130 offset:57344\n\t" \
"ds_read_b64_tr_b16 v[120:121], v132 offset:28672\n\t" \
"ds_read_b64_tr_b16 v[122:123], v132 offset:29184\n\t" \
"s_waitcnt lgkmcnt(9)\n\t" \
"v_mfma_f32_32x32x16_bf16 v[232:247], v[108:111], v[76:79], v[232:247]\n\t" \
"ds_read_b128 v[92:95], v131 offset:57344\n\t" \
"ds_read_b64_tr_b16 v[124:125], v132 offset:30720\n\t" \
"ds_read_b64_tr_b16 v[126:127], v132 offset:31232\n\t" \
"v_smfmac_f32_16x16x64_bf16 v[136:139], v[20:23], v[104:111], v142\n\t" \
"s_waitcnt lgkmcnt(9)\n\t" \
"v_mfma_f32_32x32x16_bf16 v[232:247], v[112:115], v[80:83], v[232:247]\n\t" \
"s_waitcnt lgkmcnt(6)\n\t" \
"v_mfma_f32_32x32x16_bf16 v[232:247], v[116:119], v[84:87], v[232:247]\n\t" \
"v_smfmac_f32_16x16x64_bf16 v[136:139], v[20:23], v[112:119], v142\n\t" \
"s_waitcnt lgkmcnt(3)\n\t" \
"v_mfma_f32_32x32x16_bf16 v[232:247], v[120:123], v[88:91], v[232:247]\n\t" \
"s_waitcnt lgkmcnt(0)\n\t" \
"v_mfma_f32_32x32x16_bf16 v[232:247], v[124:127], v[92:95], v[232:247]\n\t" \
"v_smfmac_f32_16x16x64_bf16 v[136:139], v[20:23], v[120:127], v142\n\t" \
"v_readfirstlane_b32 s44, v140\n\t" \
"v_readfirstlane_b32 s45, v141\n\t" \
"s_nop 0\n\t" \
"s_add_u32 s44, s44, 1\n\t" \
"s_addc_u32 s45, s45, 0\n\t" \
"s_nop 2\n\t" \
"v_add_f32 v136, v136, v137\n\t" \
"v_add_f32 v137, v138, v139\n\t" \
"ds_write2_b32 v220, v136, v137 offset0:0 offset1:16\n\t" \
"ds_read_b128 v[192:195], v221 offset:0\n\t" \
"ds_read_b128 v[196:199], v221 offset:32\n\t" \
"ds_read_b128 v[200:203], v221 offset:64\n\t" \
"ds_read_b128 v[204:207], v221 offset:96\n\t" \
"s_waitcnt lgkmcnt(0)\n\t" \
"v_mul_f32 v192, v225, v192\n\t" \
"v_mul_f32 v193, v225, v193\n\t" \
"v_mul_f32 v194, v225, v194\n\t" \
"v_mul_f32 v195, v225, v195\n\t" \
"v_mul_f32 v196, v225, v196\n\t" \
"v_mul_f32 v197, v225, v197\n\t" \
"v_mul_f32 v198, v225, v198\n\t" \
"v_mul_f32 v199, v225, v199\n\t" \
"v_mul_f32 v200, v225, v200\n\t" \
"v_mul_f32 v201, v225, v201\n\t" \
"v_mul_f32 v202, v225, v202\n\t" \
"v_mul_f32 v203, v225, v203\n\t" \
"v_mul_f32 v204, v225, v204\n\t" \
"v_mul_f32 v205, v225, v205\n\t" \
"v_mul_f32 v206, v225, v206\n\t" \
"v_mul_f32 v207, v225, v207\n\t" \
"v_fmac_f32 v192, v224, v232\n\t" \
"v_fmac_f32 v193, v224, v233\n\t" \
"v_fmac_f32 v194, v224, v234\n\t" \
"v_fmac_f32 v195, v224, v235\n\t" \
"v_fmac_f32 v196, v224, v236\n\t" \
"v_fmac_f32 v197, v224, v237\n\t" \
"v_fmac_f32 v198, v224, v238\n\t" \
"v_fmac_f32 v199, v224, v239\n\t" \
"v_fmac_f32 v200, v224, v240\n\t" \
"v_fmac_f32 v201, v224, v241\n\t" \
"v_fmac_f32 v202, v224, v242\n\t" \
"v_fmac_f32 v203, v224, v243\n\t" \
"v_fmac_f32 v204, v224, v244\n\t" \
"v_fmac_f32 v205, v224, v245\n\t" \
"v_fmac_f32 v206, v224, v246\n\t" \
"v_fmac_f32 v207, v224, v247\n\t" \
"v_cvt_pk_bf16_f32 v208, v192, v193\n\t" \
"v_cvt_pk_bf16_f32 v209, v194, v195\n\t" \
"v_cvt_pk_bf16_f32 v210, v196, v197\n\t" \
"v_cvt_pk_bf16_f32 v211, v198, v199\n\t" \
"v_cvt_pk_bf16_f32 v212, v200, v201\n\t" \
"v_cvt_pk_bf16_f32 v213, v202, v203\n\t" \
"v_cvt_pk_bf16_f32 v214, v204, v205\n\t" \
"v_cvt_pk_bf16_f32 v215, v206, v207\n\t" \
"ds_write_b128 v223, v[208:211]\n\t" \
"ds_write_b128 v223, v[212:215] offset:1024\n\t" \
"s_branch Latt0_%=\n\t" \
"Lp1a_%=:\n\t" \
"s_cmp_lg_u32 s26, 1\n\t" \
"s_cbranch_scc1 Lnt1_%=\n\t" \
"s_lshl_b32 s57, s47, 16\n\t" \
"s_lshl_b32 s58, s46, 7\n\t" \
"s_add_u32 s57, s57, s58\n\t" \
"v_lshl_add_u32 v255, v0, 10, s57\n\t" \
"s_waitcnt lgkmcnt(0)\n\t" \
"global_load_dword v255, v255, s[8:9]\n\t" \
"Lnt1_%=:\n\t" \
"v_lshlrev_b32 v52, 1, v0\n\t" \
"v_and_b32 v52, 32, v52\n\t" \
"v_and_b32 v53, 3, v0\n\t" \
"v_lshl_add_u32 v52, v53, 3, v52\n\t" \
"v_lshlrev_b32 v53, 3, v0\n\t" \
"v_and_b32 v53, 64, v53\n\t" \
"v_xor_b32 v3, v52, v53\n\t" \
"v_and_b32 v52, 31, v0\n\t" \
"s_lshl_b32 s57, s54, 5\n\t" \
"v_add_u32 v52, s57, v52\n\t" \
"v_lshrrev_b32 v53, 1, v0\n\t" \
"v_and_b32 v53, 7, v53\n\t" \
"v_lshrrev_b32 v54, 5, v0\n\t" \
"v_xor_b32 v53, v53, v54\n\t" \
"v_lshlrev_b32 v53, 4, v53\n\t" \
"v_lshl_add_u32 v8, v52, 7, v53\n\t" \
"v_xor_b32 v9, 32, v8\n\t" \
"v_xor_b32 v10, 64, v8\n\t" \
"v_xor_b32 v11, 96, v8\n\t" \
"v_lshrrev_b32 v53, 2, v0\n\t" \
"v_and_b32 v53, 3, v53\n\t" \
"v_lshl_add_u32 v53, v54, 3, v53\n\t" \
"s_lshl_b32 s57, s55, 6\n\t" \
"v_xor_b32 v55, s57, v3\n\t" \
"v_lshl_add_u32 v53, v53, 7, v55\n\t" \
"v_add_u32 v12, 65536, v53\n\t" \
"v_add_u32 v13, 0x10000, v12\n\t" \
"s_lshl_b32 s57, s55, 7\n\t" \
"s_add_u32 s57, s57, 160256\n\t" \
"s_lshl_b32 s58, s54, 7\n\t" \
"s_add_u32 s58, s58, 159744\n\t" \
"v_lshrrev_b32 v52, 5, v0\n\t" \
"v_and_b32 v53, 31, v0\n\t" \
"v_lshl_add_u32 v7, v52, 4, s57\n\t" \
"v_lshl_add_u32 v16, v53, 2, s58\n\t" \
"v_lshl_add_u32 v17, v53, 2, s57\n\t" \
"v_lshl_add_u32 v18, v52, 4, s58\n\t" \
"v_and_b32 v54, 0x33, v0\n\t" \
"v_lshlrev_b32 v55, 1, v0\n\t" \
"v_and_b32 v55, 8, v55\n\t" \
"v_or_b32 v54, v54, v55\n\t" \
"v_lshrrev_b32 v55, 1, v0\n\t" \
"v_and_b32 v55, 4, v55\n\t" \
"v_or_b32 v54, v54, v55\n\t" \
"v_lshlrev_b32 v19, 4, v54\n\t" \
"v_bfe_u32 v54, v0, 4, 1\n\t" \
"v_mul_u32_u24 v54, 131072, v54\n\t" \
"v_and_b32 v55, 15, v0\n\t" \
"v_lshl_add_u32 v54, v55, 4, v54\n\t" \
"v_lshl_add_u32 v60, v52, 8, v54\n\t" \
"s_lshl_b32 s57, s46, 2\n\t" \
"s_lshl_b32 s58, s55, 1\n\t" \
"s_add_u32 s57, s57, s58\n\t" \
"s_mul_i32 s63, s57, 131072\n\t" \
"s_lshl_b32 s58, s47, 1\n\t" \
"s_add_u32 s58, s58, s54\n\t" \
"s_mul_i32 s64, s58, 2048\n\t" \
"s_add_u32 s63, s63, s64\n\t" \
"s_add_u32 s63, s63, 1048576\n\t" \
"s_add_u32 s64, s63, 131072\n\t" \
"s_bitcmp1_b32 s46, 0\n\t" \
"s_cselect_b32 s76, -1, 1\n\t" \
"s_mul_i32 s76, s76, 0\n\t" \
"s_add_u32 s76, s76, s63\n\t" \
"s_waitcnt lgkmcnt(0)\n\t" \
"v_mov_b32 v14, 163832\n\t" \
"v_mov_b32 v15, 0\n\t" \
"ds_write2_b32 v14, v15, v15 offset0:0 offset1:1\n\t" \
"s_mov_b32 s28, s24\n\t" \
"s_and_b32 s29, s25, 0xffff\n\t" \
"s_mov_b32 s30, 17563648\n\t" \
"s_mov_b32 s31, 0x20000\n\t" \
"s_mov_b32 s32, s8\n\t" \
"s_and_b32 s33, s9, 0xffff\n\t" \
"s_mov_b32 s34, 2097152\n\t" \
"s_mov_b32 s35, 0x20000\n\t" \
"buffer_load_dwordx2 v[62:63], off, s[28:31], 0 sc1\n\t" \
"s_waitcnt lgkmcnt(0)\n\t" \
"s_barrier\n\t" \
"ds_read_b128 v[64:67], v8 offset:0\n\t" \
"ds_read_b64_tr_b16 v[96:97], v12 offset:0\n\t" \
"ds_read_b64_tr_b16 v[98:99], v12 offset:512\n\t" \
"ds_read_b64_tr_b16 v[128:129], v12 offset:8192\n\t" \
"ds_read_b64_tr_b16 v[130:131], v12 offset:8704\n\t" \
"ds_read_b128 v[68:71], v9 offset:0\n\t" \
"ds_read_b64_tr_b16 v[100:101], v12 offset:2048\n\t" \
"ds_read_b64_tr_b16 v[102:103], v12 offset:2560\n\t" \
"ds_read_b64_tr_b16 v[132:133], v12 offset:10240\n\t" \
"ds_read_b64_tr_b16 v[134:135], v12 offset:10752\n\t" \
"ds_read_b128 v[72:75], v10 offset:0\n\t" \
"ds_read_b64_tr_b16 v[104:105], v12 offset:4096\n\t" \
"ds_read_b64_tr_b16 v[106:107], v12 offset:4608\n\t" \
"ds_read_b64_tr_b16 v[136:137], v12 offset:12288\n\t" \
"ds_read_b64_tr_b16 v[138:139], v12 offset:12800\n\t" \
"ds_read_b128 v[76:79], v11 offset:0\n\t" \
"ds_read_b64_tr_b16 v[108:109], v12 offset:6144\n\t" \
"ds_read_b64_tr_b16 v[110:111], v12 offset:6656\n\t" \
"ds_read_b64_tr_b16 v[140:141], v12 offset:14336\n\t" \
"ds_read_b64_tr_b16 v[142:143], v12 offset:14848\n\t" \
"s_waitcnt lgkmcnt(0)\n\t" \
"s_barrier\n\t" \
"v_mfma_f32_32x32x16_bf16 v[32:47], v[96:99], v[64:67], 0\n\t" \
"ds_read_b128 v[80:83], v8 offset:8192\n\t" \
"ds_read_b64_tr_b16 v[112:113], v12 offset:16384\n\t" \
"ds_read_b64_tr_b16 v[114:115], v12 offset:16896\n\t" \
"v_mfma_f32_32x32x16_bf16 v[208:223], v[64:67], v[128:131], 0\n\t" \
"ds_read_b64_tr_b16 v[144:145], v12 offset:24576\n\t" \
"ds_read_b64_tr_b16 v[146:147], v12 offset:25088\n\t" \
"ds_read_b128 v[84:87], v9 offset:8192\n\t" \
"v_mfma_f32_32x32x16_bf16 v[32:47], v[100:103], v[68:71], v[32:47]\n\t" \
"ds_read_b64_tr_b16 v[116:117], v12 offset:18432\n\t" \
"ds_read_b64_tr_b16 v[118:119], v12 offset:18944\n\t" \
"ds_read_b64_tr_b16 v[148:149], v12 offset:26624\n\t" \
"v_mfma_f32_32x32x16_bf16 v[208:223], v[68:71], v[132:135], v[208:223]\n\t" \
"ds_read_b64_tr_b16 v[150:151], v12 offset:27136\n\t" \
"ds_read_b128 v[88:91], v10 offset:8192\n\t" \
"ds_read_b64_tr_b16 v[120:121], v12 offset:20480\n\t" \
"v_mfma_f32_32x32x16_bf16 v[32:47], v[104:107], v[72:75], v[32:47]\n\t" \
"ds_read_b64_tr_b16 v[122:123], v12 offset:20992\n\t" \
"ds_read_b64_tr_b16 v[152:153], v12 offset:28672\n\t" \
"ds_read_b64_tr_b16 v[154:155], v12 offset:29184\n\t" \
"v_mfma_f32_32x32x16_bf16 v[208:223], v[72:75], v[136:139], v[208:223]\n\t" \
"ds_read_b128 v[92:95], v11 offset:8192\n\t" \
"ds_read_b64_tr_b16 v[124:125], v12 offset:22528\n\t" \
"ds_read_b64_tr_b16 v[126:127], v12 offset:23040\n\t" \
"v_mfma_f32_32x32x16_bf16 v[32:47], v[108:111], v[76:79], v[32:47]\n\t" \
"ds_read_b64_tr_b16 v[156:157], v12 offset:30720\n\t" \
"ds_read_b64_tr_b16 v[158:159], v12 offset:31232\n\t" \
"v_mfma_f32_32x32x16_bf16 v[208:223], v[76:79], v[140:143], v[208:223]\n\t" \
"s_waitcnt lgkmcnt(0)\n\t" \
"s_barrier\n\t" \
"v_mfma_f32_32x32x16_bf16 v[32:47], v[112:115], v[80:83], v[32:47]\n\t" \
"ds_read_b128 v[64:67], v8 offset:16384\n\t" \
"ds_read_b64_tr_b16 v[96:97], v12 offset:32768\n\t" \
"ds_read_b64_tr_b16 v[98:99], v12 offset:33280\n\t" \
"v_mfma_f32_32x32x16_bf16 v[208:223], v[80:83], v[144:147], v[208:223]\n\t" \
"ds_read_b64_tr_b16 v[128:129], v12 offset:40960\n\t" \
"ds_read_b64_tr_b16 v[130:131], v12 offset:41472\n\t" \
"ds_read_b128 v[68:71], v9 offset:16384\n\t" \
"v_mfma_f32_32x32x16_bf16 v[32:47], v[116:119], v[84:87], v[32:47]\n\t" \
"ds_read_b64_tr_b16 v[100:101], v12 offset:34816\n\t" \
"ds_read_b64_tr_b16 v[102:103], v12 offset:35328\n\t" \
"ds_read_b64_tr_b16 v[132:133], v12 offset:43008\n\t" \
"v_mfma_f32_32x32x16_bf16 v[208:223], v[84:87], v[148:151], v[208:223]\n\t" \
"ds_read_b64_tr_b16 v[134:135], v12 offset:43520\n\t" \
"ds_read_b128 v[72:75], v10 offset:16384\n\t" \
"ds_read_b64_tr_b16 v[104:105], v12 offset:36864\n\t" \
"v_mfma_f32_32x32x16_bf16 v[32:47], v[120:123], v[88:91], v[32:47]\n\t" \
"ds_read_b64_tr_b16 v[106:107], v12 offset:37376\n\t" \
"ds_read_b64_tr_b16 v[136:137], v12 offset:45056\n\t" \
"ds_read_b64_tr_b16 v[138:139], v12 offset:45568\n\t" \
"v_mfma_f32_32x32x16_bf16 v[208:223], v[88:91], v[152:155], v[208:223]\n\t" \
"ds_read_b128 v[76:79], v11 offset:16384\n\t" \
"ds_read_b64_tr_b16 v[108:109], v12 offset:38912\n\t" \
"ds_read_b64_tr_b16 v[110:111], v12 offset:39424\n\t" \
"v_mfma_f32_32x32x16_bf16 v[32:47], v[124:127], v[92:95], v[32:47]\n\t" \
"ds_read_b64_tr_b16 v[140:141], v12 offset:47104\n\t" \
"ds_read_b64_tr_b16 v[142:143], v12 offset:47616\n\t" \
"v_mfma_f32_32x32x16_bf16 v[208:223], v[92:95], v[156:159], v[208:223]\n\t" \
"s_waitcnt lgkmcnt(0)\n\t" \
"s_barrier\n\t" \
"v_mfma_f32_32x32x16_bf16 v[32:47], v[96:99], v[64:67], v[32:47]\n\t" \
"ds_read_b128 v[80:83], v8 offset:24576\n\t" \
"ds_read_b64_tr_b16 v[112:113], v12 offset:49152\n\t" \
"ds_read_b64_tr_b16 v[114:115], v12 offset:49664\n\t" \
"v_mfma_f32_32x32x16_bf16 v[208:223], v[64:67], v[128:131], v[208:223]\n\t" \
"ds_read_b64_tr_b16 v[144:145], v12 offset:57344\n\t" \
"ds_read_b64_tr_b16 v[146:147], v12 offset:57856\n\t" \
"ds_read_b128 v[84:87], v9 offset:24576\n\t" \
"v_mfma_f32_32x32x16_bf16 v[32:47], v[100:103], v[68:71], v[32:47]\n\t" \
"ds_read_b64_tr_b16 v[116:117], v12 offset:51200\n\t" \
"ds_read_b64_tr_b16 v[118:119], v12 offset:51712\n\t" \
"ds_read_b64_tr_b16 v[148:149], v12 offset:59392\n\t" \
"v_mfma_f32_32x32x16_bf16 v[208:223], v[68:71], v[132:135], v[208:223]\n\t" \
"ds_read_b64_tr_b16 v[150:151], v12 offset:59904\n\t" \
"ds_read_b128 v[88:91], v10 offset:24576\n\t" \
"ds_read_b64_tr_b16 v[120:121], v12 offset:53248\n\t" \
"v_mfma_f32_32x32x16_bf16 v[32:47], v[104:107], v[72:75], v[32:47]\n\t" \
"ds_read_b64_tr_b16 v[122:123], v12 offset:53760\n\t" \
"ds_read_b64_tr_b16 v[152:153], v12 offset:61440\n\t" \
"ds_read_b64_tr_b16 v[154:155], v12 offset:61952\n\t" \
"v_mfma_f32_32x32x16_bf16 v[208:223], v[72:75], v[136:139], v[208:223]\n\t" \
"ds_read_b128 v[92:95], v11 offset:24576\n\t" \
"ds_read_b64_tr_b16 v[124:125], v12 offset:55296\n\t" \
"ds_read_b64_tr_b16 v[126:127], v12 offset:55808\n\t" \
"v_mfma_f32_32x32x16_bf16 v[32:47], v[108:111], v[76:79], v[32:47]\n\t" \
"ds_read_b64_tr_b16 v[156:157], v12 offset:63488\n\t" \
"ds_read_b64_tr_b16 v[158:159], v12 offset:64000\n\t" \
"v_mfma_f32_32x32x16_bf16 v[208:223], v[76:79], v[140:143], v[208:223]\n\t" \
"s_waitcnt lgkmcnt(0)\n\t" \
"s_barrier\n\t" \
"v_mfma_f32_32x32x16_bf16 v[32:47], v[112:115], v[80:83], v[32:47]\n\t" \
"ds_read_b128 v[64:67], v8 offset:32768\n\t" \
"ds_read_b64_tr_b16 v[96:97], v13 offset:0\n\t" \
"ds_read_b64_tr_b16 v[98:99], v13 offset:512\n\t" \
"v_mfma_f32_32x32x16_bf16 v[208:223], v[80:83], v[144:147], v[208:223]\n\t" \
"ds_read_b64_tr_b16 v[128:129], v13 offset:8192\n\t" \
"ds_read_b64_tr_b16 v[130:131], v13 offset:8704\n\t" \
"ds_read_b128 v[68:71], v9 offset:32768\n\t" \
"v_mfma_f32_32x32x16_bf16 v[32:47], v[116:119], v[84:87], v[32:47]\n\t" \
"ds_read_b64_tr_b16 v[100:101], v13 offset:2048\n\t" \
"ds_read_b64_tr_b16 v[102:103], v13 offset:2560\n\t" \
"ds_read_b64_tr_b16 v[132:133], v13 offset:10240\n\t" \
"v_mfma_f32_32x32x16_bf16 v[208:223], v[84:87], v[148:151], v[208:223]\n\t" \
"ds_read_b64_tr_b16 v[134:135], v13 offset:10752\n\t" \
"ds_read_b128 v[72:75], v10 offset:32768\n\t" \
"ds_read_b64_tr_b16 v[104:105], v13 offset:4096\n\t" \
"v_mfma_f32_32x32x16_bf16 v[32:47], v[120:123], v[88:91], v[32:47]\n\t" \
"ds_read_b64_tr_b16 v[106:107], v13 offset:4608\n\t" \
"ds_read_b64_tr_b16 v[136:137], v13 offset:12288\n\t" \
"ds_read_b64_tr_b16 v[138:139], v13 offset:12800\n\t" \
"v_mfma_f32_32x32x16_bf16 v[208:223], v[88:91], v[152:155], v[208:223]\n\t" \
"ds_read_b128 v[76:79], v11 offset:32768\n\t" \
"ds_read_b64_tr_b16 v[108:109], v13 offset:6144\n\t" \
"ds_read_b64_tr_b16 v[110:111], v13 offset:6656\n\t" \
"v_mfma_f32_32x32x16_bf16 v[32:47], v[124:127], v[92:95], v[32:47]\n\t" \
"ds_read_b64_tr_b16 v[140:141], v13 offset:14336\n\t" \
"ds_read_b64_tr_b16 v[142:143], v13 offset:14848\n\t" \
"v_mfma_f32_32x32x16_bf16 v[208:223], v[92:95], v[156:159], v[208:223]\n\t" \
"s_waitcnt lgkmcnt(0)\n\t" \
"s_barrier\n\t" \
"v_mfma_f32_32x32x16_bf16 v[32:47], v[96:99], v[64:67], v[32:47]\n\t" \
"ds_read_b128 v[80:83], v8 offset:40960\n\t" \
"ds_read_b64_tr_b16 v[112:113], v12 offset:0\n\t" \
"ds_read_b64_tr_b16 v[114:115], v12 offset:512\n\t" \
"v_mfma_f32_32x32x16_bf16 v[208:223], v[64:67], v[128:131], v[208:223]\n\t" \
"ds_read_b64_tr_b16 v[144:145], v12 offset:8192\n\t" \
"ds_read_b64_tr_b16 v[146:147], v12 offset:8704\n\t" \
"ds_read_b128 v[84:87], v9 offset:40960\n\t" \
"v_mfma_f32_32x32x16_bf16 v[32:47], v[100:103], v[68:71], v[32:47]\n\t" \
"ds_read_b64_tr_b16 v[116:117], v12 offset:2048\n\t" \
"ds_read_b64_tr_b16 v[118:119], v12 offset:2560\n\t" \
"ds_read_b64_tr_b16 v[148:149], v12 offset:10240\n\t" \
"v_mfma_f32_32x32x16_bf16 v[208:223], v[68:71], v[132:135], v[208:223]\n\t" \
"ds_read_b64_tr_b16 v[150:151], v12 offset:10752\n\t" \
"ds_read_b128 v[88:91], v10 offset:40960\n\t" \
"ds_read_b64_tr_b16 v[120:121], v12 offset:4096\n\t" \
"v_mfma_f32_32x32x16_bf16 v[32:47], v[104:107], v[72:75], v[32:47]\n\t" \
"ds_read_b64_tr_b16 v[122:123], v12 offset:4608\n\t" \
"ds_read_b64_tr_b16 v[152:153], v12 offset:12288\n\t" \
"ds_read_b64_tr_b16 v[154:155], v12 offset:12800\n\t" \
"v_mfma_f32_32x32x16_bf16 v[208:223], v[72:75], v[136:139], v[208:223]\n\t" \
"ds_read_b128 v[92:95], v11 offset:40960\n\t" \
"ds_read_b64_tr_b16 v[124:125], v12 offset:6144\n\t" \
"ds_read_b64_tr_b16 v[126:127], v12 offset:6656\n\t" \
"v_mfma_f32_32x32x16_bf16 v[32:47], v[108:111], v[76:79], v[32:47]\n\t" \
"ds_read_b64_tr_b16 v[156:157], v12 offset:14336\n\t" \
"ds_read_b64_tr_b16 v[158:159], v12 offset:14848\n\t" \
"v_mfma_f32_32x32x16_bf16 v[208:223], v[76:79], v[140:143], v[208:223]\n\t" \
"s_waitcnt lgkmcnt(0)\n\t" \
"s_barrier\n\t" \
"v_mfma_f32_32x32x16_bf16 v[32:47], v[112:115], v[80:83], v[32:47]\n\t" \
"ds_read_b128 v[64:67], v8 offset:49152\n\t" \
"ds_read_b64_tr_b16 v[96:97], v12 offset:16384\n\t" \
"ds_read_b64_tr_b16 v[98:99], v12 offset:16896\n\t" \
"v_mfma_f32_32x32x16_bf16 v[208:223], v[80:83], v[144:147], v[208:223]\n\t" \
"ds_read_b64_tr_b16 v[128:129], v12 offset:24576\n\t" \
"ds_read_b64_tr_b16 v[130:131], v12 offset:25088\n\t" \
"ds_read_b128 v[68:71], v9 offset:49152\n\t" \
"v_mfma_f32_32x32x16_bf16 v[32:47], v[116:119], v[84:87], v[32:47]\n\t" \
"ds_read_b64_tr_b16 v[100:101], v12 offset:18432\n\t" \
"ds_read_b64_tr_b16 v[102:103], v12 offset:18944\n\t" \
"ds_read_b64_tr_b16 v[132:133], v12 offset:26624\n\t" \
"v_mfma_f32_32x32x16_bf16 v[208:223], v[84:87], v[148:151], v[208:223]\n\t" \
"ds_read_b64_tr_b16 v[134:135], v12 offset:27136\n\t" \
"ds_read_b128 v[72:75], v10 offset:49152\n\t" \
"ds_read_b64_tr_b16 v[104:105], v12 offset:20480\n\t" \
"v_mfma_f32_32x32x16_bf16 v[32:47], v[120:123], v[88:91], v[32:47]\n\t" \
"ds_read_b64_tr_b16 v[106:107], v12 offset:20992\n\t" \
"ds_read_b64_tr_b16 v[136:137], v12 offset:28672\n\t" \
"ds_read_b64_tr_b16 v[138:139], v12 offset:29184\n\t" \
"v_mfma_f32_32x32x16_bf16 v[208:223], v[88:91], v[152:155], v[208:223]\n\t" \
"ds_read_b128 v[76:79], v11 offset:49152\n\t" \
"ds_read_b64_tr_b16 v[108:109], v12 offset:22528\n\t" \
"ds_read_b64_tr_b16 v[110:111], v12 offset:23040\n\t" \
"v_mfma_f32_32x32x16_bf16 v[32:47], v[124:127], v[92:95], v[32:47]\n\t" \
"ds_read_b64_tr_b16 v[140:141], v12 offset:30720\n\t" \
"ds_read_b64_tr_b16 v[142:143], v12 offset:31232\n\t" \
"v_mfma_f32_32x32x16_bf16 v[208:223], v[92:95], v[156:159], v[208:223]\n\t" \
"s_waitcnt lgkmcnt(0)\n\t" \
"s_barrier\n\t" \
"v_mfma_f32_32x32x16_bf16 v[32:47], v[96:99], v[64:67], v[32:47]\n\t" \
"ds_read_b128 v[80:83], v8 offset:57344\n\t" \
"ds_read_b64_tr_b16 v[112:113], v12 offset:32768\n\t" \
"ds_read_b64_tr_b16 v[114:115], v12 offset:33280\n\t" \
"v_mfma_f32_32x32x16_bf16 v[208:223], v[64:67], v[128:131], v[208:223]\n\t" \
"ds_read_b64_tr_b16 v[144:145], v12 offset:40960\n\t" \
"ds_read_b64_tr_b16 v[146:147], v12 offset:41472\n\t" \
"ds_read_b128 v[84:87], v9 offset:57344\n\t" \
"v_mfma_f32_32x32x16_bf16 v[32:47], v[100:103], v[68:71], v[32:47]\n\t" \
"ds_read_b64_tr_b16 v[116:117], v12 offset:34816\n\t" \
"ds_read_b64_tr_b16 v[118:119], v12 offset:35328\n\t" \
"ds_read_b64_tr_b16 v[148:149], v12 offset:43008\n\t" \
"v_mfma_f32_32x32x16_bf16 v[208:223], v[68:71], v[132:135], v[208:223]\n\t" \
"ds_read_b64_tr_b16 v[150:151], v12 offset:43520\n\t" \
"ds_read_b128 v[88:91], v10 offset:57344\n\t" \
"ds_read_b64_tr_b16 v[120:121], v12 offset:36864\n\t" \
"v_mfma_f32_32x32x16_bf16 v[32:47], v[104:107], v[72:75], v[32:47]\n\t" \
"ds_read_b64_tr_b16 v[122:123], v12 offset:37376\n\t" \
"ds_read_b64_tr_b16 v[152:153], v12 offset:45056\n\t" \
"ds_read_b64_tr_b16 v[154:155], v12 offset:45568\n\t" \
"v_mfma_f32_32x32x16_bf16 v[208:223], v[72:75], v[136:139], v[208:223]\n\t" \
"ds_read_b128 v[92:95], v11 offset:57344\n\t" \
"ds_read_b64_tr_b16 v[124:125], v12 offset:38912\n\t" \
"ds_read_b64_tr_b16 v[126:127], v12 offset:39424\n\t" \
"v_mfma_f32_32x32x16_bf16 v[32:47], v[108:111], v[76:79], v[32:47]\n\t" \
"ds_read_b64_tr_b16 v[156:157], v12 offset:47104\n\t" \
"ds_read_b64_tr_b16 v[158:159], v12 offset:47616\n\t" \
"v_mfma_f32_32x32x16_bf16 v[208:223], v[76:79], v[140:143], v[208:223]\n\t" \
"s_waitcnt lgkmcnt(14)\n\t" \
"v_mfma_f32_32x32x16_bf16 v[32:47], v[112:115], v[80:83], v[32:47]\n\t" \
"v_mfma_f32_32x32x16_bf16 v[208:223], v[80:83], v[144:147], v[208:223]\n\t" \
"s_waitcnt lgkmcnt(12)\n\t" \
"v_mfma_f32_32x32x16_bf16 v[32:47], v[116:119], v[84:87], v[32:47]\n\t" \
"s_waitcnt lgkmcnt(10)\n\t" \
"v_mfma_f32_32x32x16_bf16 v[208:223], v[84:87], v[148:151], v[208:223]\n\t" \
"s_waitcnt lgkmcnt(7)\n\t" \
"v_mfma_f32_32x32x16_bf16 v[32:47], v[120:123], v[88:91], v[32:47]\n\t" \
"s_waitcnt lgkmcnt(5)\n\t" \
"v_mfma_f32_32x32x16_bf16 v[208:223], v[88:91], v[152:155], v[208:223]\n\t" \
"s_waitcnt lgkmcnt(2)\n\t" \
"v_mfma_f32_32x32x16_bf16 v[32:47], v[124:127], v[92:95], v[32:47]\n\t" \
"s_waitcnt lgkmcnt(0)\n\t" \
"v_mfma_f32_32x32x16_bf16 v[208:223], v[92:95], v[156:159], v[208:223]\n\t" \
"s_setprio 2\n\t" \
"s_barrier\n\t" \
"ds_read2st64_b32 v[56:57], v16 offset0:0 offset1:1\n\t" \
"ds_read_b128 v[64:67], v7 offset:0\n\t" \
"ds_read_b128 v[68:71], v7 offset:32\n\t" \
"ds_read_b128 v[72:75], v7 offset:64\n\t" \
"ds_read_b128 v[76:79], v7 offset:96\n\t" \
"ds_read_b32 v58, v17 offset:256\n\t" \
"ds_read_b128 v[96:99], v18 offset:0\n\t" \
"ds_read_b128 v[100:103], v18 offset:256\n\t" \
"ds_read_b128 v[104:107], v18 offset:32\n\t" \
"ds_read_b128 v[108:111], v18 offset:288\n\t" \
"ds_read_b128 v[112:115], v18 offset:64\n\t" \
"ds_read_b128 v[116:119], v18 offset:320\n\t" \
"ds_read_b128 v[120:123], v18 offset:96\n\t" \
"ds_read_b128 v[124:127], v18 offset:352\n\t" \
"s_waitcnt lgkmcnt(12)\n\t" \
"v_mul_f32 v64, v57, v64\n\t" \
"v_mul_f32 v65, v57, v65\n\t" \
"v_mul_f32 v66, v57, v66\n\t" \
"v_mul_f32 v67, v57, v67\n\t" \
"v_fmac_f32 v64, v56, v32\n\t" \
"v_fmac_f32 v65, v56, v33\n\t" \
"v_fmac_f32 v66, v56, v34\n\t" \
"v_fmac_f32 v67, v56, v35\n\t" \
"v_cvt_pk_bf16_f32 v192, v64, v65\n\t" \
"v_cvt_pk_bf16_f32 v193, v66, v67\n\t" \
"s_waitcnt lgkmcnt(11)\n\t" \
"v_mul_f32 v68, v57, v68\n\t" \
"v_mul_f32 v69, v57, v69\n\t" \
"v_mul_f32 v70, v57, v70\n\t" \
"v_mul_f32 v71, v57, v71\n\t" \
"v_fmac_f32 v68, v56, v36\n\t" \
"v_fmac_f32 v69, v56, v37\n\t" \
"v_fmac_f32 v70, v56, v38\n\t" \
"v_fmac_f32 v71, v56, v39\n\t" \
"v_cvt_pk_bf16_f32 v194, v68, v69\n\t" \
"v_cvt_pk_bf16_f32 v195, v70, v71\n\t" \
"buffer_store_dwordx4 v[192:195], v19, s[28:31], s63 offen sc1\n\t" \
"s_waitcnt lgkmcnt(10)\n\t" \
"v_mul_f32 v72, v57, v72\n\t" \
"v_mul_f32 v73, v57, v73\n\t" \
"v_mul_f32 v74, v57, v74\n\t" \
"v_mul_f32 v75, v57, v75\n\t" \
"v_fmac_f32 v72, v56, v40\n\t" \
"v_fmac_f32 v73, v56, v41\n\t" \
"v_fmac_f32 v74, v56, v42\n\t" \
"v_fmac_f32 v75, v56, v43\n\t" \
"v_cvt_pk_bf16_f32 v196, v72, v73\n\t" \
"v_cvt_pk_bf16_f32 v197, v74, v75\n\t" \
"s_waitcnt lgkmcnt(9)\n\t" \
"v_mul_f32 v76, v57, v76\n\t" \
"v_mul_f32 v77, v57, v77\n\t" \
"v_mul_f32 v78, v57, v78\n\t" \
"v_mul_f32 v79, v57, v79\n\t" \
"v_fmac_f32 v76, v56, v44\n\t" \
"v_fmac_f32 v77, v56, v45\n\t" \
"v_fmac_f32 v78, v56, v46\n\t" \
"v_fmac_f32 v79, v56, v47\n\t" \
"v_cvt_pk_bf16_f32 v198, v76, v77\n\t" \
"v_cvt_pk_bf16_f32 v199, v78, v79\n\t" \
"buffer_store_dwordx4 v[196:199], v19, s[28:31], s64 offen sc1\n\t" \
"s_waitcnt lgkmcnt(6)\n\t" \
"v_mul_f32 v100, v58, v100\n\t" \
"v_mul_f32 v101, v58, v101\n\t" \
"v_mul_f32 v102, v58, v102\n\t" \
"v_mul_f32 v103, v58, v103\n\t" \
"v_fmac_f32 v100, v96, v208\n\t" \
"v_fmac_f32 v101, v97, v209\n\t" \
"v_fmac_f32 v102, v98, v210\n\t" \
"v_fmac_f32 v103, v99, v211\n\t" \
"v_cvt_pk_bf16_f32 v200, v100, v101\n\t" \
"v_cvt_pk_bf16_f32 v201, v102, v103\n\t" \
"s_waitcnt lgkmcnt(4)\n\t" \
"v_mul_f32 v108, v58, v108\n\t" \
"v_mul_f32 v109, v58, v109\n\t" \
"v_mul_f32 v110, v58, v110\n\t" \
"v_mul_f32 v111, v58, v111\n\t" \
"v_fmac_f32 v108, v104, v212\n\t" \
"v_fmac_f32 v109, v105, v213\n\t" \
"v_fmac_f32 v110, v106, v214\n\t" \
"v_fmac_f32 v111, v107, v215\n\t" \
"v_cvt_pk_bf16_f32 v202, v108, v109\n\t" \
"v_cvt_pk_bf16_f32 v203, v110, v111\n\t" \
"buffer_store_dwordx4 v[200:203], v60, s[28:31], s76 offen offset:1024 sc1\n\t" \
"s_waitcnt lgkmcnt(2)\n\t" \
"v_mul_f32 v116, v58, v116\n\t" \
"v_mul_f32 v117, v58, v117\n\t" \
"v_mul_f32 v118, v58, v118\n\t" \
"v_mul_f32 v119, v58, v119\n\t" \
"v_fmac_f32 v116, v112, v216\n\t" \
"v_fmac_f32 v117, v113, v217\n\t" \
"v_fmac_f32 v118, v114, v218\n\t" \
"v_fmac_f32 v119, v115, v219\n\t" \
"v_cvt_pk_bf16_f32 v204, v116, v117\n\t" \
"v_cvt_pk_bf16_f32 v205, v118, v119\n\t" \
"s_waitcnt lgkmcnt(0)\n\t" \
"v_mul_f32 v124, v58, v124\n\t" \
"v_mul_f32 v125, v58, v125\n\t" \
"v_mul_f32 v126, v58, v126\n\t" \
"v_mul_f32 v127, v58, v127\n\t" \
"v_fmac_f32 v124, v120, v220\n\t" \
"v_fmac_f32 v125, v121, v221\n\t" \
"v_fmac_f32 v126, v122, v222\n\t" \
"v_fmac_f32 v127, v123, v223\n\t" \
"v_cvt_pk_bf16_f32 v206, v124, v125\n\t" \
"v_cvt_pk_bf16_f32 v207, v126, v127\n\t" \
"buffer_store_dwordx4 v[204:207], v60, s[28:31], s76 offen offset:1536 sc1\n\t" \
"s_setprio 0\n\t" \
"s_barrier\n\t" \
"s_waitcnt vmcnt(4)\n\t" \
"v_readfirstlane_b32 s44, v62\n\t" \
"v_readfirstlane_b32 s45, v63\n\t" \
"s_nop 0\n\t" \
"s_add_u32 s44, s44, 1\n\t" \
"s_addc_u32 s45, s45, 0\n\t" \
"s_mul_i32 s58, s49, 37\n\t" \
"s_lshr_b32 s58, s58, 8\n\t" \
"s_mul_i32 s57, s58, 7\n\t" \
"s_sub_u32 s57, s49, s57\n\t" \
"s_cmp_ge_u32 s57, 3\n\t" \
"s_addc_u32 s50, s57, 0\n\t" \
"s_cmp_ge_u32 s49, 28\n\t" \
"s_cselect_b32 s50, 3, s50\n\t" \
"s_cselect_b32 s58, s57, s58\n\t" \
"s_lshl_b32 s51, s48, 2\n\t" \
"s_add_u32 s51, s51, s58\n\t" \
"s_cmp_ge_u32 s49, 28\n\t" \
"s_cselect_b32 s52, 0, s49\n\t" \
"s_mov_b32 s36, s18\n\t" \
"s_and_b32 s37, s19, 0xffff\n\t" \
"s_mov_b32 s38, 1835008\n\t" \
"s_mov_b32 s39, 0x20000\n\t" \
"s_mov_b32 s40, s14\n\t" \
"s_and_b32 s41, s15, 0xffff\n\t" \
"s_mov_b32 s42, 1835008\n\t" \
"s_mov_b32 s43, 0x20000\n\t" \
"s_mov_b32 s84, s12\n\t" \
"s_and_b32 s85, s13, 0xffff\n\t" \
"s_mov_b32 s86, 524288\n\t" \
"s_mov_b32 s87, 0x20000\n\t" \
"s_cmp_ge_u32 s49, 28\n\t" \
"s_cselect_b32 s38, 0, s38\n\t" \
"s_cselect_b32 s42, 0, s42\n\t" \
"s_lshl_b32 s62, s56, 12\n\t" \
"s_lshl_b32 s61, s52, 7\n\t" \
"s_mul_i32 s57, s56, 114688\n\t" \
"s_add_u32 s61, s61, s57\n\t" \
"s_lshl_b32 s81, s50, 7\n\t" \
"s_lshl_b32 s57, s56, 17\n\t" \
"s_add_u32 s60, s81, s57\n\t" \
"s_mul_i32 s57, s55, 1802240\n\t" \
"s_sub_u32 s66, s60, s57\n\t" \
"s_lshl_b32 s57, s56, 15\n\t" \
"s_add_u32 s81, s81, s57\n\t" \
"s_lshr_b32 s57, s58, 1\n\t" \
"s_lshl_b32 s57, s57, 16\n\t" \
"s_sub_u32 s53, s60, s57\n\t" \
"s_sub_u32 s83, s66, s57\n\t" \
"s_add_u32 s60, s60, s57\n\t" \
"s_add_u32 s66, s66, s57\n\t" \
"s_add_u32 s82, s60, 819200\n\t" \
"s_add_u32 s57, s53, 65536\n\t" \
"s_bitcmp1_b32 s58, 0\n\t" \
"s_cselect_b32 s82, s57, s82\n\t" \
"s_add_u32 s79, s82, 524288\n\t" \
"v_lshrrev_b32 v68, 5, v0\n\t" \
"v_and_b32 v69, 15, v0\n\t" \
"v_lshlrev_b32 v68, 8, v68\n\t" \
"v_lshl_add_u32 v6, v69, 4, v68\n\t" \
"s_mov_b32 s94, 0xffff\n\t" \
"s_mov_b32 s95, 0xffff\n\t" \
"v_and_b32 v68, 27, v0\n\t" \
"v_cmp_eq_u32 vcc, 16, v68\n\t" \
"v_mov_b32 v68, 0x3f803f80\n\t" \
"s_nop 0\n\t" \
"v_cndmask_b32 v144, 0, v68, vcc\n\t" \
"v_mov_b32 v145, v144\n\t" \
"v_mov_b32 v146, v144\n\t" \
"v_mov_b32 v147, v144\n\t" \
"v_mov_b32 v148, v144\n\t" \
"v_mov_b32 v149, v144\n\t" \
"v_mov_b32 v150, v144\n\t" \
"v_mov_b32 v151, v144\n\t" \
"v_mov_b32 v152, v144\n\t" \
"v_mov_b32 v153, v144\n\t" \
"v_mov_b32 v154, v144\n\t" \
"v_mov_b32 v155, v144\n\t" \
"v_mov_b32 v156, v144\n\t" \
"v_mov_b32 v157, v144\n\t" \
"v_mov_b32 v158, v144\n\t" \
"v_mov_b32 v159, v144\n\t" \
"v_mov_b32 v160, v144\n\t" \
"v_mov_b32 v161, v144\n\t" \
"v_mov_b32 v162, v144\n\t" \
"v_mov_b32 v163, v144\n\t" \
"v_mov_b32 v164, v144\n\t" \
"v_mov_b32 v165, v144\n\t" \
"v_mov_b32 v166, v144\n\t" \
"v_mov_b32 v167, v144\n\t" \
"v_mov_b32 v168, v144\n\t" \
"v_mov_b32 v169, v144\n\t" \
"v_mov_b32 v170, v144\n\t" \
"v_mov_b32 v171, v144\n\t" \
"v_mov_b32 v172, v144\n\t" \
"v_mov_b32 v173, v144\n\t" \
"v_mov_b32 v174, v144\n\t" \
"v_mov_b32 v175, v144\n\t" \
"v_mov_b32 v7, 0xdddd\n\t" \
"s_lshl_b32 s57, s49, 2\n\t" \
"s_add_u32 s57, s57, s56\n\t" \
"s_lshl_b32 s57, s57, 9\n\t" \
"v_and_b32 v18, 7, v0\n\t" \
"v_lshl_add_u32 v18, v18, 6, s57\n\t" \
"v_min_u32 v18, 47008, v18\n\t" \
"v_lshrrev_b32 v68, 3, v0\n\t" \
"v_lshl_add_u32 v11, v68, 10, v2\n\t" \
"v_mul_u32_u24 v13, 0xe00, v68\n\t" \
"v_add_u32 v13, v13, v2\n\t" \
"v_mov_b32 v32, 0\n\t" \
"v_mov_b32 v33, 0\n\t" \
"v_mov_b32 v34, 0\n\t" \
"v_mov_b32 v35, 0\n\t" \
"v_mov_b32 v36, 0\n\t" \
"v_mov_b32 v37, 0\n\t" \
"v_mov_b32 v38, 0\n\t" \
"v_mov_b32 v39, 0\n\t" \
"v_mov_b32 v40, 0\n\t" \
"v_mov_b32 v41, 0\n\t" \
"v_mov_b32 v42, 0\n\t" \
"v_mov_b32 v43, 0\n\t" \
"v_mov_b32 v44, 0\n\t" \
"v_mov_b32 v45, 0\n\t" \
"v_mov_b32 v46, 0\n\t" \
"v_mov_b32 v47, 0\n\t" \
"v_mov_b32 v48, 0\n\t" \
"v_mov_b32 v49, 0\n\t" \
"v_mov_b32 v50, 0\n\t" \
"v_mov_b32 v51, 0\n\t" \
"v_mov_b32 v52, 0\n\t" \
"v_mov_b32 v53, 0\n\t" \
"v_mov_b32 v54, 0\n\t" \
"v_mov_b32 v55, 0\n\t" \
"v_mov_b32 v56, 0\n\t" \
"v_mov_b32 v57, 0\n\t" \
"v_mov_b32 v58, 0\n\t" \
"v_mov_b32 v59, 0\n\t" \
"v_mov_b32 v60, 0\n\t" \
"v_mov_b32 v61, 0\n\t" \
"v_mov_b32 v62, 0\n\t" \
"v_mov_b32 v63, 0\n\t" \
"s_lshl_b32 s57, s46, 2\n\t" \
"s_add_u32 s57, s57, s56\n\t" \
"s_mul_i32 s64, s57, 131072\n\t" \
"s_add_u32 s64, s64, 1048576\n\t" \
"s_cmp_lt_u32 s26, 4\n\t" \
"s_cselect_b32 s57, 0, 34816\n\t" \
"s_cselect_b32 s77, 49, 15\n\t" \
"s_add_u32 s78, s64, s57\n\t" \
"s_bitcmp1_b32 s47, 4\n\t" \
"s_cselect_b32 s57, 65536, 0\n\t" \
"s_add_u32 s78, s78, s57\n\t" \
"s_sub_u32 s76, s64, s57\n\t" \
"s_add_u32 s76, s76, 65536\n\t" \
"s_lshl_b32 s88, s46, 5\n\t" \
"s_add_u32 s88, s88, s47\n\t" \
"s_and_b32 s88, s88, 65520\n\t" \
"s_xor_b32 s88, s88, 16\n\t" \
"s_lshl_b32 s88, s88, 7\n\t" \
"s_add_u32 s88, s88, 65536\n\t" \
"v_and_b32 v19, 15, v0\n\t" \
"v_lshlrev_b32 v19, 7, v19\n\t" \
"s_xor_b32 s58, s26, 4\n\t" \
"s_lshl_b32 s57, s26, 10\n\t" \
"s_add_u32 s57, s57, 151552\n\t" \
"s_lshl_b32 s58, s58, 10\n\t" \
"s_add_u32 s58, s58, 151552\n\t" \
"v_add_u32 v10, s57, v1\n\t" \
"v_add_u32 v12, s58, v1\n\t" \
"s_lshl_b32 s57, s46, 5\n\t" \
"s_add_u32 s57, s57, s47\n\t" \
"s_lshl_b32 s57, s57, 7\n\t" \
"s_add_u32 s57, s57, 65536\n\t" \
"v_mov_b32 v15, 1\n\t" \
"s_mov_b64 exec, 1\n\t" \
"s_waitcnt vmcnt(0)\n\t" \
"ds_add_rtn_u32 v15, v14, v15\n\t" \
"s_waitcnt lgkmcnt(0)\n\t" \
"v_readfirstlane_b32 s58, v15\n\t" \
"s_mov_b64 exec, -1\n\t" \
"s_cmp_lg_u32 s58, 3\n\t" \
"s_cbranch_scc1 Latt0_%=\n\t" \
"v_mov_b32 v64, s44\n\t" \
"v_mov_b32 v65, s45\n\t" \
"v_mov_b32 v66, s44\n\t" \
"v_mov_b32 v67, s45\n\t" \
"s_mov_b64 exec, 0xff\n\t" \
"buffer_store_dwordx4 v[64:67], v1, s[28:31], s57 offen sc1\n\t" \
"s_mov_b64 exec, -1\n\t" \
"s_xor_b32 s57, s88, 2048\n\t" \
"v_mov_b32 v68, v19\n\t" \
"s_sleep 12\n\t" \
"s_mov_b32 s65, 0x40000\n\t" \
"Lpoll1_%=:\n\t" \
"buffer_load_dwordx2 v[70:71], v68, s[28:31], s57 offen sc1\n\t" \
"s_waitcnt vmcnt(0)\n\t" \
"v_cmp_ne_u64 vcc, s[44:45], v[70:71]\n\t" \
"s_nop 0\n\t" \
"s_cmp_eq_u64 vcc, 0\n\t" \
"s_cbranch_scc1 Lpdone1_%=\n\t" \
"s_sleep 1\n\t" \
"s_sub_u32 s65, s65, 1\n\t" \
"s_cmp_lg_u32 s65, 0\n\t" \
"s_cbranch_scc1 Lpoll1_%=\n\t" \
"Lpdone1_%=:\n\t" \
"Latt0_%=:\n\t" \
"s_waitcnt lgkmcnt(0)\n\t" \
"s_barrier\n\t" \
"s_mov_b32 s59, s78\n\t" \
"s_cmp_ge_u32 s26, 4\n\t" \
"s_cbranch_scc1 Lattb_%=\n\t" \
"s_setprio 2\n\t" \
"buffer_load_dwordx4 v[128:131], v1, s[28:31], s59 offen sc1\n\t" \
"ds_read_b128 v[24:27], v12\n\t" \
"ds_read_b128 v[28:31], v10\n\t" \
"s_mov_b64 exec, s[94:95]\n\t" \
"buffer_load_dwordx4 v[144:147], v6, s[28:31], s59 offen offset:1024 sc1\n\t" \
"buffer_load_dwordx4 v[160:163], v6, s[28:31], s59 offen offset:1536 sc1\n\t" \
"s_mov_b64 exec, -1\n\t" \
"s_add_u32 s59, s59, 2048\n\t" \
"buffer_load_dwordx4 v[132:135], v1, s[28:31], s59 offen sc1\n\t" \
"s_mov_b64 exec, s[94:95]\n\t" \
"buffer_load_dwordx4 v[148:151], v6, s[28:31], s59 offen offset:1024 sc1\n\t" \
"buffer_load_dwordx4 v[164:167], v6, s[28:31], s59 offen offset:1536 sc1\n\t" \
"s_mov_b64 exec, -1\n\t" \
"s_add_u32 s59, s59, 2048\n\t" \
"s_waitcnt vmcnt(5) lgkmcnt(0)\n\t" \
"v_mfma_f32_32x32x16_bf16 v[64:79], v[128:131], v[24:27], 0\n\t" \
"v_mfma_f32_32x32x16_bf16 v[80:95], v[128:131], v[28:31], 0\n\t" \
"buffer_load_dwordx4 v[136:139], v1, s[28:31], s59 offen sc1\n\t" \
"s_mov_b64 exec, s[94:95]\n\t" \
"buffer_load_dwordx4 v[152:155], v6, s[28:31], s59 offen offset:1024 sc1\n\t" \
"buffer_load_dwordx4 v[168:171], v6, s[28:31], s59 offen offset:1536 sc1\n\t" \
"s_mov_b64 exec, -1\n\t" \
"s_add_u32 s59, s59, 2048\n\t" \
"buffer_load_dwordx4 v[140:143], v1, s[28:31], s59 offen sc1\n\t" \
"s_mov_b64 exec, s[94:95]\n\t" \
"buffer_load_dwordx4 v[156:159], v6, s[28:31], s59 offen offset:1024 sc1\n\t" \
"buffer_load_dwordx4 v[172:175], v6, s[28:31], s59 offen offset:1536 sc1\n\t" \
"s_mov_b64 exec, -1\n\t" \
"s_add_u32 s59, s59, 2048\n\t" \
"v_max3_f32 v8, v64, v65, v66\n\t" \
"v_max3_f32 v8, v8, v67, v68\n\t" \
"v_max3_f32 v8, v8, v69, v70\n\t" \
"v_max3_f32 v8, v8, v71, v72\n\t" \
"v_max3_f32 v8, v8, v73, v74\n\t" \
"v_max3_f32 v8, v8, v75, v76\n\t" \
"v_max3_f32 v8, v8, v77, v78\n\t" \
"v_max_f32 v8, v8, v79\n\t" \
"v_mov_b32 v10, v8\n\t" \
"s_nop 1\n\t" \
"v_permlane32_swap_b32 v10, v8\n\t" \
"v_max_f32 v8, v8, v10\n\t" \
"v_sub_f32 v96, 0, v8\n\t" \
"v_sub_f32 v97, 0, v8\n\t" \
"v_sub_f32 v98, 0, v8\n\t" \
"v_sub_f32 v99, 0, v8\n\t" \
"v_sub_f32 v100, 0, v8\n\t" \
"v_sub_f32 v101, 0, v8\n\t" \
"v_sub_f32 v102, 0, v8\n\t" \
"v_sub_f32 v103, 0, v8\n\t" \
"v_sub_f32 v104, 0, v8\n\t" \
"v_sub_f32 v105, 0, v8\n\t" \
"v_sub_f32 v106, 0, v8\n\t" \
"v_sub_f32 v107, 0, v8\n\t" \
"v_sub_f32 v108, 0, v8\n\t" \
"v_sub_f32 v109, 0, v8\n\t" \
"v_sub_f32 v110, 0, v8\n\t" \
"v_sub_f32 v111, 0, v8\n\t" \
"s_nop 1\n\t" \
"v_mfma_f32_32x32x16_bf16 v[64:79], v[128:131], v[24:27], v[96:111]\n\t" \
"v_max3_f32 v9, v80, v81, v82\n\t" \
"v_max3_f32 v9, v9, v83, v84\n\t" \
"v_max3_f32 v9, v9, v85, v86\n\t" \
"v_max3_f32 v9, v9, v87, v88\n\t" \
"v_max3_f32 v9, v9, v89, v90\n\t" \
"v_max3_f32 v9, v9, v91, v92\n\t" \
"v_max3_f32 v9, v9, v93, v94\n\t" \
"v_max_f32 v9, v9, v95\n\t" \
"v_mov_b32 v10, v9\n\t" \
"s_nop 1\n\t" \
"v_permlane32_swap_b32 v10, v9\n\t" \
"v_max_f32 v9, v9, v10\n\t" \
"v_sub_f32 v112, 0, v9\n\t" \
"v_sub_f32 v113, 0, v9\n\t" \
"v_sub_f32 v114, 0, v9\n\t" \
"v_sub_f32 v115, 0, v9\n\t" \
"v_sub_f32 v116, 0, v9\n\t" \
"v_sub_f32 v117, 0, v9\n\t" \
"v_sub_f32 v118, 0, v9\n\t" \
"v_sub_f32 v119, 0, v9\n\t" \
"v_sub_f32 v120, 0, v9\n\t" \
"v_sub_f32 v121, 0, v9\n\t" \
"v_sub_f32 v122, 0, v9\n\t" \
"v_sub_f32 v123, 0, v9\n\t" \
"v_sub_f32 v124, 0, v9\n\t" \
"v_sub_f32 v125, 0, v9\n\t" \
"v_sub_f32 v126, 0, v9\n\t" \
"v_sub_f32 v127, 0, v9\n\t" \
"v_exp_f32 v64, v64\n\t" \
"v_exp_f32 v65, v65\n\t" \
"s_setprio 2\n\t" \
"v_exp_f32 v66, v66\n\t" \
"v_exp_f32 v67, v67\n\t" \
"v_mfma_f32_32x32x16_bf16 v[80:95], v[128:131], v[28:31], v[112:127]\n\t" \
"buffer_load_dwordx4 v[128:131], v1, s[28:31], s59 offen sc1\n\t" \
"v_exp_f32 v68, v68\n\t" \
"v_exp_f32 v69, v69\n\t" \
"v_exp_f32 v70, v70\n\t" \
"v_exp_f32 v71, v71\n\t" \
"v_exp_f32 v72, v72\n\t" \
"v_exp_f32 v73, v73\n\t" \
"s_waitcnt vmcnt(11)\n\t" \
"v_smfmac_f32_32x32x32_bf16 v[32:47], v[144:147], v[64:71], v7\n\t" \
"v_exp_f32 v74, v74\n\t" \
"v_exp_f32 v75, v75\n\t" \
"v_exp_f32 v76, v76\n\t" \
"v_exp_f32 v77, v77\n\t" \
"v_exp_f32 v78, v78\n\t" \
"v_exp_f32 v79, v79\n\t" \
"v_exp_f32 v80, v80\n\t" \
"v_exp_f32 v81, v81\n\t" \
"s_waitcnt vmcnt(10)\n\t" \
"v_smfmac_f32_32x32x32_bf16 v[32:47], v[160:163], v[72:79], v7\n\t" \
"v_exp_f32 v82, v82\n\t" \
"v_exp_f32 v83, v83\n\t" \
"s_waitcnt vmcnt(9)\n\t" \
"v_mfma_f32_32x32x16_bf16 v[64:79], v[132:135], v[24:27], v[96:111]\n\t" \
"v_exp_f32 v84, v84\n\t" \
"v_exp_f32 v85, v85\n\t" \
"v_exp_f32 v86, v86\n\t" \
"v_exp_f32 v87, v87\n\t" \
"v_exp_f32 v88, v88\n\t" \
"v_exp_f32 v89, v89\n\t" \
"v_smfmac_f32_32x32x32_bf16 v[48:63], v[144:147], v[80:87], v7\n\t" \
"s_mov_b64 exec, s[94:95]\n\t" \
"buffer_load_dwordx4 v[144:147], v6, s[28:31], s59 offen offset:1024 sc1\n\t" \
"s_mov_b64 exec, -1\n\t" \
"v_exp_f32 v90, v90\n\t" \
"v_exp_f32 v91, v91\n\t" \
"v_exp_f32 v92, v92\n\t" \
"v_exp_f32 v93, v93\n\t" \
"v_exp_f32 v94, v94\n\t" \
"v_exp_f32 v95, v95\n\t" \
"v_exp_f32 v64, v64\n\t" \
"v_exp_f32 v65, v65\n\t" \
"v_smfmac_f32_32x32x32_bf16 v[48:63], v[160:163], v[88:95], v7\n\t" \
"s_mov_b64 exec, s[94:95]\n\t" \
"buffer_load_dwordx4 v[160:163], v6, s[28:31], s59 offen offset:1536 sc1\n\t" \
"s_mov_b64 exec, -1\n\t" \
"s_add_u32 s59, s59, 2048\n\t" \
"v_exp_f32 v66, v66\n\t" \
"v_exp_f32 v67, v67\n\t" \
"v_mfma_f32_32x32x16_bf16 v[80:95], v[132:135], v[28:31], v[112:127]\n\t" \
"buffer_load_dwordx4 v[132:135], v1, s[28:31], s59 offen sc1\n\t" \
"v_exp_f32 v68, v68\n\t" \
"v_exp_f32 v69, v69\n\t" \
"v_exp_f32 v70, v70\n\t" \
"v_exp_f32 v71, v71\n\t" \
"v_exp_f32 v72, v72\n\t" \
"v_exp_f32 v73, v73\n\t" \
"s_waitcnt vmcnt(11)\n\t" \
"v_smfmac_f32_32x32x32_bf16 v[32:47], v[148:151], v[64:71], v7\n\t" \
"v_exp_f32 v74, v74\n\t" \
"v_exp_f32 v75, v75\n\t" \
"v_exp_f32 v76, v76\n\t" \
"v_exp_f32 v77, v77\n\t" \
"v_exp_f32 v78, v78\n\t" \
"v_exp_f32 v79, v79\n\t" \
"v_exp_f32 v80, v80\n\t" \
"v_exp_f32 v81, v81\n\t" \
"s_waitcnt vmcnt(10)\n\t" \
"v_smfmac_f32_32x32x32_bf16 v[32:47], v[164:167], v[72:79], v7\n\t" \
"v_exp_f32 v82, v82\n\t" \
"v_exp_f32 v83, v83\n\t" \
"s_waitcnt vmcnt(9)\n\t" \
"v_mfma_f32_32x32x16_bf16 v[64:79], v[136:139], v[24:27], v[96:111]\n\t" \
"v_exp_f32 v84, v84\n\t" \
"v_exp_f32 v85, v85\n\t" \
"v_exp_f32 v86, v86\n\t" \
"v_exp_f32 v87, v87\n\t" \
"v_exp_f32 v88, v88\n\t" \
"v_exp_f32 v89, v89\n\t" \
"v_smfmac_f32_32x32x32_bf16 v[48:63], v[148:151], v[80:87], v7\n\t" \
"s_mov_b64 exec, s[94:95]\n\t" \
"buffer_load_dwordx4 v[148:151], v6, s[28:31], s59 offen offset:1024 sc1\n\t" \
"s_mov_b64 exec, -1\n\t" \
"v_exp_f32 v90, v90\n\t" \
"v_exp_f32 v91, v91\n\t" \
"v_exp_f32 v92, v92\n\t" \
"v_exp_f32 v93, v93\n\t" \
"v_exp_f32 v94, v94\n\t" \
"v_exp_f32 v95, v95\n\t" \
"v_exp_f32 v64, v64\n\t" \
"v_exp_f32 v65, v65\n\t" \
"v_smfmac_f32_32x32x32_bf16 v[48:63], v[164:167], v[88:95], v7\n\t" \
"s_mov_b64 exec, s[94:95]\n\t" \
"buffer_load_dwordx4 v[164:167], v6, s[28:31], s59 offen offset:1536 sc1\n\t" \
"s_mov_b64 exec, -1\n\t" \
"s_add_u32 s59, s59, 2048\n\t" \
"v_exp_f32 v66, v66\n\t" \
"v_exp_f32 v67, v67\n\t" \
"v_mfma_f32_32x32x16_bf16 v[80:95], v[136:139], v[28:31], v[112:127]\n\t" \
"buffer_load_dwordx4 v[136:139], v1, s[28:31], s59 offen sc1\n\t" \
"v_exp_f32 v68, v68\n\t" \
"v_exp_f32 v69, v69\n\t" \
"v_exp_f32 v70, v70\n\t" \
"v_exp_f32 v71, v71\n\t" \
"v_exp_f32 v72, v72\n\t" \
"v_exp_f32 v73, v73\n\t" \
"s_waitcnt vmcnt(11)\n\t" \
"v_smfmac_f32_32x32x32_bf16 v[32:47], v[152:155], v[64:71], v7\n\t" \
"v_exp_f32 v74, v74\n\t" \
"v_exp_f32 v75, v75\n\t" \
"v_exp_f32 v76, v76\n\t" \
"v_exp_f32 v77, v77\n\t" \
"v_exp_f32 v78, v78\n\t" \
"v_exp_f32 v79, v79\n\t" \
"v_exp_f32 v80, v80\n\t" \
"v_exp_f32 v81, v81\n\t" \
"s_waitcnt vmcnt(10)\n\t" \
"v_smfmac_f32_32x32x32_bf16 v[32:47], v[168:171], v[72:79], v7\n\t" \
"v_exp_f32 v82, v82\n\t" \
"v_exp_f32 v83, v83\n\t" \
"s_waitcnt vmcnt(9)\n\t" \
"v_mfma_f32_32x32x16_bf16 v[64:79], v[140:143], v[24:27], v[96:111]\n\t" \
"v_exp_f32 v84, v84\n\t" \
"v_exp_f32 v85, v85\n\t" \
"v_exp_f32 v86, v86\n\t" \
"v_exp_f32 v87, v87\n\t" \
"v_exp_f32 v88, v88\n\t" \
"v_exp_f32 v89, v89\n\t" \
"v_smfmac_f32_32x32x32_bf16 v[48:63], v[152:155], v[80:87], v7\n\t" \
"s_mov_b64 exec, s[94:95]\n\t" \
"buffer_load_dwordx4 v[152:155], v6, s[28:31], s59 offen offset:1024 sc1\n\t" \
"s_mov_b64 exec, -1\n\t" \
"v_exp_f32 v90, v90\n\t" \
"v_exp_f32 v91, v91\n\t" \
"v_exp_f32 v92, v92\n\t" \
"v_exp_f32 v93, v93\n\t" \
"v_exp_f32 v94, v94\n\t" \
"v_exp_f32 v95, v95\n\t" \
"v_exp_f32 v64, v64\n\t" \
"v_exp_f32 v65, v65\n\t" \
"v_smfmac_f32_32x32x32_bf16 v[48:63], v[168:171], v[88:95], v7\n\t" \
"s_mov_b64 exec, s[94:95]\n\t" \
"buffer_load_dwordx4 v[168:171], v6, s[28:31], s59 offen offset:1536 sc1\n\t" \
"s_mov_b64 exec, -1\n\t" \
"s_add_u32 s59, s59, 2048\n\t" \
"v_exp_f32 v66, v66\n\t" \
"v_exp_f32 v67, v67\n\t" \
"v_mfma_f32_32x32x16_bf16 v[80:95], v[140:143], v[28:31], v[112:127]\n\t" \
"buffer_load_dwordx4 v[140:143], v1, s[28:31], s59 offen sc1\n\t" \
"v_exp_f32 v68, v68\n\t" \
"v_exp_f32 v69, v69\n\t" \
"v_exp_f32 v70, v70\n\t" \
"s_add_u32 m0, s62, 0\n\t" \
"s_add_u32 s63, s81, 0\n\t" \
"buffer_load_dwordx4 v11, s[84:87], s63 offen lds\n\t" \
"v_exp_f32 v71, v71\n\t" \
"v_exp_f32 v72, v72\n\t" \
"v_exp_f32 v73, v73\n\t" \
"s_waitcnt vmcnt(12)\n\t" \
"v_smfmac_f32_32x32x32_bf16 v[32:47], v[156:159], v[64:71], v7\n\t" \
"v_exp_f32 v74, v74\n\t" \
"v_exp_f32 v75, v75\n\t" \
"v_exp_f32 v76, v76\n\t" \
"v_exp_f32 v77, v77\n\t" \
"v_exp_f32 v78, v78\n\t" \
"v_exp_f32 v79, v79\n\t" \
"v_exp_f32 v80, v80\n\t" \
"v_exp_f32 v81, v81\n\t" \
"s_waitcnt vmcnt(11)\n\t" \
"v_smfmac_f32_32x32x32_bf16 v[32:47], v[172:175], v[72:79], v7\n\t" \
"v_exp_f32 v82, v82\n\t" \
"v_exp_f32 v83, v83\n\t" \
"s_waitcnt vmcnt(10)\n\t" \
"v_mfma_f32_32x32x16_bf16 v[64:79], v[128:131], v[24:27], v[96:111]\n\t" \
"v_exp_f32 v84, v84\n\t" \
"v_exp_f32 v85, v85\n\t" \
"v_exp_f32 v86, v86\n\t" \
"s_add_u32 m0, s62, 1024\n\t" \
"s_add_u32 s63, s81, 8192\n\t" \
"buffer_load_dwordx4 v11, s[84:87], s63 offen lds\n\t" \
"v_exp_f32 v87, v87\n\t" \
"v_exp_f32 v88, v88\n\t" \
"v_exp_f32 v89, v89\n\t" \
"v_smfmac_f32_32x32x32_bf16 v[48:63], v[156:159], v[80:87], v7\n\t" \
"s_mov_b64 exec, s[94:95]\n\t" \
"buffer_load_dwordx4 v[156:159], v6, s[28:31], s59 offen offset:1024 sc1\n\t" \
"s_mov_b64 exec, -1\n\t" \
"v_exp_f32 v90, v90\n\t" \
"v_exp_f32 v91, v91\n\t" \
"v_exp_f32 v92, v92\n\t" \
"v_exp_f32 v93, v93\n\t" \
"v_exp_f32 v94, v94\n\t" \
"v_exp_f32 v95, v95\n\t" \
"v_exp_f32 v64, v64\n\t" \
"v_exp_f32 v65, v65\n\t" \
"v_smfmac_f32_32x32x32_bf16 v[48:63], v[172:175], v[88:95], v7\n\t" \
"s_mov_b64 exec, s[94:95]\n\t" \
"buffer_load_dwordx4 v[172:175], v6, s[28:31], s59 offen offset:1536 sc1\n\t" \
"s_mov_b64 exec, -1\n\t" \
"s_add_u32 s59, s59, 2048\n\t" \
"v_exp_f32 v66, v66\n\t" \
"v_exp_f32 v67, v67\n\t" \
"v_mfma_f32_32x32x16_bf16 v[80:95], v[128:131], v[28:31], v[112:127]\n\t" \
"buffer_load_dwordx4 v[128:131], v1, s[28:31], s59 offen sc1\n\t" \
"v_exp_f32 v68, v68\n\t" \
"v_exp_f32 v69, v69\n\t" \
"v_exp_f32 v70, v70\n\t" \
"s_add_u32 m0, s62, 65536\n\t" \
"s_add_u32 s63, s61, 0\n\t" \
"buffer_load_dwordx4 v13, s[40:43], s63 offen lds\n\t" \
"v_exp_f32 v71, v71\n\t" \
"v_exp_f32 v72, v72\n\t" \
"v_exp_f32 v73, v73\n\t" \
"s_waitcnt vmcnt(14)\n\t" \
"v_smfmac_f32_32x32x32_bf16 v[32:47], v[144:147], v[64:71], v7\n\t" \
"v_exp_f32 v74, v74\n\t" \
"v_exp_f32 v75, v75\n\t" \
"v_exp_f32 v76, v76\n\t" \
"v_exp_f32 v77, v77\n\t" \
"v_exp_f32 v78, v78\n\t" \
"v_exp_f32 v79, v79\n\t" \
"v_exp_f32 v80, v80\n\t" \
"v_exp_f32 v81, v81\n\t" \
"s_waitcnt vmcnt(13)\n\t" \
"v_smfmac_f32_32x32x32_bf16 v[32:47], v[160:163], v[72:79], v7\n\t" \
"v_exp_f32 v82, v82\n\t" \
"v_exp_f32 v83, v83\n\t" \
"s_waitcnt vmcnt(12)\n\t" \
"v_mfma_f32_32x32x16_bf16 v[64:79], v[132:135], v[24:27], v[96:111]\n\t" \
"v_exp_f32 v84, v84\n\t" \
"v_exp_f32 v85, v85\n\t" \
"v_exp_f32 v86, v86\n\t" \
"v_exp_f32 v87, v87\n\t" \
"v_exp_f32 v88, v88\n\t" \
"v_exp_f32 v89, v89\n\t" \
"v_smfmac_f32_32x32x32_bf16 v[48:63], v[144:147], v[80:87], v7\n\t" \
"s_mov_b64 exec, s[94:95]\n\t" \
"buffer_load_dwordx4 v[144:147], v6, s[28:31], s59 offen offset:1024 sc1\n\t" \
"s_mov_b64 exec, -1\n\t" \
"v_exp_f32 v90, v90\n\t" \
"v_exp_f32 v91, v91\n\t" \
"v_exp_f32 v92, v92\n\t" \
"v_exp_f32 v93, v93\n\t" \
"v_exp_f32 v94, v94\n\t" \
"v_exp_f32 v95, v95\n\t" \
"v_exp_f32 v64, v64\n\t" \
"v_exp_f32 v65, v65\n\t" \
"v_smfmac_f32_32x32x32_bf16 v[48:63], v[160:163], v[88:95], v7\n\t" \
"s_mov_b64 exec, s[94:95]\n\t" \
"buffer_load_dwordx4 v[160:163], v6, s[28:31], s59 offen offset:1536 sc1\n\t" \
"s_mov_b64 exec, -1\n\t" \
"s_add_u32 s59, s59, 2048\n\t" \
"v_exp_f32 v66, v66\n\t" \
"v_exp_f32 v67, v67\n\t" \
"v_mfma_f32_32x32x16_bf16 v[80:95], v[132:135], v[28:31], v[112:127]\n\t" \
"buffer_load_dwordx4 v[132:135], v1, s[28:31], s59 offen sc1\n\t" \
"v_exp_f32 v68, v68\n\t" \
"v_exp_f32 v69, v69\n\t" \
"v_exp_f32 v70, v70\n\t" \
"s_add_u32 m0, s62, 2048\n\t" \
"s_add_u32 s63, s81, 16384\n\t" \
"buffer_load_dwordx4 v11, s[84:87], s63 offen lds\n\t" \
"v_exp_f32 v71, v71\n\t" \
"v_exp_f32 v72, v72\n\t" \
"v_exp_f32 v73, v73\n\t" \
"s_waitcnt vmcnt(15)\n\t" \
"v_smfmac_f32_32x32x32_bf16 v[32:47], v[148:151], v[64:71], v7\n\t" \
"v_exp_f32 v74, v74\n\t" \
"v_exp_f32 v75, v75\n\t" \
"v_exp_f32 v76, v76\n\t" \
"v_exp_f32 v77, v77\n\t" \
"v_exp_f32 v78, v78\n\t" \
"v_exp_f32 v79, v79\n\t" \
"v_exp_f32 v80, v80\n\t" \
"v_exp_f32 v81, v81\n\t" \
"s_waitcnt vmcnt(14)\n\t" \
"v_smfmac_f32_32x32x32_bf16 v[32:47], v[164:167], v[72:79], v7\n\t" \
"v_exp_f32 v82, v82\n\t" \
"v_exp_f32 v83, v83\n\t" \
"s_waitcnt vmcnt(13)\n\t" \
"v_mfma_f32_32x32x16_bf16 v[64:79], v[136:139], v[24:27], v[96:111]\n\t" \
"v_exp_f32 v84, v84\n\t" \
"v_exp_f32 v85, v85\n\t" \
"v_exp_f32 v86, v86\n\t" \
"s_add_u32 m0, s62, 3072\n\t" \
"s_add_u32 s63, s81, 24576\n\t" \
"buffer_load_dwordx4 v11, s[84:87], s63 offen lds\n\t" \
"v_exp_f32 v87, v87\n\t" \
"v_exp_f32 v88, v88\n\t" \
"v_exp_f32 v89, v89\n\t" \
"v_smfmac_f32_32x32x32_bf16 v[48:63], v[148:151], v[80:87], v7\n\t" \
"s_mov_b64 exec, s[94:95]\n\t" \
"buffer_load_dwordx4 v[148:151], v6, s[28:31], s59 offen offset:1024 sc1\n\t" \
"s_mov_b64 exec, -1\n\t" \
"v_exp_f32 v90, v90\n\t" \
"v_exp_f32 v91, v91\n\t" \
"v_exp_f32 v92, v92\n\t" \
"v_exp_f32 v93, v93\n\t" \
"v_exp_f32 v94, v94\n\t" \
"v_exp_f32 v95, v95\n\t" \
"v_exp_f32 v64, v64\n\t" \
"v_exp_f32 v65, v65\n\t" \
"v_smfmac_f32_32x32x32_bf16 v[48:63], v[164:167], v[88:95], v7\n\t" \
"s_mov_b64 exec, s[94:95]\n\t" \
"buffer_load_dwordx4 v[164:167], v6, s[28:31], s59 offen offset:1536 sc1\n\t" \
"s_mov_b64 exec, -1\n\t" \
"s_add_u32 s59, s59, 2048\n\t" \
"v_exp_f32 v66, v66\n\t" \
"v_exp_f32 v67, v67\n\t" \
"v_mfma_f32_32x32x16_bf16 v[80:95], v[136:139], v[28:31], v[112:127]\n\t" \
"buffer_load_dwordx4 v[136:139], v1, s[28:31], s59 offen sc1\n\t" \
"v_exp_f32 v68, v68\n\t" \
"v_exp_f32 v69, v69\n\t" \
"v_exp_f32 v70, v70\n\t" \
"v_exp_f32 v71, v71\n\t" \
"v_exp_f32 v72, v72\n\t" \
"v_exp_f32 v73, v73\n\t" \
"s_waitcnt vmcnt(16)\n\t" \
"v_smfmac_f32_32x32x32_bf16 v[32:47], v[152:155], v[64:71], v7\n\t" \
"v_exp_f32 v74, v74\n\t" \
"v_exp_f32 v75, v75\n\t" \
"v_exp_f32 v76, v76\n\t" \
"v_exp_f32 v77, v77\n\t" \
"v_exp_f32 v78, v78\n\t" \
"v_exp_f32 v79, v79\n\t" \
"v_exp_f32 v80, v80\n\t" \
"v_exp_f32 v81, v81\n\t" \
"s_waitcnt vmcnt(15)\n\t" \
"v_smfmac_f32_32x32x32_bf16 v[32:47], v[168:171], v[72:79], v7\n\t" \
"v_exp_f32 v82, v82\n\t" \
"v_exp_f32 v83, v83\n\t" \
"s_waitcnt vmcnt(14)\n\t" \
"v_mfma_f32_32x32x16_bf16 v[64:79], v[140:143], v[24:27], v[96:111]\n\t" \
"v_exp_f32 v84, v84\n\t" \
"v_exp_f32 v85, v85\n\t" \
"v_exp_f32 v86, v86\n\t" \
"s_add_u32 m0, s62, 66560\n\t" \
"s_add_u32 s63, s61, 28672\n\t" \
"buffer_load_dwordx4 v13, s[40:43], s63 offen lds\n\t" \
"v_exp_f32 v87, v87\n\t" \
"v_exp_f32 v88, v88\n\t" \
"v_exp_f32 v89, v89\n\t" \
"v_smfmac_f32_32x32x32_bf16 v[48:63], v[152:155], v[80:87], v7\n\t" \
"s_mov_b64 exec, s[94:95]\n\t" \
"buffer_load_dwordx4 v[152:155], v6, s[28:31], s59 offen offset:1024 sc1\n\t" \
"s_mov_b64 exec, -1\n\t" \
"v_exp_f32 v90, v90\n\t" \
"v_exp_f32 v91, v91\n\t" \
"v_exp_f32 v92, v92\n\t" \
"v_exp_f32 v93, v93\n\t" \
"v_exp_f32 v94, v94\n\t" \
"v_exp_f32 v95, v95\n\t" \
"v_exp_f32 v64, v64\n\t" \
"v_exp_f32 v65, v65\n\t" \
"v_smfmac_f32_32x32x32_bf16 v[48:63], v[168:171], v[88:95], v7\n\t" \
"s_mov_b64 exec, s[94:95]\n\t" \
"buffer_load_dwordx4 v[168:171], v6, s[28:31], s59 offen offset:1536 sc1\n\t" \
"s_mov_b64 exec, -1\n\t" \
"s_add_u32 s59, s59, 2048\n\t" \
"v_exp_f32 v66, v66\n\t" \
"v_exp_f32 v67, v67\n\t" \
"v_mfma_f32_32x32x16_bf16 v[80:95], v[140:143], v[28:31], v[112:127]\n\t" \
"buffer_load_dwordx4 v[140:143], v1, s[28:31], s59 offen sc1\n\t" \
"v_exp_f32 v68, v68\n\t" \
"v_exp_f32 v69, v69\n\t" \
"v_exp_f32 v70, v70\n\t" \
"s_add_u32 m0, s62, 16384\n\t" \
"s_add_u32 s63, s81, 131072\n\t" \
"buffer_load_dwordx4 v11, s[84:87], s63 offen lds\n\t" \
"v_exp_f32 v71, v71\n\t" \
"v_exp_f32 v72, v72\n\t" \
"v_exp_f32 v73, v73\n\t" \
"s_waitcnt vmcnt(16)\n\t" \
"v_smfmac_f32_32x32x32_bf16 v[32:47], v[156:159], v[64:71], v7\n\t" \
"v_exp_f32 v74, v74\n\t" \
"v_exp_f32 v75, v75\n\t" \
"v_exp_f32 v76, v76\n\t" \
"v_exp_f32 v77, v77\n\t" \
"v_exp_f32 v78, v78\n\t" \
"v_exp_f32 v79, v79\n\t" \
"v_exp_f32 v80, v80\n\t" \
"v_exp_f32 v81, v81\n\t" \
"s_waitcnt vmcnt(15)\n\t" \
"v_smfmac_f32_32x32x32_bf16 v[32:47], v[172:175], v[72:79], v7\n\t" \
"v_exp_f32 v82, v82\n\t" \
"v_exp_f32 v83, v83\n\t" \
"s_waitcnt vmcnt(14)\n\t" \
"v_mfma_f32_32x32x16_bf16 v[64:79], v[128:131], v[24:27], v[96:111]\n\t" \
"v_exp_f32 v84, v84\n\t" \
"v_exp_f32 v85, v85\n\t" \
"v_exp_f32 v86, v86\n\t" \
"v_exp_f32 v87, v87\n\t" \
"v_exp_f32 v88, v88\n\t" \
"v_exp_f32 v89, v89\n\t" \
"v_smfmac_f32_32x32x32_bf16 v[48:63], v[156:159], v[80:87], v7\n\t" \
"s_mov_b64 exec, s[94:95]\n\t" \
"buffer_load_dwordx4 v[156:159], v6, s[28:31], s59 offen offset:1024 sc1\n\t" \
"s_mov_b64 exec, -1\n\t" \
"v_exp_f32 v90, v90\n\t" \
"v_exp_f32 v91, v91\n\t" \
"v_exp_f32 v92, v92\n\t" \
"v_exp_f32 v93, v93\n\t" \
"v_exp_f32 v94, v94\n\t" \
"v_exp_f32 v95, v95\n\t" \
"v_exp_f32 v64, v64\n\t" \
"v_exp_f32 v65, v65\n\t" \
"v_smfmac_f32_32x32x32_bf16 v[48:63], v[172:175], v[88:95], v7\n\t" \
"s_mov_b64 exec, s[94:95]\n\t" \
"buffer_load_dwordx4 v[172:175], v6, s[28:31], s59 offen offset:1536 sc1\n\t" \
"s_mov_b64 exec, -1\n\t" \
"s_add_u32 s59, s59, 2048\n\t" \
"v_exp_f32 v66, v66\n\t" \
"v_exp_f32 v67, v67\n\t" \
"v_mfma_f32_32x32x16_bf16 v[80:95], v[128:131], v[28:31], v[112:127]\n\t" \
"buffer_load_dwordx4 v[128:131], v1, s[28:31], s59 offen sc1\n\t" \
"v_exp_f32 v68, v68\n\t" \
"v_exp_f32 v69, v69\n\t" \
"v_exp_f32 v70, v70\n\t" \
"s_add_u32 m0, s62, 17408\n\t" \
"s_add_u32 s63, s81, 139264\n\t" \
"buffer_load_dwordx4 v11, s[84:87], s63 offen lds\n\t" \
"v_exp_f32 v71, v71\n\t" \
"v_exp_f32 v72, v72\n\t" \
"v_exp_f32 v73, v73\n\t" \
"s_waitcnt vmcnt(16)\n\t" \
"v_smfmac_f32_32x32x32_bf16 v[32:47], v[144:147], v[64:71], v7\n\t" \
"v_exp_f32 v74, v74\n\t" \
"v_exp_f32 v75, v75\n\t" \
"v_exp_f32 v76, v76\n\t" \
"v_exp_f32 v77, v77\n\t" \
"v_exp_f32 v78, v78\n\t" \
"v_exp_f32 v79, v79\n\t" \
"v_exp_f32 v80, v80\n\t" \
"v_exp_f32 v81, v81\n\t" \
"s_waitcnt vmcnt(15)\n\t" \
"v_smfmac_f32_32x32x32_bf16 v[32:47], v[160:163], v[72:79], v7\n\t" \
"v_exp_f32 v82, v82\n\t" \
"v_exp_f32 v83, v83\n\t" \
"s_waitcnt vmcnt(14)\n\t" \
"v_mfma_f32_32x32x16_bf16 v[64:79], v[132:135], v[24:27], v[96:111]\n\t" \
"v_exp_f32 v84, v84\n\t" \
"v_exp_f32 v85, v85\n\t" \
"v_exp_f32 v86, v86\n\t" \
"s_add_u32 m0, s62, 18432\n\t" \
"s_add_u32 s63, s81, 147456\n\t" \
"buffer_load_dwordx4 v11, s[84:87], s63 offen lds\n\t" \
"v_exp_f32 v87, v87\n\t" \
"v_exp_f32 v88, v88\n\t" \
"v_exp_f32 v89, v89\n\t" \
"v_smfmac_f32_32x32x32_bf16 v[48:63], v[144:147], v[80:87], v7\n\t" \
"s_mov_b64 exec, s[94:95]\n\t" \
"buffer_load_dwordx4 v[144:147], v6, s[28:31], s59 offen offset:1024 sc1\n\t" \
"s_mov_b64 exec, -1\n\t" \
"v_exp_f32 v90, v90\n\t" \
"v_exp_f32 v91, v91\n\t" \
"v_exp_f32 v92, v92\n\t" \
"v_exp_f32 v93, v93\n\t" \
"v_exp_f32 v94, v94\n\t" \
"v_exp_f32 v95, v95\n\t" \
"v_exp_f32 v64, v64\n\t" \
"v_exp_f32 v65, v65\n\t" \
"v_smfmac_f32_32x32x32_bf16 v[48:63], v[160:163], v[88:95], v7\n\t" \
"s_mov_b64 exec, s[94:95]\n\t" \
"buffer_load_dwordx4 v[160:163], v6, s[28:31], s59 offen offset:1536 sc1\n\t" \
"s_mov_b64 exec, -1\n\t" \
"s_add_u32 s59, s59, 2048\n\t" \
"v_exp_f32 v66, v66\n\t" \
"v_exp_f32 v67, v67\n\t" \
"v_mfma_f32_32x32x16_bf16 v[80:95], v[132:135], v[28:31], v[112:127]\n\t" \
"buffer_load_dwordx4 v[132:135], v1, s[28:31], s59 offen sc1\n\t" \
"v_exp_f32 v68, v68\n\t" \
"v_exp_f32 v69, v69\n\t" \
"v_exp_f32 v70, v70\n\t" \
"s_add_u32 m0, s62, 67584\n\t" \
"s_add_u32 s63, s61, 57344\n\t" \
"buffer_load_dwordx4 v13, s[40:43], s63 offen lds\n\t" \
"v_exp_f32 v71, v71\n\t" \
"v_exp_f32 v72, v72\n\t" \
"v_exp_f32 v73, v73\n\t" \
"s_waitcnt vmcnt(16)\n\t" \
"v_smfmac_f32_32x32x32_bf16 v[32:47], v[148:151], v[64:71], v7\n\t" \
"buffer_load_dwordx2 v[16:17], v19, s[28:31], s88 offen sc1\n\t" \
"v_exp_f32 v74, v74\n\t" \
"v_exp_f32 v75, v75\n\t" \
"v_exp_f32 v76, v76\n\t" \
"v_exp_f32 v77, v77\n\t" \
"v_exp_f32 v78, v78\n\t" \
"v_exp_f32 v79, v79\n\t" \
"v_exp_f32 v80, v80\n\t" \
"v_exp_f32 v81, v81\n\t" \
"s_waitcnt vmcnt(16)\n\t" \
"v_smfmac_f32_32x32x32_bf16 v[32:47], v[164:167], v[72:79], v7\n\t" \
"v_exp_f32 v82, v82\n\t" \
"v_exp_f32 v83, v83\n\t" \
"s_waitcnt vmcnt(15)\n\t" \
"v_mfma_f32_32x32x16_bf16 v[64:79], v[136:139], v[24:27], v[96:111]\n\t" \
"v_exp_f32 v84, v84\n\t" \
"v_exp_f32 v85, v85\n\t" \
"v_exp_f32 v86, v86\n\t" \
"v_exp_f32 v87, v87\n\t" \
"v_exp_f32 v88, v88\n\t" \
"v_exp_f32 v89, v89\n\t" \
"v_smfmac_f32_32x32x32_bf16 v[48:63], v[148:151], v[80:87], v7\n\t" \
"s_mov_b64 exec, s[94:95]\n\t" \
"buffer_load_dwordx4 v[148:151], v6, s[28:31], s59 offen offset:1024 sc1\n\t" \
"s_mov_b64 exec, -1\n\t" \
"v_exp_f32 v90, v90\n\t" \
"v_exp_f32 v91, v91\n\t" \
"v_exp_f32 v92, v92\n\t" \
"v_exp_f32 v93, v93\n\t" \
"v_exp_f32 v94, v94\n\t" \
"v_exp_f32 v95, v95\n\t" \
"v_exp_f32 v64, v64\n\t" \
"v_exp_f32 v65, v65\n\t" \
"v_smfmac_f32_32x32x32_bf16 v[48:63], v[164:167], v[88:95], v7\n\t" \
"s_mov_b64 exec, s[94:95]\n\t" \
"buffer_load_dwordx4 v[164:167], v6, s[28:31], s59 offen offset:1536 sc1\n\t" \
"s_mov_b64 exec, -1\n\t" \
"s_add_u32 s59, s59, 2048\n\t" \
"v_exp_f32 v66, v66\n\t" \
"v_exp_f32 v67, v67\n\t" \
"v_mfma_f32_32x32x16_bf16 v[80:95], v[136:139], v[28:31], v[112:127]\n\t" \
"buffer_load_dwordx4 v[136:139], v1, s[28:31], s59 offen sc1\n\t" \
"v_exp_f32 v68, v68\n\t" \
"v_exp_f32 v69, v69\n\t" \
"v_exp_f32 v70, v70\n\t" \
"s_add_u32 m0, s62, 19456\n\t" \
"s_add_u32 s63, s81, 155648\n\t" \
"buffer_load_dwordx4 v11, s[84:87], s63 offen lds\n\t" \
"v_exp_f32 v71, v71\n\t" \
"v_exp_f32 v72, v72\n\t" \
"v_exp_f32 v73, v73\n\t" \
"s_waitcnt vmcnt(17)\n\t" \
"v_smfmac_f32_32x32x32_bf16 v[32:47], v[152:155], v[64:71], v7\n\t" \
"v_exp_f32 v74, v74\n\t" \
"v_exp_f32 v75, v75\n\t" \
"v_exp_f32 v76, v76\n\t" \
"v_exp_f32 v77, v77\n\t" \
"v_exp_f32 v78, v78\n\t" \
"v_exp_f32 v79, v79\n\t" \
"v_exp_f32 v80, v80\n\t" \
"v_exp_f32 v81, v81\n\t" \
"s_waitcnt vmcnt(16)\n\t" \
"v_smfmac_f32_32x32x32_bf16 v[32:47], v[168:171], v[72:79], v7\n\t" \
"v_exp_f32 v82, v82\n\t" \
"v_exp_f32 v83, v83\n\t" \
"s_waitcnt vmcnt(15)\n\t" \
"v_mfma_f32_32x32x16_bf16 v[64:79], v[140:143], v[24:27], v[96:111]\n\t" \
"v_exp_f32 v84, v84\n\t" \
"v_exp_f32 v85, v85\n\t" \
"v_exp_f32 v86, v86\n\t" \
"s_add_u32 m0, s62, 32768\n\t" \
"s_add_u32 s63, s81, 262144\n\t" \
"buffer_load_dwordx4 v11, s[84:87], s63 offen lds\n\t" \
"v_exp_f32 v87, v87\n\t" \
"v_exp_f32 v88, v88\n\t" \
"v_exp_f32 v89, v89\n\t" \
"v_smfmac_f32_32x32x32_bf16 v[48:63], v[152:155], v[80:87], v7\n\t" \
"s_mov_b64 exec, s[94:95]\n\t" \
"buffer_load_dwordx4 v[152:155], v6, s[28:31], s59 offen offset:1024 sc1\n\t" \
"s_mov_b64 exec, -1\n\t" \
"v_exp_f32 v90, v90\n\t" \
"v_exp_f32 v91, v91\n\t" \
"v_exp_f32 v92, v92\n\t" \
"v_exp_f32 v93, v93\n\t" \
"v_exp_f32 v94, v94\n\t" \
"v_exp_f32 v95, v95\n\t" \
"v_exp_f32 v64, v64\n\t" \
"v_exp_f32 v65, v65\n\t" \
"v_smfmac_f32_32x32x32_bf16 v[48:63], v[168:171], v[88:95], v7\n\t" \
"s_mov_b64 exec, s[94:95]\n\t" \
"buffer_load_dwordx4 v[168:171], v6, s[28:31], s59 offen offset:1536 sc1\n\t" \
"s_mov_b64 exec, -1\n\t" \
"s_add_u32 s59, s59, 2048\n\t" \
"v_exp_f32 v66, v66\n\t" \
"v_exp_f32 v67, v67\n\t" \
"v_mfma_f32_32x32x16_bf16 v[80:95], v[140:143], v[28:31], v[112:127]\n\t" \
"buffer_load_dwordx4 v[140:143], v1, s[28:31], s59 offen sc1\n\t" \
"v_exp_f32 v68, v68\n\t" \
"v_exp_f32 v69, v69\n\t" \
"v_exp_f32 v70, v70\n\t" \
"v_exp_f32 v71, v71\n\t" \
"v_exp_f32 v72, v72\n\t" \
"v_exp_f32 v73, v73\n\t" \
"s_waitcnt vmcnt(17)\n\t" \
"v_smfmac_f32_32x32x32_bf16 v[32:47], v[156:159], v[64:71], v7\n\t" \
"v_exp_f32 v74, v74\n\t" \
"v_exp_f32 v75, v75\n\t" \
"v_exp_f32 v76, v76\n\t" \
"v_exp_f32 v77, v77\n\t" \
"v_exp_f32 v78, v78\n\t" \
"v_exp_f32 v79, v79\n\t" \
"v_exp_f32 v80, v80\n\t" \
"v_exp_f32 v81, v81\n\t" \
"s_waitcnt vmcnt(16)\n\t" \
"v_smfmac_f32_32x32x32_bf16 v[32:47], v[172:175], v[72:79], v7\n\t" \
"v_exp_f32 v82, v82\n\t" \
"v_exp_f32 v83, v83\n\t" \
"s_waitcnt vmcnt(15)\n\t" \
"v_mfma_f32_32x32x16_bf16 v[64:79], v[128:131], v[24:27], v[96:111]\n\t" \
"v_exp_f32 v84, v84\n\t" \
"v_exp_f32 v85, v85\n\t" \
"v_exp_f32 v86, v86\n\t" \
"s_add_u32 m0, s62, 33792\n\t" \
"s_add_u32 s63, s81, 270336\n\t" \
"buffer_load_dwordx4 v11, s[84:87], s63 offen lds\n\t" \
"v_exp_f32 v87, v87\n\t" \
"v_exp_f32 v88, v88\n\t" \
"v_exp_f32 v89, v89\n\t" \
"v_smfmac_f32_32x32x32_bf16 v[48:63], v[156:159], v[80:87], v7\n\t" \
"s_mov_b64 exec, s[94:95]\n\t" \
"buffer_load_dwordx4 v[156:159], v6, s[28:31], s59 offen offset:1024 sc1\n\t" \
"s_mov_b64 exec, -1\n\t" \
"v_exp_f32 v90, v90\n\t" \
"v_exp_f32 v91, v91\n\t" \
"v_exp_f32 v92, v92\n\t" \
"v_exp_f32 v93, v93\n\t" \
"v_exp_f32 v94, v94\n\t" \
"v_exp_f32 v95, v95\n\t" \
"v_exp_f32 v64, v64\n\t" \
"v_exp_f32 v65, v65\n\t" \
"v_smfmac_f32_32x32x32_bf16 v[48:63], v[172:175], v[88:95], v7\n\t" \
"s_mov_b64 exec, s[94:95]\n\t" \
"buffer_load_dwordx4 v[172:175], v6, s[28:31], s59 offen offset:1536 sc1\n\t" \
"s_mov_b64 exec, -1\n\t" \
"s_add_u32 s59, s59, 2048\n\t" \
"v_exp_f32 v66, v66\n\t" \
"v_exp_f32 v67, v67\n\t" \
"v_mfma_f32_32x32x16_bf16 v[80:95], v[128:131], v[28:31], v[112:127]\n\t" \
"buffer_load_dwordx4 v[128:131], v1, s[28:31], s59 offen sc1\n\t" \
"v_exp_f32 v68, v68\n\t" \
"v_exp_f32 v69, v69\n\t" \
"v_exp_f32 v70, v70\n\t" \
"s_add_u32 m0, s62, 68608\n\t" \
"s_add_u32 s63, s61, 86016\n\t" \
"buffer_load_dwordx4 v13, s[40:43], s63 offen lds\n\t" \
"v_exp_f32 v71, v71\n\t" \
"v_exp_f32 v72, v72\n\t" \
"v_exp_f32 v73, v73\n\t" \
"s_waitcnt vmcnt(17)\n\t" \
"v_smfmac_f32_32x32x32_bf16 v[32:47], v[144:147], v[64:71], v7\n\t" \
"v_exp_f32 v74, v74\n\t" \
"v_exp_f32 v75, v75\n\t" \
"v_exp_f32 v76, v76\n\t" \
"v_exp_f32 v77, v77\n\t" \
"v_exp_f32 v78, v78\n\t" \
"v_exp_f32 v79, v79\n\t" \
"v_exp_f32 v80, v80\n\t" \
"v_exp_f32 v81, v81\n\t" \
"s_waitcnt vmcnt(16)\n\t" \
"v_smfmac_f32_32x32x32_bf16 v[32:47], v[160:163], v[72:79], v7\n\t" \
"v_exp_f32 v82, v82\n\t" \
"v_exp_f32 v83, v83\n\t" \
"s_waitcnt vmcnt(15)\n\t" \
"v_mfma_f32_32x32x16_bf16 v[64:79], v[132:135], v[24:27], v[96:111]\n\t" \
"v_exp_f32 v84, v84\n\t" \
"v_exp_f32 v85, v85\n\t" \
"v_exp_f32 v86, v86\n\t" \
"v_exp_f32 v87, v87\n\t" \
"v_exp_f32 v88, v88\n\t" \
"v_exp_f32 v89, v89\n\t" \
"v_smfmac_f32_32x32x32_bf16 v[48:63], v[144:147], v[80:87], v7\n\t" \
"s_mov_b64 exec, s[94:95]\n\t" \
"buffer_load_dwordx4 v[144:147], v6, s[28:31], s59 offen offset:1024 sc1\n\t" \
"s_mov_b64 exec, -1\n\t" \
"v_exp_f32 v90, v90\n\t" \
"v_exp_f32 v91, v91\n\t" \
"v_exp_f32 v92, v92\n\t" \
"v_exp_f32 v93, v93\n\t" \
"v_exp_f32 v94, v94\n\t" \
"v_exp_f32 v95, v95\n\t" \
"v_exp_f32 v64, v64\n\t" \
"v_exp_f32 v65, v65\n\t" \
"v_smfmac_f32_32x32x32_bf16 v[48:63], v[160:163], v[88:95], v7\n\t" \
"s_mov_b64 exec, s[94:95]\n\t" \
"buffer_load_dwordx4 v[160:163], v6, s[28:31], s59 offen offset:1536 sc1\n\t" \
"s_mov_b64 exec, -1\n\t" \
"s_add_u32 s59, s59, 2048\n\t" \
"v_exp_f32 v66, v66\n\t" \
"v_exp_f32 v67, v67\n\t" \
"v_mfma_f32_32x32x16_bf16 v[80:95], v[132:135], v[28:31], v[112:127]\n\t" \
"s_waitcnt vmcnt(15)\n\t" \
"v_cmp_ne_u64 vcc, s[44:45], v[16:17]\n\t" \
"s_mov_b32 s59, s76\n\t" \
"s_cmp_lg_u64 vcc, 0\n\t" \
"s_cbranch_scc1 Lspinf_%=\n\t" \
"Lspunf_%=:\n\t" \
"buffer_load_dwordx4 v[132:135], v1, s[28:31], s59 offen sc1\n\t" \
"v_exp_f32 v68, v68\n\t" \
"v_exp_f32 v69, v69\n\t" \
"v_exp_f32 v70, v70\n\t" \
"s_add_u32 m0, s62, 34816\n\t" \
"s_add_u32 s63, s81, 278528\n\t" \
"buffer_load_dwordx4 v11, s[84:87], s63 offen lds\n\t" \
"v_exp_f32 v71, v71\n\t" \
"v_exp_f32 v72, v72\n\t" \
"v_exp_f32 v73, v73\n\t" \
"s_waitcnt vmcnt(16)\n\t" \
"v_smfmac_f32_32x32x32_bf16 v[32:47], v[148:151], v[64:71], v7\n\t" \
"v_exp_f32 v74, v74\n\t" \
"v_exp_f32 v75, v75\n\t" \
"v_exp_f32 v76, v76\n\t" \
"v_exp_f32 v77, v77\n\t" \
"v_exp_f32 v78, v78\n\t" \
"v_exp_f32 v79, v79\n\t" \
"v_exp_f32 v80, v80\n\t" \
"v_exp_f32 v81, v81\n\t" \
"s_waitcnt vmcnt(15)\n\t" \
"v_smfmac_f32_32x32x32_bf16 v[32:47], v[164:167], v[72:79], v7\n\t" \
"v_exp_f32 v82, v82\n\t" \
"v_exp_f32 v83, v83\n\t" \
"s_waitcnt vmcnt(14)\n\t" \
"v_mfma_f32_32x32x16_bf16 v[64:79], v[136:139], v[24:27], v[96:111]\n\t" \
"v_exp_f32 v84, v84\n\t" \
"v_exp_f32 v85, v85\n\t" \
"v_exp_f32 v86, v86\n\t" \
"s_add_u32 m0, s62, 35840\n\t" \
"s_add_u32 s63, s81, 286720\n\t" \
"buffer_load_dwordx4 v11, s[84:87], s63 offen lds\n\t" \
"v_exp_f32 v87, v87\n\t" \
"v_exp_f32 v88, v88\n\t" \
"v_exp_f32 v89, v89\n\t" \
"v_smfmac_f32_32x32x32_bf16 v[48:63], v[148:151], v[80:87], v7\n\t" \
"s_mov_b64 exec, s[94:95]\n\t" \
"buffer_load_dwordx4 v[148:151], v6, s[28:31], s59 offen offset:1024 sc1\n\t" \
"s_mov_b64 exec, -1\n\t" \
"v_exp_f32 v90, v90\n\t" \
"v_exp_f32 v91, v91\n\t" \
"v_exp_f32 v92, v92\n\t" \
"v_exp_f32 v93, v93\n\t" \
"v_exp_f32 v94, v94\n\t" \
"v_exp_f32 v95, v95\n\t" \
"v_exp_f32 v64, v64\n\t" \
"v_exp_f32 v65, v65\n\t" \
"v_smfmac_f32_32x32x32_bf16 v[48:63], v[164:167], v[88:95], v7\n\t" \
"s_mov_b64 exec, s[94:95]\n\t" \
"buffer_load_dwordx4 v[164:167], v6, s[28:31], s59 offen offset:1536 sc1\n\t" \
"s_mov_b64 exec, -1\n\t" \
"s_add_u32 s59, s59, 2048\n\t" \
"v_exp_f32 v66, v66\n\t" \
"v_exp_f32 v67, v67\n\t" \
"v_mfma_f32_32x32x16_bf16 v[80:95], v[136:139], v[28:31], v[112:127]\n\t" \
"buffer_load_dwordx4 v[136:139], v1, s[28:31], s59 offen sc1\n\t" \
"v_exp_f32 v68, v68\n\t" \
"v_exp_f32 v69, v69\n\t" \
"v_exp_f32 v70, v70\n\t" \
"s_add_u32 m0, s62, 49152\n\t" \
"s_add_u32 s63, s81, 393216\n\t" \
"buffer_load_dwordx4 v11, s[84:87], s63 offen lds\n\t" \
"v_exp_f32 v71, v71\n\t" \
"v_exp_f32 v72, v72\n\t" \
"v_exp_f32 v73, v73\n\t" \
"s_waitcnt vmcnt(16)\n\t" \
"v_smfmac_f32_32x32x32_bf16 v[32:47], v[152:155], v[64:71], v7\n\t" \
"v_exp_f32 v74, v74\n\t" \
"v_exp_f32 v75, v75\n\t" \
"v_exp_f32 v76, v76\n\t" \
"v_exp_f32 v77, v77\n\t" \
"v_exp_f32 v78, v78\n\t" \
"v_exp_f32 v79, v79\n\t" \
"v_exp_f32 v80, v80\n\t" \
"v_exp_f32 v81, v81\n\t" \
"s_waitcnt vmcnt(15)\n\t" \
"v_smfmac_f32_32x32x32_bf16 v[32:47], v[168:171], v[72:79], v7\n\t" \
"v_exp_f32 v82, v82\n\t" \
"v_exp_f32 v83, v83\n\t" \
"s_waitcnt vmcnt(14)\n\t" \
"v_mfma_f32_32x32x16_bf16 v[64:79], v[140:143], v[24:27], v[96:111]\n\t" \
"v_exp_f32 v84, v84\n\t" \
"v_exp_f32 v85, v85\n\t" \
"v_exp_f32 v86, v86\n\t" \
"v_exp_f32 v87, v87\n\t" \
"v_exp_f32 v88, v88\n\t" \
"v_exp_f32 v89, v89\n\t" \
"v_smfmac_f32_32x32x32_bf16 v[48:63], v[152:155], v[80:87], v7\n\t" \
"s_mov_b64 exec, s[94:95]\n\t" \
"buffer_load_dwordx4 v[152:155], v6, s[28:31], s59 offen offset:1024 sc1\n\t" \
"s_mov_b64 exec, -1\n\t" \
"v_exp_f32 v90, v90\n\t" \
"v_exp_f32 v91, v91\n\t" \
"v_exp_f32 v92, v92\n\t" \
"v_exp_f32 v93, v93\n\t" \
"v_exp_f32 v94, v94\n\t" \
"v_exp_f32 v95, v95\n\t" \
"v_exp_f32 v64, v64\n\t" \
"v_exp_f32 v65, v65\n\t" \
"v_smfmac_f32_32x32x32_bf16 v[48:63], v[168:171], v[88:95], v7\n\t" \
"s_mov_b64 exec, s[94:95]\n\t" \
"buffer_load_dwordx4 v[168:171], v6, s[28:31], s59 offen offset:1536 sc1\n\t" \
"s_mov_b64 exec, -1\n\t" \
"s_add_u32 s59, s59, 2048\n\t" \
"v_exp_f32 v66, v66\n\t" \
"v_exp_f32 v67, v67\n\t" \
"v_mfma_f32_32x32x16_bf16 v[80:95], v[140:143], v[28:31], v[112:127]\n\t" \
"buffer_load_dwordx4 v[140:143], v1, s[28:31], s59 offen sc1\n\t" \
"v_exp_f32 v68, v68\n\t" \
"v_exp_f32 v69, v69\n\t" \
"v_exp_f32 v70, v70\n\t" \
"s_add_u32 m0, s62, 81920\n\t" \
"s_add_u32 s63, s61, 458752\n\t" \
"buffer_load_dwordx4 v13, s[40:43], s63 offen lds\n\t" \
"v_exp_f32 v71, v71\n\t" \
"v_exp_f32 v72, v72\n\t" \
"v_exp_f32 v73, v73\n\t" \
"s_waitcnt vmcnt(16)\n\t" \
"v_smfmac_f32_32x32x32_bf16 v[32:47], v[156:159], v[64:71], v7\n\t" \
"v_exp_f32 v74, v74\n\t" \
"v_exp_f32 v75, v75\n\t" \
"v_exp_f32 v76, v76\n\t" \
"v_exp_f32 v77, v77\n\t" \
"v_exp_f32 v78, v78\n\t" \
"v_exp_f32 v79, v79\n\t" \
"v_exp_f32 v80, v80\n\t" \
"v_exp_f32 v81, v81\n\t" \
"s_waitcnt vmcnt(15)\n\t" \
"v_smfmac_f32_32x32x32_bf16 v[32:47], v[172:175], v[72:79], v7\n\t" \
"v_exp_f32 v82, v82\n\t" \
"v_exp_f32 v83, v83\n\t" \
"s_waitcnt vmcnt(14)\n\t" \
"v_mfma_f32_32x32x16_bf16 v[64:79], v[128:131], v[24:27], v[96:111]\n\t" \
"v_exp_f32 v84, v84\n\t" \
"v_exp_f32 v85, v85\n\t" \
"v_exp_f32 v86, v86\n\t" \
"s_add_u32 m0, s62, 50176\n\t" \
"s_add_u32 s63, s81, 401408\n\t" \
"buffer_load_dwordx4 v11, s[84:87], s63 offen lds\n\t" \
"v_exp_f32 v87, v87\n\t" \
"v_exp_f32 v88, v88\n\t" \
"v_exp_f32 v89, v89\n\t" \
"v_smfmac_f32_32x32x32_bf16 v[48:63], v[156:159], v[80:87], v7\n\t" \
"s_mov_b64 exec, s[94:95]\n\t" \
"buffer_load_dwordx4 v[156:159], v6, s[28:31], s59 offen offset:1024 sc1\n\t" \
"s_mov_b64 exec, -1\n\t" \
"v_exp_f32 v90, v90\n\t" \
"v_exp_f32 v91, v91\n\t" \
"v_exp_f32 v92, v92\n\t" \
"v_exp_f32 v93, v93\n\t" \
"v_exp_f32 v94, v94\n\t" \
"v_exp_f32 v95, v95\n\t" \
"v_exp_f32 v64, v64\n\t" \
"v_exp_f32 v65, v65\n\t" \
"v_smfmac_f32_32x32x32_bf16 v[48:63], v[172:175], v[88:95], v7\n\t" \
"s_mov_b64 exec, s[94:95]\n\t" \
"buffer_load_dwordx4 v[172:175], v6, s[28:31], s59 offen offset:1536 sc1\n\t" \
"s_mov_b64 exec, -1\n\t" \
"s_add_u32 s59, s59, 2048\n\t" \
"v_exp_f32 v66, v66\n\t" \
"v_exp_f32 v67, v67\n\t" \
"v_mfma_f32_32x32x16_bf16 v[80:95], v[128:131], v[28:31], v[112:127]\n\t" \
"buffer_load_dwordx4 v[128:131], v1, s[28:31], s59 offen sc1\n\t" \
"v_exp_f32 v68, v68\n\t" \
"v_exp_f32 v69, v69\n\t" \
"v_exp_f32 v70, v70\n\t" \
"v_exp_f32 v71, v71\n\t" \
"v_exp_f32 v72, v72\n\t" \
"v_exp_f32 v73, v73\n\t" \
"s_waitcnt vmcnt(16)\n\t" \
"v_smfmac_f32_32x32x32_bf16 v[32:47], v[144:147], v[64:71], v7\n\t" \
"v_exp_f32 v74, v74\n\t" \
"v_exp_f32 v75, v75\n\t" \
"v_exp_f32 v76, v76\n\t" \
"v_exp_f32 v77, v77\n\t" \
"v_exp_f32 v78, v78\n\t" \
"v_exp_f32 v79, v79\n\t" \
"v_exp_f32 v80, v80\n\t" \
"v_exp_f32 v81, v81\n\t" \
"s_waitcnt vmcnt(15)\n\t" \
"v_smfmac_f32_32x32x32_bf16 v[32:47], v[160:163], v[72:79], v7\n\t" \
"v_exp_f32 v82, v82\n\t" \
"v_exp_f32 v83, v83\n\t" \
"s_waitcnt vmcnt(14)\n\t" \
"v_mfma_f32_32x32x16_bf16 v[64:79], v[132:135], v[24:27], v[96:111]\n\t" \
"v_exp_f32 v84, v84\n\t" \
"v_exp_f32 v85, v85\n\t" \
"v_exp_f32 v86, v86\n\t" \
"s_add_u32 m0, s62, 51200\n\t" \
"s_add_u32 s63, s81, 409600\n\t" \
"buffer_load_dwordx4 v11, s[84:87], s63 offen lds\n\t" \
"v_exp_f32 v87, v87\n\t" \
"v_exp_f32 v88, v88\n\t" \
"v_exp_f32 v89, v89\n\t" \
"v_smfmac_f32_32x32x32_bf16 v[48:63], v[144:147], v[80:87], v7\n\t" \
"s_mov_b64 exec, s[94:95]\n\t" \
"buffer_load_dwordx4 v[144:147], v6, s[28:31], s59 offen offset:1024 sc1\n\t" \
"s_mov_b64 exec, -1\n\t" \
"v_exp_f32 v90, v90\n\t" \
"v_exp_f32 v91, v91\n\t" \
"v_exp_f32 v92, v92\n\t" \
"v_exp_f32 v93, v93\n\t" \
"v_exp_f32 v94, v94\n\t" \
"v_exp_f32 v95, v95\n\t" \
"v_exp_f32 v64, v64\n\t" \
"v_exp_f32 v65, v65\n\t" \
"v_smfmac_f32_32x32x32_bf16 v[48:63], v[160:163], v[88:95], v7\n\t" \
"s_mov_b64 exec, s[94:95]\n\t" \
"buffer_load_dwordx4 v[160:163], v6, s[28:31], s59 offen offset:1536 sc1\n\t" \
"s_mov_b64 exec, -1\n\t" \
"s_add_u32 s59, s59, 2048\n\t" \
"v_exp_f32 v66, v66\n\t" \
"v_exp_f32 v67, v67\n\t" \
"v_mfma_f32_32x32x16_bf16 v[80:95], v[132:135], v[28:31], v[112:127]\n\t" \
"buffer_load_dwordx4 v[132:135], v1, s[28:31], s59 offen sc1\n\t" \
"v_exp_f32 v68, v68\n\t" \
"v_exp_f32 v69, v69\n\t" \
"v_exp_f32 v70, v70\n\t" \
"s_add_u32 m0, s62, 82944\n\t" \
"s_add_u32 s63, s61, 487424\n\t" \
"buffer_load_dwordx4 v13, s[40:43], s63 offen lds\n\t" \
"v_exp_f32 v71, v71\n\t" \
"v_exp_f32 v72, v72\n\t" \
"v_exp_f32 v73, v73\n\t" \
"s_waitcnt vmcnt(16)\n\t" \
"v_smfmac_f32_32x32x32_bf16 v[32:47], v[148:151], v[64:71], v7\n\t" \
"v_exp_f32 v74, v74\n\t" \
"v_exp_f32 v75, v75\n\t" \
"v_exp_f32 v76, v76\n\t" \
"v_exp_f32 v77, v77\n\t" \
"v_exp_f32 v78, v78\n\t" \
"v_exp_f32 v79, v79\n\t" \
"v_exp_f32 v80, v80\n\t" \
"v_exp_f32 v81, v81\n\t" \
"s_waitcnt vmcnt(15)\n\t" \
"v_smfmac_f32_32x32x32_bf16 v[32:47], v[164:167], v[72:79], v7\n\t" \
"v_exp_f32 v82, v82\n\t" \
"v_exp_f32 v83, v83\n\t" \
"s_waitcnt vmcnt(14)\n\t" \
"v_mfma_f32_32x32x16_bf16 v[64:79], v[136:139], v[24:27], v[96:111]\n\t" \
"v_exp_f32 v84, v84\n\t" \
"v_exp_f32 v85, v85\n\t" \
"v_exp_f32 v86, v86\n\t" \
"v_exp_f32 v87, v87\n\t" \
"v_exp_f32 v88, v88\n\t" \
"v_exp_f32 v89, v89\n\t" \
"v_smfmac_f32_32x32x32_bf16 v[48:63], v[148:151], v[80:87], v7\n\t" \
"s_mov_b64 exec, s[94:95]\n\t" \
"buffer_load_dwordx4 v[148:151], v6, s[28:31], s59 offen offset:1024 sc1\n\t" \
"s_mov_b64 exec, -1\n\t" \
"v_exp_f32 v90, v90\n\t" \
"v_exp_f32 v91, v91\n\t" \
"v_exp_f32 v92, v92\n\t" \
"v_exp_f32 v93, v93\n\t" \
"v_exp_f32 v94, v94\n\t" \
"v_exp_f32 v95, v95\n\t" \
"v_exp_f32 v64, v64\n\t" \
"v_exp_f32 v65, v65\n\t" \
"v_smfmac_f32_32x32x32_bf16 v[48:63], v[164:167], v[88:95], v7\n\t" \
"s_mov_b64 exec, s[94:95]\n\t" \
"buffer_load_dwordx4 v[164:167], v6, s[28:31], s59 offen offset:1536 sc1\n\t" \
"s_mov_b64 exec, -1\n\t" \
"s_add_u32 s59, s59, 2048\n\t" \
"v_exp_f32 v66, v66\n\t" \
"v_exp_f32 v67, v67\n\t" \
"v_mfma_f32_32x32x16_bf16 v[80:95], v[136:139], v[28:31], v[112:127]\n\t" \
"buffer_load_dwordx4 v[136:139], v1, s[28:31], s59 offen sc1\n\t" \
"v_exp_f32 v68, v68\n\t" \
"v_exp_f32 v69, v69\n\t" \
"v_exp_f32 v70, v70\n\t" \
"s_add_u32 m0, s62, 52224\n\t" \
"s_add_u32 s63, s81, 417792\n\t" \
"buffer_load_dwordx4 v11, s[84:87], s63 offen lds\n\t" \
"v_exp_f32 v71, v71\n\t" \
"v_exp_f32 v72, v72\n\t" \
"v_exp_f32 v73, v73\n\t" \
"s_waitcnt vmcnt(16)\n\t" \
"v_smfmac_f32_32x32x32_bf16 v[32:47], v[152:155], v[64:71], v7\n\t" \
"v_exp_f32 v74, v74\n\t" \
"v_exp_f32 v75, v75\n\t" \
"v_exp_f32 v76, v76\n\t" \
"v_exp_f32 v77, v77\n\t" \
"v_exp_f32 v78, v78\n\t" \
"v_exp_f32 v79, v79\n\t" \
"v_exp_f32 v80, v80\n\t" \
"v_exp_f32 v81, v81\n\t" \
"s_waitcnt vmcnt(15)\n\t" \
"v_smfmac_f32_32x32x32_bf16 v[32:47], v[168:171], v[72:79], v7\n\t" \
"v_exp_f32 v82, v82\n\t" \
"v_exp_f32 v83, v83\n\t" \
"s_waitcnt vmcnt(14)\n\t" \
"v_mfma_f32_32x32x16_bf16 v[64:79], v[140:143], v[24:27], v[96:111]\n\t" \
"v_exp_f32 v84, v84\n\t" \
"v_exp_f32 v85, v85\n\t" \
"v_exp_f32 v86, v86\n\t" \
"s_add_u32 s63, s82, 0\n\t" \
"buffer_load_dwordx4 v[20:23], v11, s[36:39], s63 offen\n\t" \
"v_exp_f32 v87, v87\n\t" \
"v_exp_f32 v88, v88\n\t" \
"v_exp_f32 v89, v89\n\t" \
"v_smfmac_f32_32x32x32_bf16 v[48:63], v[152:155], v[80:87], v7\n\t" \
"s_mov_b64 exec, s[94:95]\n\t" \
"buffer_load_dwordx4 v[152:155], v6, s[28:31], s59 offen offset:1024 sc1\n\t" \
"s_mov_b64 exec, -1\n\t" \
"v_exp_f32 v90, v90\n\t" \
"v_exp_f32 v91, v91\n\t" \
"v_exp_f32 v92, v92\n\t" \
"v_exp_f32 v93, v93\n\t" \
"v_exp_f32 v94, v94\n\t" \
"v_exp_f32 v95, v95\n\t" \
"v_exp_f32 v64, v64\n\t" \
"v_exp_f32 v65, v65\n\t" \
"v_smfmac_f32_32x32x32_bf16 v[48:63], v[168:171], v[88:95], v7\n\t" \
"s_mov_b64 exec, s[94:95]\n\t" \
"buffer_load_dwordx4 v[168:171], v6, s[28:31], s59 offen offset:1536 sc1\n\t" \
"s_mov_b64 exec, -1\n\t" \
"s_add_u32 s59, s59, 2048\n\t" \
"v_exp_f32 v66, v66\n\t" \
"v_exp_f32 v67, v67\n\t" \
"v_mfma_f32_32x32x16_bf16 v[80:95], v[140:143], v[28:31], v[112:127]\n\t" \
"buffer_load_dwordx4 v[140:143], v1, s[28:31], s59 offen sc1\n\t" \
"v_exp_f32 v68, v68\n\t" \
"v_exp_f32 v69, v69\n\t" \
"v_exp_f32 v70, v70\n\t" \
"s_add_u32 s63, s82, 8192\n\t" \
"buffer_load_dwordx4 v[20:23], v11, s[36:39], s63 offen\n\t" \
"v_exp_f32 v71, v71\n\t" \
"v_exp_f32 v72, v72\n\t" \
"v_exp_f32 v73, v73\n\t" \
"s_waitcnt vmcnt(16)\n\t" \
"v_smfmac_f32_32x32x32_bf16 v[32:47], v[156:159], v[64:71], v7\n\t" \
"v_exp_f32 v74, v74\n\t" \
"v_exp_f32 v75, v75\n\t" \
"v_exp_f32 v76, v76\n\t" \
"v_exp_f32 v77, v77\n\t" \
"v_exp_f32 v78, v78\n\t" \
"v_exp_f32 v79, v79\n\t" \
"v_exp_f32 v80, v80\n\t" \
"v_exp_f32 v81, v81\n\t" \
"s_waitcnt vmcnt(15)\n\t" \
"v_smfmac_f32_32x32x32_bf16 v[32:47], v[172:175], v[72:79], v7\n\t" \
"v_exp_f32 v82, v82\n\t" \
"v_exp_f32 v83, v83\n\t" \
"s_waitcnt vmcnt(14)\n\t" \
"v_mfma_f32_32x32x16_bf16 v[64:79], v[128:131], v[24:27], v[96:111]\n\t" \
"v_exp_f32 v84, v84\n\t" \
"v_exp_f32 v85, v85\n\t" \
"v_exp_f32 v86, v86\n\t" \
"v_exp_f32 v87, v87\n\t" \
"v_exp_f32 v88, v88\n\t" \
"v_exp_f32 v89, v89\n\t" \
"v_smfmac_f32_32x32x32_bf16 v[48:63], v[156:159], v[80:87], v7\n\t" \
"s_mov_b64 exec, s[94:95]\n\t" \
"buffer_load_dwordx4 v[156:159], v6, s[28:31], s59 offen offset:1024 sc1\n\t" \
"s_mov_b64 exec, -1\n\t" \
"v_exp_f32 v90, v90\n\t" \
"v_exp_f32 v91, v91\n\t" \
"v_exp_f32 v92, v92\n\t" \
"v_exp_f32 v93, v93\n\t" \
"v_exp_f32 v94, v94\n\t" \
"v_exp_f32 v95, v95\n\t" \
"v_exp_f32 v64, v64\n\t" \
"v_exp_f32 v65, v65\n\t" \
"v_smfmac_f32_32x32x32_bf16 v[48:63], v[172:175], v[88:95], v7\n\t" \
"s_mov_b64 exec, s[94:95]\n\t" \
"buffer_load_dwordx4 v[172:175], v6, s[28:31], s59 offen offset:1536 sc1\n\t" \
"s_mov_b64 exec, -1\n\t" \
"s_add_u32 s59, s59, 2048\n\t" \
"v_exp_f32 v66, v66\n\t" \
"v_exp_f32 v67, v67\n\t" \
"v_mfma_f32_32x32x16_bf16 v[80:95], v[128:131], v[28:31], v[112:127]\n\t" \
"buffer_load_dwordx4 v[128:131], v1, s[28:31], s59 offen sc1\n\t" \
"v_exp_f32 v68, v68\n\t" \
"v_exp_f32 v69, v69\n\t" \
"v_exp_f32 v70, v70\n\t" \
"s_add_u32 m0, s62, 83968\n\t" \
"s_add_u32 s63, s61, 516096\n\t" \
"buffer_load_dwordx4 v13, s[40:43], s63 offen lds\n\t" \
"v_exp_f32 v71, v71\n\t" \
"v_exp_f32 v72, v72\n\t" \
"v_exp_f32 v73, v73\n\t" \
"s_waitcnt vmcnt(16)\n\t" \
"v_smfmac_f32_32x32x32_bf16 v[32:47], v[144:147], v[64:71], v7\n\t" \
"v_exp_f32 v74, v74\n\t" \
"v_exp_f32 v75, v75\n\t" \
"v_exp_f32 v76, v76\n\t" \
"v_exp_f32 v77, v77\n\t" \
"v_exp_f32 v78, v78\n\t" \
"v_exp_f32 v79, v79\n\t" \
"v_exp_f32 v80, v80\n\t" \
"v_exp_f32 v81, v81\n\t" \
"s_waitcnt vmcnt(15)\n\t" \
"v_smfmac_f32_32x32x32_bf16 v[32:47], v[160:163], v[72:79], v7\n\t" \
"v_exp_f32 v82, v82\n\t" \
"v_exp_f32 v83, v83\n\t" \
"s_waitcnt vmcnt(14)\n\t" \
"v_mfma_f32_32x32x16_bf16 v[64:79], v[132:135], v[24:27], v[96:111]\n\t" \
"v_exp_f32 v84, v84\n\t" \
"v_exp_f32 v85, v85\n\t" \
"v_exp_f32 v86, v86\n\t" \
"s_add_u32 s63, s82, 16384\n\t" \
"buffer_load_dwordx4 v[20:23], v11, s[36:39], s63 offen\n\t" \
"v_exp_f32 v87, v87\n\t" \
"v_exp_f32 v88, v88\n\t" \
"v_exp_f32 v89, v89\n\t" \
"v_smfmac_f32_32x32x32_bf16 v[48:63], v[144:147], v[80:87], v7\n\t" \
"s_mov_b64 exec, s[94:95]\n\t" \
"buffer_load_dwordx4 v[144:147], v6, s[28:31], s59 offen offset:1024 sc1\n\t" \
"s_mov_b64 exec, -1\n\t" \
"v_exp_f32 v90, v90\n\t" \
"v_exp_f32 v91, v91\n\t" \
"v_exp_f32 v92, v92\n\t" \
"v_exp_f32 v93, v93\n\t" \
"v_exp_f32 v94, v94\n\t" \
"v_exp_f32 v95, v95\n\t" \
"v_exp_f32 v64, v64\n\t" \
"v_exp_f32 v65, v65\n\t" \
"v_smfmac_f32_32x32x32_bf16 v[48:63], v[160:163], v[88:95], v7\n\t" \
"s_mov_b64 exec, s[94:95]\n\t" \
"buffer_load_dwordx4 v[160:163], v6, s[28:31], s59 offen offset:1536 sc1\n\t" \
"s_mov_b64 exec, -1\n\t" \
"s_add_u32 s59, s59, 2048\n\t" \
"v_exp_f32 v66, v66\n\t" \
"v_exp_f32 v67, v67\n\t" \
"v_mfma_f32_32x32x16_bf16 v[80:95], v[132:135], v[28:31], v[112:127]\n\t" \
"buffer_load_dwordx4 v[132:135], v1, s[28:31], s59 offen sc1\n\t" \
"v_exp_f32 v68, v68\n\t" \
"v_exp_f32 v69, v69\n\t" \
"v_exp_f32 v70, v70\n\t" \
"v_exp_f32 v71, v71\n\t" \
"v_exp_f32 v72, v72\n\t" \
"v_exp_f32 v73, v73\n\t" \
"s_waitcnt vmcnt(16)\n\t" \
"v_smfmac_f32_32x32x32_bf16 v[32:47], v[148:151], v[64:71], v7\n\t" \
"v_exp_f32 v74, v74\n\t" \
"v_exp_f32 v75, v75\n\t" \
"v_exp_f32 v76, v76\n\t" \
"v_exp_f32 v77, v77\n\t" \
"v_exp_f32 v78, v78\n\t" \
"v_exp_f32 v79, v79\n\t" \
"v_exp_f32 v80, v80\n\t" \
"v_exp_f32 v81, v81\n\t" \
"s_waitcnt vmcnt(15)\n\t" \
"v_smfmac_f32_32x32x32_bf16 v[32:47], v[164:167], v[72:79], v7\n\t" \
"v_exp_f32 v82, v82\n\t" \
"v_exp_f32 v83, v83\n\t" \
"s_waitcnt vmcnt(14)\n\t" \
"v_mfma_f32_32x32x16_bf16 v[64:79], v[136:139], v[24:27], v[96:111]\n\t" \
"v_exp_f32 v84, v84\n\t" \
"v_exp_f32 v85, v85\n\t" \
"v_exp_f32 v86, v86\n\t" \
"s_add_u32 s63, s82, 24576\n\t" \
"buffer_load_dwordx4 v[20:23], v11, s[36:39], s63 offen\n\t" \
"v_exp_f32 v87, v87\n\t" \
"v_exp_f32 v88, v88\n\t" \
"v_exp_f32 v89, v89\n\t" \
"v_smfmac_f32_32x32x32_bf16 v[48:63], v[148:151], v[80:87], v7\n\t" \
"s_mov_b64 exec, s[94:95]\n\t" \
"buffer_load_dwordx4 v[148:151], v6, s[28:31], s59 offen offset:1024 sc1\n\t" \
"s_mov_b64 exec, -1\n\t" \
"v_exp_f32 v90, v90\n\t" \
"v_exp_f32 v91, v91\n\t" \
"v_exp_f32 v92, v92\n\t" \
"v_exp_f32 v93, v93\n\t" \
"v_exp_f32 v94, v94\n\t" \
"v_exp_f32 v95, v95\n\t" \
"v_exp_f32 v64, v64\n\t" \
"v_exp_f32 v65, v65\n\t" \
"v_smfmac_f32_32x32x32_bf16 v[48:63], v[164:167], v[88:95], v7\n\t" \
"s_mov_b64 exec, s[94:95]\n\t" \
"buffer_load_dwordx4 v[164:167], v6, s[28:31], s59 offen offset:1536 sc1\n\t" \
"s_mov_b64 exec, -1\n\t" \
"s_add_u32 s59, s59, 2048\n\t" \
"v_exp_f32 v66, v66\n\t" \
"v_exp_f32 v67, v67\n\t" \
"v_mfma_f32_32x32x16_bf16 v[80:95], v[136:139], v[28:31], v[112:127]\n\t" \
"buffer_load_dwordx4 v[136:139], v1, s[28:31], s59 offen sc1\n\t" \
"v_exp_f32 v68, v68\n\t" \
"v_exp_f32 v69, v69\n\t" \
"v_exp_f32 v70, v70\n\t" \
"s_add_u32 s63, s79, 0\n\t" \
"buffer_load_dwordx4 v[20:23], v11, s[36:39], s63 offen\n\t" \
"v_exp_f32 v71, v71\n\t" \
"v_exp_f32 v72, v72\n\t" \
"v_exp_f32 v73, v73\n\t" \
"s_waitcnt vmcnt(16)\n\t" \
"v_smfmac_f32_32x32x32_bf16 v[32:47], v[152:155], v[64:71], v7\n\t" \
"v_exp_f32 v74, v74\n\t" \
"v_exp_f32 v75, v75\n\t" \
"v_exp_f32 v76, v76\n\t" \
"v_exp_f32 v77, v77\n\t" \
"v_exp_f32 v78, v78\n\t" \
"v_exp_f32 v79, v79\n\t" \
"v_exp_f32 v80, v80\n\t" \
"v_exp_f32 v81, v81\n\t" \
"s_waitcnt vmcnt(15)\n\t" \
"v_smfmac_f32_32x32x32_bf16 v[32:47], v[168:171], v[72:79], v7\n\t" \
"v_exp_f32 v82, v82\n\t" \
"v_exp_f32 v83, v83\n\t" \
"s_waitcnt vmcnt(14)\n\t" \
"v_mfma_f32_32x32x16_bf16 v[64:79], v[140:143], v[24:27], v[96:111]\n\t" \
"v_exp_f32 v84, v84\n\t" \
"v_exp_f32 v85, v85\n\t" \
"v_exp_f32 v86, v86\n\t" \
"v_exp_f32 v87, v87\n\t" \
"v_exp_f32 v88, v88\n\t" \
"v_exp_f32 v89, v89\n\t" \
"v_smfmac_f32_32x32x32_bf16 v[48:63], v[152:155], v[80:87], v7\n\t" \
"s_mov_b64 exec, s[94:95]\n\t" \
"buffer_load_dwordx4 v[152:155], v6, s[28:31], s59 offen offset:1024 sc1\n\t" \
"s_mov_b64 exec, -1\n\t" \
"v_exp_f32 v90, v90\n\t" \
"v_exp_f32 v91, v91\n\t" \
"v_exp_f32 v92, v92\n\t" \
"v_exp_f32 v93, v93\n\t" \
"v_exp_f32 v94, v94\n\t" \
"v_exp_f32 v95, v95\n\t" \
"v_exp_f32 v64, v64\n\t" \
"v_exp_f32 v65, v65\n\t" \
"v_smfmac_f32_32x32x32_bf16 v[48:63], v[168:171], v[88:95], v7\n\t" \
"s_mov_b64 exec, s[94:95]\n\t" \
"buffer_load_dwordx4 v[168:171], v6, s[28:31], s59 offen offset:1536 sc1\n\t" \
"s_mov_b64 exec, -1\n\t" \
"s_add_u32 s59, s59, 2048\n\t" \
"v_exp_f32 v66, v66\n\t" \
"v_exp_f32 v67, v67\n\t" \
"v_mfma_f32_32x32x16_bf16 v[80:95], v[140:143], v[28:31], v[112:127]\n\t" \
"buffer_load_dwordx4 v[140:143], v1, s[28:31], s59 offen sc1\n\t" \
"v_exp_f32 v68, v68\n\t" \
"v_exp_f32 v69, v69\n\t" \
"v_exp_f32 v70, v70\n\t" \
"s_add_u32 m0, s62, 84992\n\t" \
"s_add_u32 s63, s61, 544768\n\t" \
"buffer_load_dwordx4 v13, s[40:43], s63 offen lds\n\t" \
"v_exp_f32 v71, v71\n\t" \
"v_exp_f32 v72, v72\n\t" \
"v_exp_f32 v73, v73\n\t" \
"s_waitcnt vmcnt(16)\n\t" \
"v_smfmac_f32_32x32x32_bf16 v[32:47], v[156:159], v[64:71], v7\n\t" \
"v_exp_f32 v74, v74\n\t" \
"v_exp_f32 v75, v75\n\t" \
"v_exp_f32 v76, v76\n\t" \
"v_exp_f32 v77, v77\n\t" \
"v_exp_f32 v78, v78\n\t" \
"v_exp_f32 v79, v79\n\t" \
"v_exp_f32 v80, v80\n\t" \
"v_exp_f32 v81, v81\n\t" \
"s_waitcnt vmcnt(15)\n\t" \
"v_smfmac_f32_32x32x32_bf16 v[32:47], v[172:175], v[72:79], v7\n\t" \
"v_exp_f32 v82, v82\n\t" \
"v_exp_f32 v83, v83\n\t" \
"s_waitcnt vmcnt(14)\n\t" \
"v_mfma_f32_32x32x16_bf16 v[64:79], v[128:131], v[24:27], v[96:111]\n\t" \
"v_exp_f32 v84, v84\n\t" \
"v_exp_f32 v85, v85\n\t" \
"v_exp_f32 v86, v86\n\t" \
"s_add_u32 s63, s79, 8192\n\t" \
"buffer_load_dwordx4 v[20:23], v11, s[36:39], s63 offen\n\t" \
"v_exp_f32 v87, v87\n\t" \
"v_exp_f32 v88, v88\n\t" \
"v_exp_f32 v89, v89\n\t" \
"v_smfmac_f32_32x32x32_bf16 v[48:63], v[156:159], v[80:87], v7\n\t" \
"s_mov_b64 exec, s[94:95]\n\t" \
"buffer_load_dwordx4 v[156:159], v6, s[28:31], s59 offen offset:1024 sc1\n\t" \
"s_mov_b64 exec, -1\n\t" \
"v_exp_f32 v90, v90\n\t" \
"v_exp_f32 v91, v91\n\t" \
"v_exp_f32 v92, v92\n\t" \
"v_exp_f32 v93, v93\n\t" \
"v_exp_f32 v94, v94\n\t" \
"v_exp_f32 v95, v95\n\t" \
"v_exp_f32 v64, v64\n\t" \
"v_exp_f32 v65, v65\n\t" \
"v_smfmac_f32_32x32x32_bf16 v[48:63], v[172:175], v[88:95], v7\n\t" \
"s_mov_b64 exec, s[94:95]\n\t" \
"buffer_load_dwordx4 v[172:175], v6, s[28:31], s59 offen offset:1536 sc1\n\t" \
"s_mov_b64 exec, -1\n\t" \
"s_add_u32 s59, s59, 2048\n\t" \
"v_exp_f32 v66, v66\n\t" \
"v_exp_f32 v67, v67\n\t" \
"v_mfma_f32_32x32x16_bf16 v[80:95], v[128:131], v[28:31], v[112:127]\n\t" \
"buffer_load_dwordx4 v[128:131], v1, s[28:31], s59 offen sc1\n\t" \
"v_exp_f32 v68, v68\n\t" \
"v_exp_f32 v69, v69\n\t" \
"v_exp_f32 v70, v70\n\t" \
"v_exp_f32 v71, v71\n\t" \
"v_exp_f32 v72, v72\n\t" \
"v_exp_f32 v73, v73\n\t" \
"s_waitcnt vmcnt(15)\n\t" \
"v_smfmac_f32_32x32x32_bf16 v[32:47], v[144:147], v[64:71], v7\n\t" \
"v_exp_f32 v74, v74\n\t" \
"v_exp_f32 v75, v75\n\t" \
"v_exp_f32 v76, v76\n\t" \
"v_exp_f32 v77, v77\n\t" \
"v_exp_f32 v78, v78\n\t" \
"v_exp_f32 v79, v79\n\t" \
"v_exp_f32 v80, v80\n\t" \
"v_exp_f32 v81, v81\n\t" \
"s_waitcnt vmcnt(14)\n\t" \
"v_smfmac_f32_32x32x32_bf16 v[32:47], v[160:163], v[72:79], v7\n\t" \
"v_exp_f32 v82, v82\n\t" \
"v_exp_f32 v83, v83\n\t" \
"s_waitcnt vmcnt(13)\n\t" \
"v_mfma_f32_32x32x16_bf16 v[64:79], v[132:135], v[24:27], v[96:111]\n\t" \
"v_exp_f32 v84, v84\n\t" \
"v_exp_f32 v85, v85\n\t" \
"v_exp_f32 v86, v86\n\t" \
"s_add_u32 s63, s79, 16384\n\t" \
"buffer_load_dwordx4 v[20:23], v11, s[36:39], s63 offen\n\t" \
"v_exp_f32 v87, v87\n\t" \
"v_exp_f32 v88, v88\n\t" \
"v_exp_f32 v89, v89\n\t" \
"v_smfmac_f32_32x32x32_bf16 v[48:63], v[144:147], v[80:87], v7\n\t" \
"s_mov_b64 exec, s[94:95]\n\t" \
"buffer_load_dwordx4 v[144:147], v6, s[28:31], s59 offen offset:1024 sc1\n\t" \
"s_mov_b64 exec, -1\n\t" \
"v_exp_f32 v90, v90\n\t" \
"v_exp_f32 v91, v91\n\t" \
"v_exp_f32 v92, v92\n\t" \
"v_exp_f32 v93, v93\n\t" \
"v_exp_f32 v94, v94\n\t" \
"v_exp_f32 v95, v95\n\t" \
"v_exp_f32 v64, v64\n\t" \
"v_exp_f32 v65, v65\n\t" \
"v_smfmac_f32_32x32x32_bf16 v[48:63], v[160:163], v[88:95], v7\n\t" \
"s_mov_b64 exec, s[94:95]\n\t" \
"buffer_load_dwordx4 v[160:163], v6, s[28:31], s59 offen offset:1536 sc1\n\t" \
"s_mov_b64 exec, -1\n\t" \
"s_add_u32 s59, s59, 2048\n\t" \
"v_exp_f32 v66, v66\n\t" \
"v_exp_f32 v67, v67\n\t" \
"v_mfma_f32_32x32x16_bf16 v[80:95], v[132:135], v[28:31], v[112:127]\n\t" \
"buffer_load_dwordx4 v[132:135], v1, s[28:31], s59 offen sc1\n\t" \
"v_exp_f32 v68, v68\n\t" \
"v_exp_f32 v69, v69\n\t" \
"v_exp_f32 v70, v70\n\t" \
"s_add_u32 s63, s79, 24576\n\t" \
"buffer_load_dwordx4 v[20:23], v11, s[36:39], s63 offen\n\t" \
"v_exp_f32 v71, v71\n\t" \
"v_exp_f32 v72, v72\n\t" \
"v_exp_f32 v73, v73\n\t" \
"s_waitcnt vmcnt(16)\n\t" \
"v_smfmac_f32_32x32x32_bf16 v[32:47], v[148:151], v[64:71], v7\n\t" \
"v_exp_f32 v74, v74\n\t" \
"v_exp_f32 v75, v75\n\t" \
"v_exp_f32 v76, v76\n\t" \
"v_exp_f32 v77, v77\n\t" \
"v_exp_f32 v78, v78\n\t" \
"v_exp_f32 v79, v79\n\t" \
"v_exp_f32 v80, v80\n\t" \
"v_exp_f32 v81, v81\n\t" \
"s_waitcnt vmcnt(15)\n\t" \
"v_smfmac_f32_32x32x32_bf16 v[32:47], v[164:167], v[72:79], v7\n\t" \
"v_exp_f32 v82, v82\n\t" \
"v_exp_f32 v83, v83\n\t" \
"s_waitcnt vmcnt(14)\n\t" \
"v_mfma_f32_32x32x16_bf16 v[64:79], v[136:139], v[24:27], v[96:111]\n\t" \
"v_exp_f32 v84, v84\n\t" \
"v_exp_f32 v85, v85\n\t" \
"v_exp_f32 v86, v86\n\t" \
"s_add_u32 m0, s62, 98304\n\t" \
"s_add_u32 s63, s61, 917504\n\t" \
"buffer_load_dwordx4 v13, s[40:43], s63 offen lds\n\t" \
"v_exp_f32 v87, v87\n\t" \
"v_exp_f32 v88, v88\n\t" \
"v_exp_f32 v89, v89\n\t" \
"v_smfmac_f32_32x32x32_bf16 v[48:63], v[148:151], v[80:87], v7\n\t" \
"s_mov_b64 exec, s[94:95]\n\t" \
"buffer_load_dwordx4 v[148:151], v6, s[28:31], s59 offen offset:1024 sc1\n\t" \
"s_mov_b64 exec, -1\n\t" \
"v_exp_f32 v90, v90\n\t" \
"v_exp_f32 v91, v91\n\t" \
"v_exp_f32 v92, v92\n\t" \
"v_exp_f32 v93, v93\n\t" \
"v_exp_f32 v94, v94\n\t" \
"v_exp_f32 v95, v95\n\t" \
"v_exp_f32 v64, v64\n\t" \
"v_exp_f32 v65, v65\n\t" \
"v_smfmac_f32_32x32x32_bf16 v[48:63], v[164:167], v[88:95], v7\n\t" \
"s_mov_b64 exec, s[94:95]\n\t" \
"buffer_load_dwordx4 v[164:167], v6, s[28:31], s59 offen offset:1536 sc1\n\t" \
"s_mov_b64 exec, -1\n\t" \
"s_add_u32 s59, s59, 2048\n\t" \
"v_exp_f32 v66, v66\n\t" \
"v_exp_f32 v67, v67\n\t" \
"v_mfma_f32_32x32x16_bf16 v[80:95], v[136:139], v[28:31], v[112:127]\n\t" \
"buffer_load_dwordx4 v[136:139], v1, s[28:31], s59 offen sc1\n\t" \
"v_exp_f32 v68, v68\n\t" \
"v_exp_f32 v69, v69\n\t" \
"v_exp_f32 v70, v70\n\t" \
"v_exp_f32 v71, v71\n\t" \
"v_exp_f32 v72, v72\n\t" \
"v_exp_f32 v73, v73\n\t" \
"s_waitcnt vmcnt(16)\n\t" \
"v_smfmac_f32_32x32x32_bf16 v[32:47], v[152:155], v[64:71], v7\n\t" \
"v_exp_f32 v74, v74\n\t" \
"v_exp_f32 v75, v75\n\t" \
"v_exp_f32 v76, v76\n\t" \
"v_exp_f32 v77, v77\n\t" \
"v_exp_f32 v78, v78\n\t" \
"v_exp_f32 v79, v79\n\t" \
"v_exp_f32 v80, v80\n\t" \
"v_exp_f32 v81, v81\n\t" \
"s_waitcnt vmcnt(15)\n\t" \
"v_smfmac_f32_32x32x32_bf16 v[32:47], v[168:171], v[72:79], v7\n\t" \
"v_exp_f32 v82, v82\n\t" \
"v_exp_f32 v83, v83\n\t" \
"s_waitcnt vmcnt(14)\n\t" \
"v_mfma_f32_32x32x16_bf16 v[64:79], v[140:143], v[24:27], v[96:111]\n\t" \
"v_exp_f32 v84, v84\n\t" \
"v_exp_f32 v85, v85\n\t" \
"v_exp_f32 v86, v86\n\t" \
"s_add_u32 s63, s60, 0\n\t" \
"buffer_load_dwordx4 v[176:179], v11, s[36:39], s63 offen\n\t" \
"v_exp_f32 v87, v87\n\t" \
"v_exp_f32 v88, v88\n\t" \
"v_exp_f32 v89, v89\n\t" \
"v_smfmac_f32_32x32x32_bf16 v[48:63], v[152:155], v[80:87], v7\n\t" \
"s_mov_b64 exec, s[94:95]\n\t" \
"buffer_load_dwordx4 v[152:155], v6, s[28:31], s59 offen offset:1024 sc1\n\t" \
"s_mov_b64 exec, -1\n\t" \
"v_exp_f32 v90, v90\n\t" \
"v_exp_f32 v91, v91\n\t" \
"v_exp_f32 v92, v92\n\t" \
"v_exp_f32 v93, v93\n\t" \
"v_exp_f32 v94, v94\n\t" \
"v_exp_f32 v95, v95\n\t" \
"v_exp_f32 v64, v64\n\t" \
"v_exp_f32 v65, v65\n\t" \
"v_smfmac_f32_32x32x32_bf16 v[48:63], v[168:171], v[88:95], v7\n\t" \
"s_mov_b64 exec, s[94:95]\n\t" \
"buffer_load_dwordx4 v[168:171], v6, s[28:31], s59 offen offset:1536 sc1\n\t" \
"s_mov_b64 exec, -1\n\t" \
"s_add_u32 s59, s59, 2048\n\t" \
"v_exp_f32 v66, v66\n\t" \
"v_exp_f32 v67, v67\n\t" \
"v_mfma_f32_32x32x16_bf16 v[80:95], v[140:143], v[28:31], v[112:127]\n\t" \
"buffer_load_dwordx4 v[140:143], v1, s[28:31], s59 offen sc1\n\t" \
"v_exp_f32 v68, v68\n\t" \
"v_exp_f32 v69, v69\n\t" \
"v_exp_f32 v70, v70\n\t" \
"s_add_u32 s63, s60, 8192\n\t" \
"buffer_load_dwordx4 v[180:183], v11, s[36:39], s63 offen\n\t" \
"v_exp_f32 v71, v71\n\t" \
"v_exp_f32 v72, v72\n\t" \
"v_exp_f32 v73, v73\n\t" \
"s_waitcnt vmcnt(16)\n\t" \
"v_smfmac_f32_32x32x32_bf16 v[32:47], v[156:159], v[64:71], v7\n\t" \
"v_exp_f32 v74, v74\n\t" \
"v_exp_f32 v75, v75\n\t" \
"v_exp_f32 v76, v76\n\t" \
"v_exp_f32 v77, v77\n\t" \
"v_exp_f32 v78, v78\n\t" \
"v_exp_f32 v79, v79\n\t" \
"v_exp_f32 v80, v80\n\t" \
"v_exp_f32 v81, v81\n\t" \
"s_waitcnt vmcnt(15)\n\t" \
"v_smfmac_f32_32x32x32_bf16 v[32:47], v[172:175], v[72:79], v7\n\t" \
"v_exp_f32 v82, v82\n\t" \
"v_exp_f32 v83, v83\n\t" \
"s_waitcnt vmcnt(14)\n\t" \
"v_mfma_f32_32x32x16_bf16 v[64:79], v[128:131], v[24:27], v[96:111]\n\t" \
"v_exp_f32 v84, v84\n\t" \
"v_exp_f32 v85, v85\n\t" \
"v_exp_f32 v86, v86\n\t" \
"v_exp_f32 v87, v87\n\t" \
"v_exp_f32 v88, v88\n\t" \
"v_exp_f32 v89, v89\n\t" \
"v_smfmac_f32_32x32x32_bf16 v[48:63], v[156:159], v[80:87], v7\n\t" \
"s_mov_b64 exec, s[94:95]\n\t" \
"buffer_load_dwordx4 v[156:159], v6, s[28:31], s59 offen offset:1024 sc1\n\t" \
"s_mov_b64 exec, -1\n\t" \
"v_exp_f32 v90, v90\n\t" \
"v_exp_f32 v91, v91\n\t" \
"v_exp_f32 v92, v92\n\t" \
"v_exp_f32 v93, v93\n\t" \
"v_exp_f32 v94, v94\n\t" \
"v_exp_f32 v95, v95\n\t" \
"v_exp_f32 v64, v64\n\t" \
"v_exp_f32 v65, v65\n\t" \
"v_smfmac_f32_32x32x32_bf16 v[48:63], v[172:175], v[88:95], v7\n\t" \
"s_mov_b64 exec, s[94:95]\n\t" \
"buffer_load_dwordx4 v[172:175], v6, s[28:31], s59 offen offset:1536 sc1\n\t" \
"s_mov_b64 exec, -1\n\t" \
"s_add_u32 s59, s59, 2048\n\t" \
"v_exp_f32 v66, v66\n\t" \
"v_exp_f32 v67, v67\n\t" \
"v_mfma_f32_32x32x16_bf16 v[80:95], v[128:131], v[28:31], v[112:127]\n\t" \
"buffer_load_dwordx4 v[128:131], v1, s[28:31], s59 offen sc1\n\t" \
"v_exp_f32 v68, v68\n\t" \
"v_exp_f32 v69, v69\n\t" \
"v_exp_f32 v70, v70\n\t" \
"s_add_u32 m0, s62, 99328\n\t" \
"s_add_u32 s63, s61, 946176\n\t" \
"buffer_load_dwordx4 v13, s[40:43], s63 offen lds\n\t" \
"v_exp_f32 v71, v71\n\t" \
"v_exp_f32 v72, v72\n\t" \
"v_exp_f32 v73, v73\n\t" \
"s_waitcnt vmcnt(16)\n\t" \
"v_smfmac_f32_32x32x32_bf16 v[32:47], v[144:147], v[64:71], v7\n\t" \
"v_exp_f32 v74, v74\n\t" \
"v_exp_f32 v75, v75\n\t" \
"v_exp_f32 v76, v76\n\t" \
"v_exp_f32 v77, v77\n\t" \
"v_exp_f32 v78, v78\n\t" \
"v_exp_f32 v79, v79\n\t" \
"v_exp_f32 v80, v80\n\t" \
"v_exp_f32 v81, v81\n\t" \
"s_waitcnt vmcnt(15)\n\t" \
"v_smfmac_f32_32x32x32_bf16 v[32:47], v[160:163], v[72:79], v7\n\t" \
"v_exp_f32 v82, v82\n\t" \
"v_exp_f32 v83, v83\n\t" \
"s_waitcnt vmcnt(14)\n\t" \
"v_mfma_f32_32x32x16_bf16 v[64:79], v[132:135], v[24:27], v[96:111]\n\t" \
"v_exp_f32 v84, v84\n\t" \
"v_exp_f32 v85, v85\n\t" \
"v_exp_f32 v86, v86\n\t" \
"s_add_u32 s63, s60, 16384\n\t" \
"buffer_load_dwordx4 v[184:187], v11, s[36:39], s63 offen\n\t" \
"v_exp_f32 v87, v87\n\t" \
"v_exp_f32 v88, v88\n\t" \
"v_exp_f32 v89, v89\n\t" \
"v_smfmac_f32_32x32x32_bf16 v[48:63], v[144:147], v[80:87], v7\n\t" \
"s_mov_b64 exec, s[94:95]\n\t" \
"buffer_load_dwordx4 v[144:147], v6, s[28:31], s59 offen offset:1024 sc1\n\t" \
"s_mov_b64 exec, -1\n\t" \
"v_exp_f32 v90, v90\n\t" \
"v_exp_f32 v91, v91\n\t" \
"v_exp_f32 v92, v92\n\t" \
"v_exp_f32 v93, v93\n\t" \
"v_exp_f32 v94, v94\n\t" \
"v_exp_f32 v95, v95\n\t" \
"v_exp_f32 v64, v64\n\t" \
"v_exp_f32 v65, v65\n\t" \
"v_smfmac_f32_32x32x32_bf16 v[48:63], v[160:163], v[88:95], v7\n\t" \
"s_mov_b64 exec, s[94:95]\n\t" \
"buffer_load_dwordx4 v[160:163], v6, s[28:31], s59 offen offset:1536 sc1\n\t" \
"s_mov_b64 exec, -1\n\t" \
"s_add_u32 s59, s59, 2048\n\t" \
"v_exp_f32 v66, v66\n\t" \
"v_exp_f32 v67, v67\n\t" \
"v_mfma_f32_32x32x16_bf16 v[80:95], v[132:135], v[28:31], v[112:127]\n\t" \
"buffer_load_dwordx4 v[132:135], v1, s[28:31], s59 offen sc1\n\t" \
"v_exp_f32 v68, v68\n\t" \
"v_exp_f32 v69, v69\n\t" \
"v_exp_f32 v70, v70\n\t" \
"v_exp_f32 v71, v71\n\t" \
"v_exp_f32 v72, v72\n\t" \
"v_exp_f32 v73, v73\n\t" \
"s_waitcnt vmcnt(15)\n\t" \
"v_smfmac_f32_32x32x32_bf16 v[32:47], v[148:151], v[64:71], v7\n\t" \
"v_exp_f32 v74, v74\n\t" \
"v_exp_f32 v75, v75\n\t" \
"v_exp_f32 v76, v76\n\t" \
"v_exp_f32 v77, v77\n\t" \
"v_exp_f32 v78, v78\n\t" \
"v_exp_f32 v79, v79\n\t" \
"v_exp_f32 v80, v80\n\t" \
"v_exp_f32 v81, v81\n\t" \
"s_waitcnt vmcnt(14)\n\t" \
"v_smfmac_f32_32x32x32_bf16 v[32:47], v[164:167], v[72:79], v7\n\t" \
"v_exp_f32 v82, v82\n\t" \
"v_exp_f32 v83, v83\n\t" \
"s_waitcnt vmcnt(13)\n\t" \
"v_mfma_f32_32x32x16_bf16 v[64:79], v[136:139], v[24:27], v[96:111]\n\t" \
"v_exp_f32 v84, v84\n\t" \
"v_exp_f32 v85, v85\n\t" \
"v_exp_f32 v86, v86\n\t" \
"s_add_u32 s63, s60, 24576\n\t" \
"buffer_load_dwordx4 v[188:191], v11, s[36:39], s63 offen\n\t" \
"v_exp_f32 v87, v87\n\t" \
"v_exp_f32 v88, v88\n\t" \
"v_exp_f32 v89, v89\n\t" \
"v_smfmac_f32_32x32x32_bf16 v[48:63], v[148:151], v[80:87], v7\n\t" \
"s_mov_b64 exec, s[94:95]\n\t" \
"buffer_load_dwordx4 v[148:151], v6, s[28:31], s59 offen offset:1024 sc1\n\t" \
"s_mov_b64 exec, -1\n\t" \
"v_exp_f32 v90, v90\n\t" \
"v_exp_f32 v91, v91\n\t" \
"v_exp_f32 v92, v92\n\t" \
"v_exp_f32 v93, v93\n\t" \
"v_exp_f32 v94, v94\n\t" \
"v_exp_f32 v95, v95\n\t" \
"v_exp_f32 v64, v64\n\t" \
"v_exp_f32 v65, v65\n\t" \
"v_smfmac_f32_32x32x32_bf16 v[48:63], v[164:167], v[88:95], v7\n\t" \
"s_mov_b64 exec, s[94:95]\n\t" \
"buffer_load_dwordx4 v[164:167], v6, s[28:31], s59 offen offset:1536 sc1\n\t" \
"s_mov_b64 exec, -1\n\t" \
"s_add_u32 s59, s59, 2048\n\t" \
"v_exp_f32 v66, v66\n\t" \
"v_exp_f32 v67, v67\n\t" \
"v_mfma_f32_32x32x16_bf16 v[80:95], v[136:139], v[28:31], v[112:127]\n\t" \
"buffer_load_dwordx4 v[136:139], v1, s[28:31], s59 offen sc1\n\t" \
"v_exp_f32 v68, v68\n\t" \
"v_exp_f32 v69, v69\n\t" \
"v_exp_f32 v70, v70\n\t" \
"s_add_u32 s63, s60, 524288\n\t" \
"buffer_load_dwordx4 v[192:195], v11, s[36:39], s63 offen\n\t" \
"v_exp_f32 v71, v71\n\t" \
"v_exp_f32 v72, v72\n\t" \
"v_exp_f32 v73, v73\n\t" \
"s_waitcnt vmcnt(16)\n\t" \
"v_smfmac_f32_32x32x32_bf16 v[32:47], v[152:155], v[64:71], v7\n\t" \
"v_exp_f32 v74, v74\n\t" \
"v_exp_f32 v75, v75\n\t" \
"v_exp_f32 v76, v76\n\t" \
"v_exp_f32 v77, v77\n\t" \
"v_exp_f32 v78, v78\n\t" \
"v_exp_f32 v79, v79\n\t" \
"v_exp_f32 v80, v80\n\t" \
"v_exp_f32 v81, v81\n\t" \
"s_waitcnt vmcnt(15)\n\t" \
"v_smfmac_f32_32x32x32_bf16 v[32:47], v[168:171], v[72:79], v7\n\t" \
"v_exp_f32 v82, v82\n\t" \
"v_exp_f32 v83, v83\n\t" \
"s_waitcnt vmcnt(14)\n\t" \
"v_mfma_f32_32x32x16_bf16 v[64:79], v[140:143], v[24:27], v[96:111]\n\t" \
"v_exp_f32 v84, v84\n\t" \
"v_exp_f32 v85, v85\n\t" \
"v_exp_f32 v86, v86\n\t" \
"s_add_u32 m0, s62, 100352\n\t" \
"s_add_u32 s63, s61, 974848\n\t" \
"buffer_load_dwordx4 v13, s[40:43], s63 offen lds\n\t" \
"v_exp_f32 v87, v87\n\t" \
"v_exp_f32 v88, v88\n\t" \
"v_exp_f32 v89, v89\n\t" \
"v_smfmac_f32_32x32x32_bf16 v[48:63], v[152:155], v[80:87], v7\n\t" \
"s_mov_b64 exec, s[94:95]\n\t" \
"buffer_load_dwordx4 v[152:155], v6, s[28:31], s59 offen offset:1024 sc1\n\t" \
"s_mov_b64 exec, -1\n\t" \
"v_exp_f32 v90, v90\n\t" \
"v_exp_f32 v91, v91\n\t" \
"v_exp_f32 v92, v92\n\t" \
"v_exp_f32 v93, v93\n\t" \
"v_exp_f32 v94, v94\n\t" \
"v_exp_f32 v95, v95\n\t" \
"v_exp_f32 v64, v64\n\t" \
"v_exp_f32 v65, v65\n\t" \
"v_smfmac_f32_32x32x32_bf16 v[48:63], v[168:171], v[88:95], v7\n\t" \
"s_mov_b64 exec, s[94:95]\n\t" \
"buffer_load_dwordx4 v[168:171], v6, s[28:31], s59 offen offset:1536 sc1\n\t" \
"s_mov_b64 exec, -1\n\t" \
"s_add_u32 s59, s59, 2048\n\t" \
"v_exp_f32 v66, v66\n\t" \
"v_exp_f32 v67, v67\n\t" \
"v_mfma_f32_32x32x16_bf16 v[80:95], v[140:143], v[28:31], v[112:127]\n\t" \
"buffer_load_dwordx4 v[140:143], v1, s[28:31], s59 offen sc1\n\t" \
"v_exp_f32 v68, v68\n\t" \
"v_exp_f32 v69, v69\n\t" \
"v_exp_f32 v70, v70\n\t" \
"v_exp_f32 v71, v71\n\t" \
"v_exp_f32 v72, v72\n\t" \
"v_exp_f32 v73, v73\n\t" \
"s_waitcnt vmcnt(16)\n\t" \
"v_smfmac_f32_32x32x32_bf16 v[32:47], v[156:159], v[64:71], v7\n\t" \
"v_exp_f32 v74, v74\n\t" \
"v_exp_f32 v75, v75\n\t" \
"v_exp_f32 v76, v76\n\t" \
"v_exp_f32 v77, v77\n\t" \
"v_exp_f32 v78, v78\n\t" \
"v_exp_f32 v79, v79\n\t" \
"v_exp_f32 v80, v80\n\t" \
"v_exp_f32 v81, v81\n\t" \
"s_waitcnt vmcnt(15)\n\t" \
"v_smfmac_f32_32x32x32_bf16 v[32:47], v[172:175], v[72:79], v7\n\t" \
"v_exp_f32 v82, v82\n\t" \
"v_exp_f32 v83, v83\n\t" \
"s_waitcnt vmcnt(14)\n\t" \
"v_mfma_f32_32x32x16_bf16 v[64:79], v[128:131], v[24:27], v[96:111]\n\t" \
"v_exp_f32 v84, v84\n\t" \
"v_exp_f32 v85, v85\n\t" \
"v_exp_f32 v86, v86\n\t" \
"s_add_u32 s63, s60, 532480\n\t" \
"buffer_load_dwordx4 v[196:199], v11, s[36:39], s63 offen\n\t" \
"v_exp_f32 v87, v87\n\t" \
"v_exp_f32 v88, v88\n\t" \
"v_exp_f32 v89, v89\n\t" \
"v_smfmac_f32_32x32x32_bf16 v[48:63], v[156:159], v[80:87], v7\n\t" \
"s_mov_b64 exec, s[94:95]\n\t" \
"buffer_load_dwordx4 v[156:159], v6, s[28:31], s59 offen offset:1024 sc1\n\t" \
"s_mov_b64 exec, -1\n\t" \
"v_exp_f32 v90, v90\n\t" \
"v_exp_f32 v91, v91\n\t" \
"v_exp_f32 v92, v92\n\t" \
"v_exp_f32 v93, v93\n\t" \
"v_exp_f32 v94, v94\n\t" \
"v_exp_f32 v95, v95\n\t" \
"v_exp_f32 v64, v64\n\t" \
"v_exp_f32 v65, v65\n\t" \
"v_smfmac_f32_32x32x32_bf16 v[48:63], v[172:175], v[88:95], v7\n\t" \
"s_mov_b64 exec, s[94:95]\n\t" \
"buffer_load_dwordx4 v[172:175], v6, s[28:31], s59 offen offset:1536 sc1\n\t" \
"s_mov_b64 exec, -1\n\t" \
"s_add_u32 s59, s59, 2048\n\t" \
"v_exp_f32 v66, v66\n\t" \
"v_exp_f32 v67, v67\n\t" \
"v_mfma_f32_32x32x16_bf16 v[80:95], v[128:131], v[28:31], v[112:127]\n\t" \
"buffer_load_dwordx4 v[128:131], v1, s[28:31], s59 offen sc1\n\t" \
"v_exp_f32 v68, v68\n\t" \
"v_exp_f32 v69, v69\n\t" \
"v_exp_f32 v70, v70\n\t" \
"s_add_u32 s63, s60, 540672\n\t" \
"buffer_load_dwordx4 v[200:203], v11, s[36:39], s63 offen\n\t" \
"v_exp_f32 v71, v71\n\t" \
"v_exp_f32 v72, v72\n\t" \
"v_exp_f32 v73, v73\n\t" \
"s_waitcnt vmcnt(16)\n\t" \
"v_smfmac_f32_32x32x32_bf16 v[32:47], v[144:147], v[64:71], v7\n\t" \
"v_exp_f32 v74, v74\n\t" \
"v_exp_f32 v75, v75\n\t" \
"v_exp_f32 v76, v76\n\t" \
"v_exp_f32 v77, v77\n\t" \
"v_exp_f32 v78, v78\n\t" \
"v_exp_f32 v79, v79\n\t" \
"v_exp_f32 v80, v80\n\t" \
"v_exp_f32 v81, v81\n\t" \
"s_waitcnt vmcnt(15)\n\t" \
"v_smfmac_f32_32x32x32_bf16 v[32:47], v[160:163], v[72:79], v7\n\t" \
"v_exp_f32 v82, v82\n\t" \
"v_exp_f32 v83, v83\n\t" \
"s_waitcnt vmcnt(14)\n\t" \
"v_mfma_f32_32x32x16_bf16 v[64:79], v[132:135], v[24:27], v[96:111]\n\t" \
"v_exp_f32 v84, v84\n\t" \
"v_exp_f32 v85, v85\n\t" \
"v_exp_f32 v86, v86\n\t" \
"v_exp_f32 v87, v87\n\t" \
"v_exp_f32 v88, v88\n\t" \
"v_exp_f32 v89, v89\n\t" \
"v_smfmac_f32_32x32x32_bf16 v[48:63], v[144:147], v[80:87], v7\n\t" \
"s_mov_b64 exec, s[94:95]\n\t" \
"buffer_load_dwordx4 v[144:147], v6, s[28:31], s59 offen offset:1024 sc1\n\t" \
"s_mov_b64 exec, -1\n\t" \
"v_exp_f32 v90, v90\n\t" \
"v_exp_f32 v91, v91\n\t" \
"v_exp_f32 v92, v92\n\t" \
"v_exp_f32 v93, v93\n\t" \
"v_exp_f32 v94, v94\n\t" \
"v_exp_f32 v95, v95\n\t" \
"v_exp_f32 v64, v64\n\t" \
"v_exp_f32 v65, v65\n\t" \
"v_smfmac_f32_32x32x32_bf16 v[48:63], v[160:163], v[88:95], v7\n\t" \
"s_mov_b64 exec, s[94:95]\n\t" \
"buffer_load_dwordx4 v[160:163], v6, s[28:31], s59 offen offset:1536 sc1\n\t" \
"s_mov_b64 exec, -1\n\t" \
"s_add_u32 s59, s59, 2048\n\t" \
"v_exp_f32 v66, v66\n\t" \
"v_exp_f32 v67, v67\n\t" \
"v_mfma_f32_32x32x16_bf16 v[80:95], v[132:135], v[28:31], v[112:127]\n\t" \
"buffer_load_dwordx4 v[132:135], v1, s[28:31], s59 offen sc1\n\t" \
"v_exp_f32 v68, v68\n\t" \
"v_exp_f32 v69, v69\n\t" \
"v_exp_f32 v70, v70\n\t" \
"s_add_u32 s63, s60, 548864\n\t" \
"buffer_load_dwordx4 v[204:207], v11, s[36:39], s63 offen\n\t" \
"v_exp_f32 v71, v71\n\t" \
"v_exp_f32 v72, v72\n\t" \
"v_exp_f32 v73, v73\n\t" \
"s_waitcnt vmcnt(16)\n\t" \
"v_smfmac_f32_32x32x32_bf16 v[32:47], v[148:151], v[64:71], v7\n\t" \
"v_exp_f32 v74, v74\n\t" \
"v_exp_f32 v75, v75\n\t" \
"v_exp_f32 v76, v76\n\t" \
"v_exp_f32 v77, v77\n\t" \
"v_exp_f32 v78, v78\n\t" \
"v_exp_f32 v79, v79\n\t" \
"v_exp_f32 v80, v80\n\t" \
"v_exp_f32 v81, v81\n\t" \
"s_waitcnt vmcnt(15)\n\t" \
"v_smfmac_f32_32x32x32_bf16 v[32:47], v[164:167], v[72:79], v7\n\t" \
"v_exp_f32 v82, v82\n\t" \
"v_exp_f32 v83, v83\n\t" \
"s_waitcnt vmcnt(14)\n\t" \
"v_mfma_f32_32x32x16_bf16 v[64:79], v[136:139], v[24:27], v[96:111]\n\t" \
"v_exp_f32 v84, v84\n\t" \
"v_exp_f32 v85, v85\n\t" \
"v_exp_f32 v86, v86\n\t" \
"s_add_u32 m0, s62, 101376\n\t" \
"s_add_u32 s63, s61, 1003520\n\t" \
"buffer_load_dwordx4 v13, s[40:43], s63 offen lds\n\t" \
"v_exp_f32 v87, v87\n\t" \
"v_exp_f32 v88, v88\n\t" \
"v_exp_f32 v89, v89\n\t" \
"v_smfmac_f32_32x32x32_bf16 v[48:63], v[148:151], v[80:87], v7\n\t" \
"s_mov_b64 exec, s[94:95]\n\t" \
"buffer_load_dwordx4 v[148:151], v6, s[28:31], s59 offen offset:1024 sc1\n\t" \
"s_mov_b64 exec, -1\n\t" \
"v_exp_f32 v90, v90\n\t" \
"v_exp_f32 v91, v91\n\t" \
"v_exp_f32 v92, v92\n\t" \
"v_exp_f32 v93, v93\n\t" \
"v_exp_f32 v94, v94\n\t" \
"v_exp_f32 v95, v95\n\t" \
"v_exp_f32 v64, v64\n\t" \
"v_exp_f32 v65, v65\n\t" \
"v_smfmac_f32_32x32x32_bf16 v[48:63], v[164:167], v[88:95], v7\n\t" \
"s_mov_b64 exec, s[94:95]\n\t" \
"buffer_load_dwordx4 v[164:167], v6, s[28:31], s59 offen offset:1536 sc1\n\t" \
"s_mov_b64 exec, -1\n\t" \
"s_add_u32 s59, s59, 2048\n\t" \
"v_exp_f32 v66, v66\n\t" \
"v_exp_f32 v67, v67\n\t" \
"v_mfma_f32_32x32x16_bf16 v[80:95], v[136:139], v[28:31], v[112:127]\n\t" \
"buffer_load_dwordx4 v[136:139], v1, s[28:31], s59 offen sc1\n\t" \
"v_exp_f32 v68, v68\n\t" \
"v_exp_f32 v69, v69\n\t" \
"v_exp_f32 v70, v70\n\t" \
"v_exp_f32 v71, v71\n\t" \
"v_exp_f32 v72, v72\n\t" \
"v_exp_f32 v73, v73\n\t" \
"s_waitcnt vmcnt(15)\n\t" \
"v_smfmac_f32_32x32x32_bf16 v[32:47], v[152:155], v[64:71], v7\n\t" \
"v_exp_f32 v74, v74\n\t" \
"v_exp_f32 v75, v75\n\t" \
"v_exp_f32 v76, v76\n\t" \
"v_exp_f32 v77, v77\n\t" \
"v_exp_f32 v78, v78\n\t" \
"v_exp_f32 v79, v79\n\t" \
"v_exp_f32 v80, v80\n\t" \
"v_exp_f32 v81, v81\n\t" \
"s_waitcnt vmcnt(14)\n\t" \
"v_smfmac_f32_32x32x32_bf16 v[32:47], v[168:171], v[72:79], v7\n\t" \
"v_exp_f32 v82, v82\n\t" \
"v_exp_f32 v83, v83\n\t" \
"s_waitcnt vmcnt(13)\n\t" \
"v_mfma_f32_32x32x16_bf16 v[64:79], v[140:143], v[24:27], v[96:111]\n\t" \
"v_exp_f32 v84, v84\n\t" \
"v_exp_f32 v85, v85\n\t" \
"v_exp_f32 v86, v86\n\t" \
"s_add_u32 s63, s60, 1048576\n\t" \
"buffer_load_dwordx4 v[208:211], v11, s[36:39], s63 offen\n\t" \
"v_exp_f32 v87, v87\n\t" \
"v_exp_f32 v88, v88\n\t" \
"v_exp_f32 v89, v89\n\t" \
"v_smfmac_f32_32x32x32_bf16 v[48:63], v[152:155], v[80:87], v7\n\t" \
"s_mov_b64 exec, s[94:95]\n\t" \
"buffer_load_dwordx4 v[152:155], v6, s[28:31], s59 offen offset:1024 sc1\n\t" \
"s_mov_b64 exec, -1\n\t" \
"v_exp_f32 v90, v90\n\t" \
"v_exp_f32 v91, v91\n\t" \
"v_exp_f32 v92, v92\n\t" \
"v_exp_f32 v93, v93\n\t" \
"v_exp_f32 v94, v94\n\t" \
"v_exp_f32 v95, v95\n\t" \
"v_exp_f32 v64, v64\n\t" \
"v_exp_f32 v65, v65\n\t" \
"v_smfmac_f32_32x32x32_bf16 v[48:63], v[168:171], v[88:95], v7\n\t" \
"s_mov_b64 exec, s[94:95]\n\t" \
"buffer_load_dwordx4 v[168:171], v6, s[28:31], s59 offen offset:1536 sc1\n\t" \
"s_mov_b64 exec, -1\n\t" \
"s_add_u32 s59, s59, 2048\n\t" \
"v_exp_f32 v66, v66\n\t" \
"v_exp_f32 v67, v67\n\t" \
"v_mfma_f32_32x32x16_bf16 v[80:95], v[140:143], v[28:31], v[112:127]\n\t" \
"buffer_load_dwordx4 v[140:143], v1, s[28:31], s59 offen sc1\n\t" \
"v_exp_f32 v68, v68\n\t" \
"v_exp_f32 v69, v69\n\t" \
"v_exp_f32 v70, v70\n\t" \
"s_add_u32 s63, s60, 1056768\n\t" \
"buffer_load_dwordx4 v[212:215], v11, s[36:39], s63 offen\n\t" \
"v_exp_f32 v71, v71\n\t" \
"v_exp_f32 v72, v72\n\t" \
"v_exp_f32 v73, v73\n\t" \
"s_waitcnt vmcnt(16)\n\t" \
"v_smfmac_f32_32x32x32_bf16 v[32:47], v[156:159], v[64:71], v7\n\t" \
"v_exp_f32 v74, v74\n\t" \
"v_exp_f32 v75, v75\n\t" \
"v_exp_f32 v76, v76\n\t" \
"v_exp_f32 v77, v77\n\t" \
"v_exp_f32 v78, v78\n\t" \
"v_exp_f32 v79, v79\n\t" \
"v_exp_f32 v80, v80\n\t" \
"v_exp_f32 v81, v81\n\t" \
"s_waitcnt vmcnt(15)\n\t" \
"v_smfmac_f32_32x32x32_bf16 v[32:47], v[172:175], v[72:79], v7\n\t" \
"v_exp_f32 v82, v82\n\t" \
"v_exp_f32 v83, v83\n\t" \
"s_waitcnt vmcnt(14)\n\t" \
"v_mfma_f32_32x32x16_bf16 v[64:79], v[128:131], v[24:27], v[96:111]\n\t" \
"v_exp_f32 v84, v84\n\t" \
"v_exp_f32 v85, v85\n\t" \
"v_exp_f32 v86, v86\n\t" \
"s_add_u32 s63, s60, 1064960\n\t" \
"buffer_load_dwordx4 v[216:219], v11, s[36:39], s63 offen\n\t" \
"v_exp_f32 v87, v87\n\t" \
"v_exp_f32 v88, v88\n\t" \
"v_exp_f32 v89, v89\n\t" \
"v_smfmac_f32_32x32x32_bf16 v[48:63], v[156:159], v[80:87], v7\n\t" \
"s_mov_b64 exec, s[94:95]\n\t" \
"buffer_load_dwordx4 v[156:159], v6, s[28:31], s59 offen offset:1024 sc1\n\t" \
"s_mov_b64 exec, -1\n\t" \
"v_exp_f32 v90, v90\n\t" \
"v_exp_f32 v91, v91\n\t" \
"v_exp_f32 v92, v92\n\t" \
"v_exp_f32 v93, v93\n\t" \
"v_exp_f32 v94, v94\n\t" \
"v_exp_f32 v95, v95\n\t" \
"v_exp_f32 v64, v64\n\t" \
"v_exp_f32 v65, v65\n\t" \
"v_smfmac_f32_32x32x32_bf16 v[48:63], v[172:175], v[88:95], v7\n\t" \
"s_mov_b64 exec, s[94:95]\n\t" \
"buffer_load_dwordx4 v[172:175], v6, s[28:31], s59 offen offset:1536 sc1\n\t" \
"s_mov_b64 exec, -1\n\t" \
"s_add_u32 s59, s59, 2048\n\t" \
"v_exp_f32 v66, v66\n\t" \
"v_exp_f32 v67, v67\n\t" \
"v_mfma_f32_32x32x16_bf16 v[80:95], v[128:131], v[28:31], v[112:127]\n\t" \
"buffer_load_dwordx4 v[128:131], v1, s[28:31], s59 offen sc1\n\t" \
"v_exp_f32 v68, v68\n\t" \
"v_exp_f32 v69, v69\n\t" \
"v_exp_f32 v70, v70\n\t" \
"v_exp_f32 v71, v71\n\t" \
"v_exp_f32 v72, v72\n\t" \
"v_exp_f32 v73, v73\n\t" \
"s_waitcnt vmcnt(16)\n\t" \
"v_smfmac_f32_32x32x32_bf16 v[32:47], v[144:147], v[64:71], v7\n\t" \
"v_exp_f32 v74, v74\n\t" \
"v_exp_f32 v75, v75\n\t" \
"v_exp_f32 v76, v76\n\t" \
"v_exp_f32 v77, v77\n\t" \
"v_exp_f32 v78, v78\n\t" \
"v_exp_f32 v79, v79\n\t" \
"v_exp_f32 v80, v80\n\t" \
"v_exp_f32 v81, v81\n\t" \
"s_waitcnt vmcnt(15)\n\t" \
"v_smfmac_f32_32x32x32_bf16 v[32:47], v[160:163], v[72:79], v7\n\t" \
"v_exp_f32 v82, v82\n\t" \
"v_exp_f32 v83, v83\n\t" \
"s_waitcnt vmcnt(14)\n\t" \
"v_mfma_f32_32x32x16_bf16 v[64:79], v[132:135], v[24:27], v[96:111]\n\t" \
"v_exp_f32 v84, v84\n\t" \
"v_exp_f32 v85, v85\n\t" \
"v_exp_f32 v86, v86\n\t" \
"s_add_u32 m0, s62, 114688\n\t" \
"s_add_u32 s63, s61, 1376256\n\t" \
"buffer_load_dwordx4 v13, s[40:43], s63 offen lds\n\t" \
"v_exp_f32 v87, v87\n\t" \
"v_exp_f32 v88, v88\n\t" \
"v_exp_f32 v89, v89\n\t" \
"v_smfmac_f32_32x32x32_bf16 v[48:63], v[144:147], v[80:87], v7\n\t" \
"s_mov_b64 exec, s[94:95]\n\t" \
"buffer_load_dwordx4 v[144:147], v6, s[28:31], s59 offen offset:1024 sc1\n\t" \
"s_mov_b64 exec, -1\n\t" \
"v_exp_f32 v90, v90\n\t" \
"v_exp_f32 v91, v91\n\t" \
"v_exp_f32 v92, v92\n\t" \
"v_exp_f32 v93, v93\n\t" \
"v_exp_f32 v94, v94\n\t" \
"v_exp_f32 v95, v95\n\t" \
"v_exp_f32 v64, v64\n\t" \
"v_exp_f32 v65, v65\n\t" \
"v_smfmac_f32_32x32x32_bf16 v[48:63], v[160:163], v[88:95], v7\n\t" \
"s_mov_b64 exec, s[94:95]\n\t" \
"buffer_load_dwordx4 v[160:163], v6, s[28:31], s59 offen offset:1536 sc1\n\t" \
"s_mov_b64 exec, -1\n\t" \
"s_add_u32 s59, s59, 2048\n\t" \
"v_exp_f32 v66, v66\n\t" \
"v_exp_f32 v67, v67\n\t" \
"v_mfma_f32_32x32x16_bf16 v[80:95], v[132:135], v[28:31], v[112:127]\n\t" \
"buffer_load_dwordx4 v[132:135], v1, s[28:31], s59 offen sc1\n\t" \
"v_exp_f32 v68, v68\n\t" \
"v_exp_f32 v69, v69\n\t" \
"v_exp_f32 v70, v70\n\t" \
"s_add_u32 s63, s60, 1073152\n\t" \
"buffer_load_dwordx4 v[220:223], v11, s[36:39], s63 offen\n\t" \
"v_exp_f32 v71, v71\n\t" \
"v_exp_f32 v72, v72\n\t" \
"v_exp_f32 v73, v73\n\t" \
"s_waitcnt vmcnt(16)\n\t" \
"v_smfmac_f32_32x32x32_bf16 v[32:47], v[148:151], v[64:71], v7\n\t" \
"v_exp_f32 v74, v74\n\t" \
"v_exp_f32 v75, v75\n\t" \
"v_exp_f32 v76, v76\n\t" \
"v_exp_f32 v77, v77\n\t" \
"v_exp_f32 v78, v78\n\t" \
"v_exp_f32 v79, v79\n\t" \
"v_exp_f32 v80, v80\n\t" \
"v_exp_f32 v81, v81\n\t" \
"s_waitcnt vmcnt(15)\n\t" \
"v_smfmac_f32_32x32x32_bf16 v[32:47], v[164:167], v[72:79], v7\n\t" \
"v_exp_f32 v82, v82\n\t" \
"v_exp_f32 v83, v83\n\t" \
"s_waitcnt vmcnt(14)\n\t" \
"v_mfma_f32_32x32x16_bf16 v[64:79], v[136:139], v[24:27], v[96:111]\n\t" \
"v_exp_f32 v84, v84\n\t" \
"v_exp_f32 v85, v85\n\t" \
"v_exp_f32 v86, v86\n\t" \
"v_exp_f32 v87, v87\n\t" \
"v_exp_f32 v88, v88\n\t" \
"v_exp_f32 v89, v89\n\t" \
"v_smfmac_f32_32x32x32_bf16 v[48:63], v[148:151], v[80:87], v7\n\t" \
"s_mov_b64 exec, s[94:95]\n\t" \
"buffer_load_dwordx4 v[148:151], v6, s[28:31], s59 offen offset:1024 sc1\n\t" \
"s_mov_b64 exec, -1\n\t" \
"v_exp_f32 v90, v90\n\t" \
"v_exp_f32 v91, v91\n\t" \
"v_exp_f32 v92, v92\n\t" \
"v_exp_f32 v93, v93\n\t" \
"v_exp_f32 v94, v94\n\t" \
"v_exp_f32 v95, v95\n\t" \
"v_exp_f32 v64, v64\n\t" \
"v_exp_f32 v65, v65\n\t" \
"v_smfmac_f32_32x32x32_bf16 v[48:63], v[164:167], v[88:95], v7\n\t" \
"s_mov_b64 exec, s[94:95]\n\t" \
"buffer_load_dwordx4 v[164:167], v6, s[28:31], s59 offen offset:1536 sc1\n\t" \
"s_mov_b64 exec, -1\n\t" \
"s_add_u32 s59, s59, 2048\n\t" \
"v_exp_f32 v66, v66\n\t" \
"v_exp_f32 v67, v67\n\t" \
"v_mfma_f32_32x32x16_bf16 v[80:95], v[136:139], v[28:31], v[112:127]\n\t" \
"buffer_load_dwordx4 v[136:139], v1, s[28:31], s59 offen sc1\n\t" \
"v_exp_f32 v68, v68\n\t" \
"v_exp_f32 v69, v69\n\t" \
"v_exp_f32 v70, v70\n\t" \
"s_add_u32 s63, s66, 1572864\n\t" \
"buffer_load_dwordx4 v[224:227], v11, s[36:39], s63 offen\n\t" \
"v_exp_f32 v71, v71\n\t" \
"v_exp_f32 v72, v72\n\t" \
"v_exp_f32 v73, v73\n\t" \
"s_waitcnt vmcnt(16)\n\t" \
"v_smfmac_f32_32x32x32_bf16 v[32:47], v[152:155], v[64:71], v7\n\t" \
"v_exp_f32 v74, v74\n\t" \
"v_exp_f32 v75, v75\n\t" \
"v_exp_f32 v76, v76\n\t" \
"v_exp_f32 v77, v77\n\t" \
"v_exp_f32 v78, v78\n\t" \
"v_exp_f32 v79, v79\n\t" \
"v_exp_f32 v80, v80\n\t" \
"v_exp_f32 v81, v81\n\t" \
"s_waitcnt vmcnt(15)\n\t" \
"v_smfmac_f32_32x32x32_bf16 v[32:47], v[168:171], v[72:79], v7\n\t" \
"v_exp_f32 v82, v82\n\t" \
"v_exp_f32 v83, v83\n\t" \
"s_waitcnt vmcnt(14)\n\t" \
"v_mfma_f32_32x32x16_bf16 v[64:79], v[140:143], v[24:27], v[96:111]\n\t" \
"v_exp_f32 v84, v84\n\t" \
"v_exp_f32 v85, v85\n\t" \
"v_exp_f32 v86, v86\n\t" \
"s_add_u32 m0, s62, 115712\n\t" \
"s_add_u32 s63, s61, 1404928\n\t" \
"buffer_load_dwordx4 v13, s[40:43], s63 offen lds\n\t" \
"v_exp_f32 v87, v87\n\t" \
"v_exp_f32 v88, v88\n\t" \
"v_exp_f32 v89, v89\n\t" \
"v_smfmac_f32_32x32x32_bf16 v[48:63], v[152:155], v[80:87], v7\n\t" \
"s_mov_b64 exec, s[94:95]\n\t" \
"buffer_load_dwordx4 v[152:155], v6, s[28:31], s59 offen offset:1024 sc1\n\t" \
"s_mov_b64 exec, -1\n\t" \
"v_exp_f32 v90, v90\n\t" \
"v_exp_f32 v91, v91\n\t" \
"v_exp_f32 v92, v92\n\t" \
"v_exp_f32 v93, v93\n\t" \
"v_exp_f32 v94, v94\n\t" \
"v_exp_f32 v95, v95\n\t" \
"v_exp_f32 v64, v64\n\t" \
"v_exp_f32 v65, v65\n\t" \
"v_smfmac_f32_32x32x32_bf16 v[48:63], v[168:171], v[88:95], v7\n\t" \
"s_mov_b64 exec, s[94:95]\n\t" \
"buffer_load_dwordx4 v[168:171], v6, s[28:31], s59 offen offset:1536 sc1\n\t" \
"s_mov_b64 exec, -1\n\t" \
"s_add_u32 s59, s59, 2048\n\t" \
"v_exp_f32 v66, v66\n\t" \
"v_exp_f32 v67, v67\n\t" \
"v_mfma_f32_32x32x16_bf16 v[80:95], v[140:143], v[28:31], v[112:127]\n\t" \
"buffer_load_dwordx4 v[140:143], v1, s[28:31], s59 offen sc1\n\t" \
"v_exp_f32 v68, v68\n\t" \
"v_exp_f32 v69, v69\n\t" \
"v_exp_f32 v70, v70\n\t" \
"v_exp_f32 v71, v71\n\t" \
"v_exp_f32 v72, v72\n\t" \
"v_exp_f32 v73, v73\n\t" \
"s_waitcnt vmcnt(15)\n\t" \
"v_smfmac_f32_32x32x32_bf16 v[32:47], v[156:159], v[64:71], v7\n\t" \
"v_exp_f32 v74, v74\n\t" \
"v_exp_f32 v75, v75\n\t" \
"v_exp_f32 v76, v76\n\t" \
"v_exp_f32 v77, v77\n\t" \
"v_exp_f32 v78, v78\n\t" \
"v_exp_f32 v79, v79\n\t" \
"v_exp_f32 v80, v80\n\t" \
"v_exp_f32 v81, v81\n\t" \
"s_waitcnt vmcnt(14)\n\t" \
"v_smfmac_f32_32x32x32_bf16 v[32:47], v[172:175], v[72:79], v7\n\t" \
"v_exp_f32 v82, v82\n\t" \
"v_exp_f32 v83, v83\n\t" \
"s_waitcnt vmcnt(13)\n\t" \
"v_mfma_f32_32x32x16_bf16 v[64:79], v[128:131], v[24:27], v[96:111]\n\t" \
"v_exp_f32 v84, v84\n\t" \
"v_exp_f32 v85, v85\n\t" \
"v_exp_f32 v86, v86\n\t" \
"s_add_u32 s63, s66, 1581056\n\t" \
"buffer_load_dwordx4 v[228:231], v11, s[36:39], s63 offen\n\t" \
"v_exp_f32 v87, v87\n\t" \
"v_exp_f32 v88, v88\n\t" \
"v_exp_f32 v89, v89\n\t" \
"v_smfmac_f32_32x32x32_bf16 v[48:63], v[156:159], v[80:87], v7\n\t" \
"s_mov_b64 exec, s[94:95]\n\t" \
"buffer_load_dwordx4 v[156:159], v6, s[28:31], s59 offen offset:1024 sc1\n\t" \
"s_mov_b64 exec, -1\n\t" \
"v_exp_f32 v90, v90\n\t" \
"v_exp_f32 v91, v91\n\t" \
"v_exp_f32 v92, v92\n\t" \
"v_exp_f32 v93, v93\n\t" \
"v_exp_f32 v94, v94\n\t" \
"v_exp_f32 v95, v95\n\t" \
"v_exp_f32 v64, v64\n\t" \
"v_exp_f32 v65, v65\n\t" \
"v_smfmac_f32_32x32x32_bf16 v[48:63], v[172:175], v[88:95], v7\n\t" \
"s_mov_b64 exec, s[94:95]\n\t" \
"buffer_load_dwordx4 v[172:175], v6, s[28:31], s59 offen offset:1536 sc1\n\t" \
"s_mov_b64 exec, -1\n\t" \
"s_add_u32 s59, s59, 2048\n\t" \
"v_exp_f32 v66, v66\n\t" \
"v_exp_f32 v67, v67\n\t" \
"v_mfma_f32_32x32x16_bf16 v[80:95], v[128:131], v[28:31], v[112:127]\n\t" \
"buffer_load_dwordx4 v[128:131], v1, s[28:31], s59 offen sc1\n\t" \
"v_exp_f32 v68, v68\n\t" \
"v_exp_f32 v69, v69\n\t" \
"v_exp_f32 v70, v70\n\t" \
"s_add_u32 s63, s66, 1589248\n\t" \
"buffer_load_dwordx4 v[232:235], v11, s[36:39], s63 offen\n\t" \
"v_exp_f32 v71, v71\n\t" \
"v_exp_f32 v72, v72\n\t" \
"v_exp_f32 v73, v73\n\t" \
"s_waitcnt vmcnt(16)\n\t" \
"v_smfmac_f32_32x32x32_bf16 v[32:47], v[144:147], v[64:71], v7\n\t" \
"v_exp_f32 v74, v74\n\t" \
"v_exp_f32 v75, v75\n\t" \
"v_exp_f32 v76, v76\n\t" \
"v_exp_f32 v77, v77\n\t" \
"v_exp_f32 v78, v78\n\t" \
"v_exp_f32 v79, v79\n\t" \
"v_exp_f32 v80, v80\n\t" \
"v_exp_f32 v81, v81\n\t" \
"s_waitcnt vmcnt(15)\n\t" \
"v_smfmac_f32_32x32x32_bf16 v[32:47], v[160:163], v[72:79], v7\n\t" \
"v_exp_f32 v82, v82\n\t" \
"v_exp_f32 v83, v83\n\t" \
"s_waitcnt vmcnt(14)\n\t" \
"v_mfma_f32_32x32x16_bf16 v[64:79], v[132:135], v[24:27], v[96:111]\n\t" \
"v_exp_f32 v84, v84\n\t" \
"v_exp_f32 v85, v85\n\t" \
"v_exp_f32 v86, v86\n\t" \
"s_add_u32 s63, s66, 1597440\n\t" \
"buffer_load_dwordx4 v[236:239], v11, s[36:39], s63 offen\n\t" \
"v_exp_f32 v87, v87\n\t" \
"v_exp_f32 v88, v88\n\t" \
"v_exp_f32 v89, v89\n\t" \
"v_smfmac_f32_32x32x32_bf16 v[48:63], v[144:147], v[80:87], v7\n\t" \
"s_mov_b64 exec, s[94:95]\n\t" \
"buffer_load_dwordx4 v[144:147], v6, s[28:31], s59 offen offset:1024 sc1\n\t" \
"s_mov_b64 exec, -1\n\t" \
"v_exp_f32 v90, v90\n\t" \
"v_exp_f32 v91, v91\n\t" \
"v_exp_f32 v92, v92\n\t" \
"v_exp_f32 v93, v93\n\t" \
"v_exp_f32 v94, v94\n\t" \
"v_exp_f32 v95, v95\n\t" \
"v_exp_f32 v64, v64\n\t" \
"v_exp_f32 v65, v65\n\t" \
"v_smfmac_f32_32x32x32_bf16 v[48:63], v[160:163], v[88:95], v7\n\t" \
"s_mov_b64 exec, s[94:95]\n\t" \
"buffer_load_dwordx4 v[160:163], v6, s[28:31], s59 offen offset:1536 sc1\n\t" \
"s_mov_b64 exec, -1\n\t" \
"s_add_u32 s59, s59, 2048\n\t" \
"v_exp_f32 v66, v66\n\t" \
"v_exp_f32 v67, v67\n\t" \
"v_mfma_f32_32x32x16_bf16 v[80:95], v[132:135], v[28:31], v[112:127]\n\t" \
"buffer_load_dwordx4 v[132:135], v1, s[28:31], s59 offen sc1\n\t" \
"v_exp_f32 v68, v68\n\t" \
"v_exp_f32 v69, v69\n\t" \
"v_exp_f32 v70, v70\n\t" \
"v_exp_f32 v71, v71\n\t" \
"v_exp_f32 v72, v72\n\t" \
"v_exp_f32 v73, v73\n\t" \
"s_waitcnt vmcnt(16)\n\t" \
"v_smfmac_f32_32x32x32_bf16 v[32:47], v[148:151], v[64:71], v7\n\t" \
"v_exp_f32 v74, v74\n\t" \
"v_exp_f32 v75, v75\n\t" \
"v_exp_f32 v76, v76\n\t" \
"v_exp_f32 v77, v77\n\t" \
"v_exp_f32 v78, v78\n\t" \
"v_exp_f32 v79, v79\n\t" \
"v_exp_f32 v80, v80\n\t" \
"v_exp_f32 v81, v81\n\t" \
"s_waitcnt vmcnt(15)\n\t" \
"v_smfmac_f32_32x32x32_bf16 v[32:47], v[164:167], v[72:79], v7\n\t" \
"v_exp_f32 v82, v82\n\t" \
"v_exp_f32 v83, v83\n\t" \
"s_waitcnt vmcnt(14)\n\t" \
"v_mfma_f32_32x32x16_bf16 v[64:79], v[136:139], v[24:27], v[96:111]\n\t" \
"v_exp_f32 v84, v84\n\t" \
"v_exp_f32 v85, v85\n\t" \
"v_exp_f32 v86, v86\n\t" \
"s_add_u32 m0, s62, 116736\n\t" \
"s_add_u32 s63, s61, 1433600\n\t" \
"buffer_load_dwordx4 v13, s[40:43], s63 offen lds\n\t" \
"v_exp_f32 v87, v87\n\t" \
"v_exp_f32 v88, v88\n\t" \
"v_exp_f32 v89, v89\n\t" \
"v_smfmac_f32_32x32x32_bf16 v[48:63], v[148:151], v[80:87], v7\n\t" \
"s_mov_b64 exec, s[94:95]\n\t" \
"buffer_load_dwordx4 v[148:151], v6, s[28:31], s59 offen offset:1024 sc1\n\t" \
"s_mov_b64 exec, -1\n\t" \
"v_exp_f32 v90, v90\n\t" \
"v_exp_f32 v91, v91\n\t" \
"v_exp_f32 v92, v92\n\t" \
"v_exp_f32 v93, v93\n\t" \
"v_exp_f32 v94, v94\n\t" \
"v_exp_f32 v95, v95\n\t" \
"v_exp_f32 v64, v64\n\t" \
"v_exp_f32 v65, v65\n\t" \
"v_smfmac_f32_32x32x32_bf16 v[48:63], v[164:167], v[88:95], v7\n\t" \
"s_mov_b64 exec, s[94:95]\n\t" \
"buffer_load_dwordx4 v[164:167], v6, s[28:31], s59 offen offset:1536 sc1\n\t" \
"s_mov_b64 exec, -1\n\t" \
"s_add_u32 s59, s59, 2048\n\t" \
"v_exp_f32 v66, v66\n\t" \
"v_exp_f32 v67, v67\n\t" \
"v_mfma_f32_32x32x16_bf16 v[80:95], v[136:139], v[28:31], v[112:127]\n\t" \
"buffer_load_dwordx4 v[136:139], v1, s[28:31], s59 offen sc1\n\t" \
"v_exp_f32 v68, v68\n\t" \
"v_exp_f32 v69, v69\n\t" \
"v_exp_f32 v70, v70\n\t" \
"s_add_u32 s63, s60, 294912\n\t" \
"buffer_load_dwordx4 v[240:243], v11, s[36:39], s63 offen\n\t" \
"v_exp_f32 v71, v71\n\t" \
"v_exp_f32 v72, v72\n\t" \
"v_exp_f32 v73, v73\n\t" \
"s_waitcnt vmcnt(16)\n\t" \
"v_smfmac_f32_32x32x32_bf16 v[32:47], v[152:155], v[64:71], v7\n\t" \
"v_exp_f32 v74, v74\n\t" \
"v_exp_f32 v75, v75\n\t" \
"v_exp_f32 v76, v76\n\t" \
"v_exp_f32 v77, v77\n\t" \
"v_exp_f32 v78, v78\n\t" \
"v_exp_f32 v79, v79\n\t" \
"v_exp_f32 v80, v80\n\t" \
"v_exp_f32 v81, v81\n\t" \
"s_waitcnt vmcnt(15)\n\t" \
"v_smfmac_f32_32x32x32_bf16 v[32:47], v[168:171], v[72:79], v7\n\t" \
"v_exp_f32 v82, v82\n\t" \
"v_exp_f32 v83, v83\n\t" \
"s_waitcnt vmcnt(14)\n\t" \
"v_mfma_f32_32x32x16_bf16 v[64:79], v[140:143], v[24:27], v[96:111]\n\t" \
"v_exp_f32 v84, v84\n\t" \
"v_exp_f32 v85, v85\n\t" \
"v_exp_f32 v86, v86\n\t" \
"v_exp_f32 v87, v87\n\t" \
"v_exp_f32 v88, v88\n\t" \
"v_exp_f32 v89, v89\n\t" \
"v_smfmac_f32_32x32x32_bf16 v[48:63], v[152:155], v[80:87], v7\n\t" \
"s_mov_b64 exec, s[94:95]\n\t" \
"buffer_load_dwordx4 v[152:155], v6, s[28:31], s59 offen offset:1024 sc1\n\t" \
"s_mov_b64 exec, -1\n\t" \
"v_exp_f32 v90, v90\n\t" \
"v_exp_f32 v91, v91\n\t" \
"v_exp_f32 v92, v92\n\t" \
"v_exp_f32 v93, v93\n\t" \
"v_exp_f32 v94, v94\n\t" \
"v_exp_f32 v95, v95\n\t" \
"v_exp_f32 v64, v64\n\t" \
"v_exp_f32 v65, v65\n\t" \
"v_smfmac_f32_32x32x32_bf16 v[48:63], v[168:171], v[88:95], v7\n\t" \
"s_mov_b64 exec, s[94:95]\n\t" \
"buffer_load_dwordx4 v[168:171], v6, s[28:31], s59 offen offset:1536 sc1\n\t" \
"s_mov_b64 exec, -1\n\t" \
"s_add_u32 s59, s59, 2048\n\t" \
"v_exp_f32 v66, v66\n\t" \
"v_exp_f32 v67, v67\n\t" \
"v_mfma_f32_32x32x16_bf16 v[80:95], v[140:143], v[28:31], v[112:127]\n\t" \
"buffer_load_dwordx4 v[140:143], v1, s[28:31], s59 offen sc1\n\t" \
"v_exp_f32 v68, v68\n\t" \
"v_exp_f32 v69, v69\n\t" \
"v_exp_f32 v70, v70\n\t" \
"s_add_u32 s63, s60, 303104\n\t" \
"buffer_load_dwordx4 v[244:247], v11, s[36:39], s63 offen\n\t" \
"v_exp_f32 v71, v71\n\t" \
"v_exp_f32 v72, v72\n\t" \
"v_exp_f32 v73, v73\n\t" \
"s_waitcnt vmcnt(16)\n\t" \
"v_smfmac_f32_32x32x32_bf16 v[32:47], v[156:159], v[64:71], v7\n\t" \
"v_exp_f32 v74, v74\n\t" \
"v_exp_f32 v75, v75\n\t" \
"v_exp_f32 v76, v76\n\t" \
"v_exp_f32 v77, v77\n\t" \
"v_exp_f32 v78, v78\n\t" \
"v_exp_f32 v79, v79\n\t" \
"v_exp_f32 v80, v80\n\t" \
"v_exp_f32 v81, v81\n\t" \
"s_waitcnt vmcnt(15)\n\t" \
"v_smfmac_f32_32x32x32_bf16 v[32:47], v[172:175], v[72:79], v7\n\t" \
"v_exp_f32 v82, v82\n\t" \
"v_exp_f32 v83, v83\n\t" \
"s_waitcnt vmcnt(14)\n\t" \
"v_mfma_f32_32x32x16_bf16 v[64:79], v[128:131], v[24:27], v[96:111]\n\t" \
"v_exp_f32 v84, v84\n\t" \
"v_exp_f32 v85, v85\n\t" \
"v_exp_f32 v86, v86\n\t" \
"s_add_u32 s63, s60, 311296\n\t" \
"buffer_load_dwordx4 v[248:251], v11, s[36:39], s63 offen\n\t" \
"v_exp_f32 v87, v87\n\t" \
"v_exp_f32 v88, v88\n\t" \
"v_exp_f32 v89, v89\n\t" \
"v_smfmac_f32_32x32x32_bf16 v[48:63], v[156:159], v[80:87], v7\n\t" \
"s_mov_b64 exec, s[94:95]\n\t" \
"buffer_load_dwordx4 v[156:159], v6, s[28:31], s59 offen offset:1024 sc1\n\t" \
"s_mov_b64 exec, -1\n\t" \
"v_exp_f32 v90, v90\n\t" \
"v_exp_f32 v91, v91\n\t" \
"v_exp_f32 v92, v92\n\t" \
"v_exp_f32 v93, v93\n\t" \
"v_exp_f32 v94, v94\n\t" \
"v_exp_f32 v95, v95\n\t" \
"v_exp_f32 v64, v64\n\t" \
"v_exp_f32 v65, v65\n\t" \
"v_smfmac_f32_32x32x32_bf16 v[48:63], v[172:175], v[88:95], v7\n\t" \
"s_mov_b64 exec, s[94:95]\n\t" \
"buffer_load_dwordx4 v[172:175], v6, s[28:31], s59 offen offset:1536 sc1\n\t" \
"s_mov_b64 exec, -1\n\t" \
"s_add_u32 s59, s59, 2048\n\t" \
"v_exp_f32 v66, v66\n\t" \
"v_exp_f32 v67, v67\n\t" \
"v_mfma_f32_32x32x16_bf16 v[80:95], v[128:131], v[28:31], v[112:127]\n\t" \
"buffer_load_dwordx4 v[128:131], v1, s[28:31], s59 offen sc1\n\t" \
"v_exp_f32 v68, v68\n\t" \
"v_exp_f32 v69, v69\n\t" \
"v_exp_f32 v70, v70\n\t" \
"v_exp_f32 v71, v71\n\t" \
"v_exp_f32 v72, v72\n\t" \
"v_exp_f32 v73, v73\n\t" \
"s_waitcnt vmcnt(15)\n\t" \
"v_smfmac_f32_32x32x32_bf16 v[32:47], v[144:147], v[64:71], v7\n\t" \
"v_exp_f32 v74, v74\n\t" \
"v_exp_f32 v75, v75\n\t" \
"v_exp_f32 v76, v76\n\t" \
"v_exp_f32 v77, v77\n\t" \
"v_exp_f32 v78, v78\n\t" \
"v_exp_f32 v79, v79\n\t" \
"v_exp_f32 v80, v80\n\t" \
"v_exp_f32 v81, v81\n\t" \
"s_waitcnt vmcnt(14)\n\t" \
"v_smfmac_f32_32x32x32_bf16 v[32:47], v[160:163], v[72:79], v7\n\t" \
"v_exp_f32 v82, v82\n\t" \
"v_exp_f32 v83, v83\n\t" \
"s_waitcnt vmcnt(13)\n\t" \
"v_mfma_f32_32x32x16_bf16 v[64:79], v[132:135], v[24:27], v[96:111]\n\t" \
"v_exp_f32 v84, v84\n\t" \
"v_exp_f32 v85, v85\n\t" \
"v_exp_f32 v86, v86\n\t" \
"s_add_u32 m0, s62, 117760\n\t" \
"s_add_u32 s63, s61, 1462272\n\t" \
"buffer_load_dwordx4 v13, s[40:43], s63 offen lds\n\t" \
"v_exp_f32 v87, v87\n\t" \
"v_exp_f32 v88, v88\n\t" \
"v_exp_f32 v89, v89\n\t" \
"v_smfmac_f32_32x32x32_bf16 v[48:63], v[144:147], v[80:87], v7\n\t" \
"s_mov_b64 exec, s[94:95]\n\t" \
"buffer_load_dwordx4 v[144:147], v6, s[28:31], s59 offen offset:1024 sc1\n\t" \
"s_mov_b64 exec, -1\n\t" \
"v_exp_f32 v90, v90\n\t" \
"v_exp_f32 v91, v91\n\t" \
"v_exp_f32 v92, v92\n\t" \
"v_exp_f32 v93, v93\n\t" \
"v_exp_f32 v94, v94\n\t" \
"v_exp_f32 v95, v95\n\t" \
"v_exp_f32 v64, v64\n\t" \
"v_exp_f32 v65, v65\n\t" \
"v_smfmac_f32_32x32x32_bf16 v[48:63], v[160:163], v[88:95], v7\n\t" \
"s_mov_b64 exec, s[94:95]\n\t" \
"buffer_load_dwordx4 v[160:163], v6, s[28:31], s59 offen offset:1536 sc1\n\t" \
"s_mov_b64 exec, -1\n\t" \
"s_add_u32 s59, s59, 2048\n\t" \
"v_exp_f32 v66, v66\n\t" \
"v_exp_f32 v67, v67\n\t" \
"v_mfma_f32_32x32x16_bf16 v[80:95], v[132:135], v[28:31], v[112:127]\n\t" \
"v_exp_f32 v68, v68\n\t" \
"v_exp_f32 v69, v69\n\t" \
"v_exp_f32 v70, v70\n\t" \
"s_add_u32 s63, s60, 319488\n\t" \
"buffer_load_dwordx4 v[252:255], v11, s[36:39], s63 offen\n\t" \
"v_exp_f32 v71, v71\n\t" \
"v_exp_f32 v72, v72\n\t" \
"v_exp_f32 v73, v73\n\t" \
"s_waitcnt vmcnt(15)\n\t" \
"v_smfmac_f32_32x32x32_bf16 v[32:47], v[148:151], v[64:71], v7\n\t" \
"v_exp_f32 v74, v74\n\t" \
"v_exp_f32 v75, v75\n\t" \
"v_exp_f32 v76, v76\n\t" \
"v_exp_f32 v77, v77\n\t" \
"v_exp_f32 v78, v78\n\t" \
"v_exp_f32 v79, v79\n\t" \
"v_exp_f32 v80, v80\n\t" \
"v_exp_f32 v81, v81\n\t" \
"s_waitcnt vmcnt(14)\n\t" \
"v_smfmac_f32_32x32x32_bf16 v[32:47], v[164:167], v[72:79], v7\n\t" \
"v_exp_f32 v82, v82\n\t" \
"v_exp_f32 v83, v83\n\t" \
"s_waitcnt vmcnt(13)\n\t" \
"v_mfma_f32_32x32x16_bf16 v[64:79], v[136:139], v[24:27], v[96:111]\n\t" \
"v_exp_f32 v84, v84\n\t" \
"v_exp_f32 v85, v85\n\t" \
"v_exp_f32 v86, v86\n\t" \
"v_exp_f32 v87, v87\n\t" \
"v_exp_f32 v88, v88\n\t" \
"v_exp_f32 v89, v89\n\t" \
"v_smfmac_f32_32x32x32_bf16 v[48:63], v[148:151], v[80:87], v7\n\t" \
"v_exp_f32 v90, v90\n\t" \
"v_exp_f32 v91, v91\n\t" \
"v_exp_f32 v92, v92\n\t" \
"v_exp_f32 v93, v93\n\t" \
"v_exp_f32 v94, v94\n\t" \
"v_exp_f32 v95, v95\n\t" \
"v_exp_f32 v64, v64\n\t" \
"v_exp_f32 v65, v65\n\t" \
"v_smfmac_f32_32x32x32_bf16 v[48:63], v[164:167], v[88:95], v7\n\t" \
"v_exp_f32 v66, v66\n\t" \
"v_exp_f32 v67, v67\n\t" \
"v_mfma_f32_32x32x16_bf16 v[80:95], v[136:139], v[28:31], v[112:127]\n\t" \
"v_exp_f32 v68, v68\n\t" \
"v_exp_f32 v69, v69\n\t" \
"v_exp_f32 v70, v70\n\t" \
"v_exp_f32 v71, v71\n\t" \
"v_exp_f32 v72, v72\n\t" \
"v_exp_f32 v73, v73\n\t" \
"s_waitcnt vmcnt(11)\n\t" \
"v_smfmac_f32_32x32x32_bf16 v[32:47], v[152:155], v[64:71], v7\n\t" \
"v_exp_f32 v74, v74\n\t" \
"v_exp_f32 v75, v75\n\t" \
"v_exp_f32 v76, v76\n\t" \
"v_exp_f32 v77, v77\n\t" \
"v_exp_f32 v78, v78\n\t" \
"v_exp_f32 v79, v79\n\t" \
"v_exp_f32 v80, v80\n\t" \
"v_exp_f32 v81, v81\n\t" \
"s_waitcnt vmcnt(10)\n\t" \
"v_smfmac_f32_32x32x32_bf16 v[32:47], v[168:171], v[72:79], v7\n\t" \
"v_exp_f32 v82, v82\n\t" \
"v_exp_f32 v83, v83\n\t" \
"s_waitcnt vmcnt(9)\n\t" \
"v_mfma_f32_32x32x16_bf16 v[64:79], v[140:143], v[24:27], v[96:111]\n\t" \
"v_exp_f32 v84, v84\n\t" \
"v_exp_f32 v85, v85\n\t" \
"v_exp_f32 v86, v86\n\t" \
"s_mul_i32 s57, s26, 2560\n\t" \
"s_add_u32 s57, s57, 131072\n\t" \
"v_add_u32 v14, s57, v1\n\t" \
"v_lshlrev_b32 v15, 3, v0\n\t" \
"v_add_u32 v15, s57, v15\n\t" \
"v_exp_f32 v87, v87\n\t" \
"v_exp_f32 v88, v88\n\t" \
"v_exp_f32 v89, v89\n\t" \
"v_smfmac_f32_32x32x32_bf16 v[48:63], v[152:155], v[80:87], v7\n\t" \
"v_exp_f32 v90, v90\n\t" \
"v_exp_f32 v91, v91\n\t" \
"v_exp_f32 v92, v92\n\t" \
"v_exp_f32 v93, v93\n\t" \
"v_exp_f32 v94, v94\n\t" \
"v_exp_f32 v95, v95\n\t" \
"v_exp_f32 v64, v64\n\t" \
"v_exp_f32 v65, v65\n\t" \
"v_smfmac_f32_32x32x32_bf16 v[48:63], v[168:171], v[88:95], v7\n\t" \
"v_exp_f32 v66, v66\n\t" \
"v_exp_f32 v67, v67\n\t" \
"v_mfma_f32_32x32x16_bf16 v[80:95], v[140:143], v[28:31], v[112:127]\n\t" \
"v_exp_f32 v68, v68\n\t" \
"v_exp_f32 v69, v69\n\t" \
"v_exp_f32 v70, v70\n\t" \
"s_xor_b32 s58, s26, 4\n\t" \
"s_mul_i32 s58, s58, 2560\n\t" \
"s_add_u32 s58, s58, 131072\n\t" \
"s_sub_u32 s58, s58, s57\n\t" \
"v_add_u32 v16, s58, v14\n\t" \
"v_exp_f32 v71, v71\n\t" \
"v_exp_f32 v72, v72\n\t" \
"v_exp_f32 v73, v73\n\t" \
"s_waitcnt vmcnt(6)\n\t" \
"v_smfmac_f32_32x32x32_bf16 v[32:47], v[156:159], v[64:71], v7\n\t" \
"v_exp_f32 v74, v74\n\t" \
"v_exp_f32 v75, v75\n\t" \
"v_exp_f32 v76, v76\n\t" \
"v_exp_f32 v77, v77\n\t" \
"v_exp_f32 v78, v78\n\t" \
"v_exp_f32 v79, v79\n\t" \
"v_exp_f32 v80, v80\n\t" \
"v_exp_f32 v81, v81\n\t" \
"s_waitcnt vmcnt(5)\n\t" \
"v_smfmac_f32_32x32x32_bf16 v[32:47], v[172:175], v[72:79], v7\n\t" \
"v_exp_f32 v82, v82\n\t" \
"v_exp_f32 v83, v83\n\t" \
"s_waitcnt vmcnt(4)\n\t" \
"v_mfma_f32_32x32x16_bf16 v[64:79], v[128:131], v[24:27], v[96:111]\n\t" \
"v_exp_f32 v84, v84\n\t" \
"v_exp_f32 v85, v85\n\t" \
"v_exp_f32 v86, v86\n\t" \
"v_add_u32 v17, s58, v15\n\t" \
"s_lshl_b32 s57, s47, 1\n\t" \
"s_lshr_b32 s58, s26, 2\n\t" \
"s_add_u32 s57, s57, s58\n\t" \
"s_lshl_b32 s57, s57, 5\n\t" \
"v_exp_f32 v87, v87\n\t" \
"v_exp_f32 v88, v88\n\t" \
"v_exp_f32 v89, v89\n\t" \
"v_smfmac_f32_32x32x32_bf16 v[48:63], v[156:159], v[80:87], v7\n\t" \
"v_exp_f32 v90, v90\n\t" \
"v_exp_f32 v91, v91\n\t" \
"v_exp_f32 v92, v92\n\t" \
"v_exp_f32 v93, v93\n\t" \
"v_exp_f32 v94, v94\n\t" \
"v_exp_f32 v95, v95\n\t" \
"v_exp_f32 v64, v64\n\t" \
"v_exp_f32 v65, v65\n\t" \
"v_smfmac_f32_32x32x32_bf16 v[48:63], v[172:175], v[88:95], v7\n\t" \
"v_exp_f32 v66, v66\n\t" \
"v_exp_f32 v67, v67\n\t" \
"v_mfma_f32_32x32x16_bf16 v[80:95], v[128:131], v[28:31], v[112:127]\n\t" \
"v_exp_f32 v68, v68\n\t" \
"v_exp_f32 v69, v69\n\t" \
"v_exp_f32 v70, v70\n\t" \
"s_lshl_b32 s58, s46, 2\n\t" \
"s_add_u32 s57, s57, s58\n\t" \
"s_add_u32 s57, s57, s56\n\t" \
"s_lshl_b32 s57, s57, 10\n\t" \
"s_add_u32 s92, s57, 5242880\n\t" \
"v_exp_f32 v71, v71\n\t" \
"v_exp_f32 v72, v72\n\t" \
"v_exp_f32 v73, v73\n\t" \
"s_waitcnt vmcnt(2)\n\t" \
"v_smfmac_f32_32x32x32_bf16 v[32:47], v[144:147], v[64:71], v7\n\t" \
"v_exp_f32 v74, v74\n\t" \
"v_exp_f32 v75, v75\n\t" \
"v_exp_f32 v76, v76\n\t" \
"v_exp_f32 v77, v77\n\t" \
"v_exp_f32 v78, v78\n\t" \
"v_exp_f32 v79, v79\n\t" \
"v_exp_f32 v80, v80\n\t" \
"v_exp_f32 v81, v81\n\t" \
"s_waitcnt vmcnt(1)\n\t" \
"v_smfmac_f32_32x32x32_bf16 v[32:47], v[160:163], v[72:79], v7\n\t" \
"v_exp_f32 v82, v82\n\t" \
"v_exp_f32 v83, v83\n\t" \
"v_exp_f32 v84, v84\n\t" \
"v_exp_f32 v85, v85\n\t" \
"v_exp_f32 v86, v86\n\t" \
"v_exp_f32 v87, v87\n\t" \
"v_exp_f32 v88, v88\n\t" \
"v_exp_f32 v89, v89\n\t" \
"v_smfmac_f32_32x32x32_bf16 v[48:63], v[144:147], v[80:87], v7\n\t" \
"v_exp_f32 v90, v90\n\t" \
"v_exp_f32 v91, v91\n\t" \
"v_exp_f32 v92, v92\n\t" \
"v_exp_f32 v93, v93\n\t" \
"v_exp_f32 v94, v94\n\t" \
"v_exp_f32 v95, v95\n\t" \
"v_mov_b32 v41, v8\n\t" \
"ds_write_b128 v14, v[32:35]\n\t" \
"ds_write_b128 v14, v[36:39] offset:1024\n\t" \
"ds_write_b64 v15, v[40:41] offset:2048\n\t" \
"v_smfmac_f32_32x32x32_bf16 v[48:63], v[160:163], v[88:95], v7\n\t" \
"s_branch Latte_%=\n\t" \
"Lattb_%=:\n\t" \
"s_setprio 1\n\t" \
"buffer_load_dwordx4 v[128:131], v1, s[28:31], s59 offen sc1\n\t" \
"ds_read_b128 v[24:27], v12\n\t" \
"ds_read_b128 v[28:31], v10\n\t" \
"s_mov_b64 exec, s[94:95]\n\t" \
"buffer_load_dwordx4 v[144:147], v6, s[28:31], s59 offen offset:1024 sc1\n\t" \
"buffer_load_dwordx4 v[160:163], v6, s[28:31], s59 offen offset:1536 sc1\n\t" \
"s_mov_b64 exec, -1\n\t" \
"s_add_u32 s59, s59, 2048\n\t" \
"buffer_load_dwordx4 v[132:135], v1, s[28:31], s59 offen sc1\n\t" \
"s_mov_b64 exec, s[94:95]\n\t" \
"buffer_load_dwordx4 v[148:151], v6, s[28:31], s59 offen offset:1024 sc1\n\t" \
"buffer_load_dwordx4 v[164:167], v6, s[28:31], s59 offen offset:1536 sc1\n\t" \
"s_mov_b64 exec, -1\n\t" \
"s_add_u32 s59, s59, 2048\n\t" \
"s_waitcnt vmcnt(5) lgkmcnt(0)\n\t" \
"v_mfma_f32_32x32x16_bf16 v[64:79], v[128:131], v[24:27], 0\n\t" \
"v_mfma_f32_32x32x16_bf16 v[80:95], v[128:131], v[28:31], 0\n\t" \
"buffer_load_dwordx4 v[136:139], v1, s[28:31], s59 offen sc1\n\t" \
"s_mov_b64 exec, s[94:95]\n\t" \
"buffer_load_dwordx4 v[152:155], v6, s[28:31], s59 offen offset:1024 sc1\n\t" \
"buffer_load_dwordx4 v[168:171], v6, s[28:31], s59 offen offset:1536 sc1\n\t" \
"s_mov_b64 exec, -1\n\t" \
"s_add_u32 s59, s59, 2048\n\t" \
"buffer_load_dwordx4 v[140:143], v1, s[28:31], s59 offen sc1\n\t" \
"s_mov_b64 exec, s[94:95]\n\t" \
"buffer_load_dwordx4 v[156:159], v6, s[28:31], s59 offen offset:1024 sc1\n\t" \
"buffer_load_dwordx4 v[172:175], v6, s[28:31], s59 offen offset:1536 sc1\n\t" \
"s_mov_b64 exec, -1\n\t" \
"s_add_u32 s59, s59, 2048\n\t" \
"v_max3_f32 v8, v64, v65, v66\n\t" \
"v_max3_f32 v8, v8, v67, v68\n\t" \
"v_max3_f32 v8, v8, v69, v70\n\t" \
"v_max3_f32 v8, v8, v71, v72\n\t" \
"v_max3_f32 v8, v8, v73, v74\n\t" \
"v_max3_f32 v8, v8, v75, v76\n\t" \
"v_max3_f32 v8, v8, v77, v78\n\t" \
"v_max_f32 v8, v8, v79\n\t" \
"v_mov_b32 v10, v8\n\t" \
"s_nop 1\n\t" \
"v_permlane32_swap_b32 v10, v8\n\t" \
"v_max_f32 v8, v8, v10\n\t" \
"v_sub_f32 v96, 0, v8\n\t" \
"v_sub_f32 v97, 0, v8\n\t" \
"v_sub_f32 v98, 0, v8\n\t" \
"v_sub_f32 v99, 0, v8\n\t" \
"v_sub_f32 v100, 0, v8\n\t" \
"v_sub_f32 v101, 0, v8\n\t" \
"v_sub_f32 v102, 0, v8\n\t" \
"v_sub_f32 v103, 0, v8\n\t" \
"v_sub_f32 v104, 0, v8\n\t" \
"v_sub_f32 v105, 0, v8\n\t" \
"v_sub_f32 v106, 0, v8\n\t" \
"v_sub_f32 v107, 0, v8\n\t" \
"v_sub_f32 v108, 0, v8\n\t" \
"v_sub_f32 v109, 0, v8\n\t" \
"v_sub_f32 v110, 0, v8\n\t" \
"v_sub_f32 v111, 0, v8\n\t" \
"s_nop 1\n\t" \
"v_mfma_f32_32x32x16_bf16 v[64:79], v[128:131], v[24:27], v[96:111]\n\t" \
"v_max3_f32 v9, v80, v81, v82\n\t" \
"v_max3_f32 v9, v9, v83, v84\n\t" \
"v_max3_f32 v9, v9, v85, v86\n\t" \
"v_max3_f32 v9, v9, v87, v88\n\t" \
"v_max3_f32 v9, v9, v89, v90\n\t" \
"v_max3_f32 v9, v9, v91, v92\n\t" \
"v_max3_f32 v9, v9, v93, v94\n\t" \
"v_max_f32 v9, v9, v95\n\t" \
"v_mov_b32 v10, v9\n\t" \
"s_nop 1\n\t" \
"v_permlane32_swap_b32 v10, v9\n\t" \
"v_max_f32 v9, v9, v10\n\t" \
"v_sub_f32 v112, 0, v9\n\t" \
"v_sub_f32 v113, 0, v9\n\t" \
"v_sub_f32 v114, 0, v9\n\t" \
"v_sub_f32 v115, 0, v9\n\t" \
"v_sub_f32 v116, 0, v9\n\t" \
"v_sub_f32 v117, 0, v9\n\t" \
"v_sub_f32 v118, 0, v9\n\t" \
"v_sub_f32 v119, 0, v9\n\t" \
"v_sub_f32 v120, 0, v9\n\t" \
"v_sub_f32 v121, 0, v9\n\t" \
"v_sub_f32 v122, 0, v9\n\t" \
"v_sub_f32 v123, 0, v9\n\t" \
"v_sub_f32 v124, 0, v9\n\t" \
"v_sub_f32 v125, 0, v9\n\t" \
"v_sub_f32 v126, 0, v9\n\t" \
"v_sub_f32 v127, 0, v9\n\t" \
"v_exp_f32 v64, v64\n\t" \
"v_exp_f32 v65, v65\n\t" \
"v_exp_f32 v66, v66\n\t" \
"v_exp_f32 v67, v67\n\t" \
"v_mfma_f32_32x32x16_bf16 v[80:95], v[128:131], v[28:31], v[112:127]\n\t" \
"buffer_load_dwordx4 v[128:131], v1, s[28:31], s59 offen sc1\n\t" \
"v_exp_f32 v68, v68\n\t" \
"v_exp_f32 v69, v69\n\t" \
"v_exp_f32 v70, v70\n\t" \
"global_load_dword v20, v18, s[96:97]\n\t" \
"v_exp_f32 v71, v71\n\t" \
"v_exp_f32 v72, v72\n\t" \
"v_exp_f32 v73, v73\n\t" \
"s_waitcnt vmcnt(12)\n\t" \
"v_smfmac_f32_32x32x32_bf16 v[32:47], v[144:147], v[64:71], v7\n\t" \
"v_exp_f32 v74, v74\n\t" \
"v_exp_f32 v75, v75\n\t" \
"v_exp_f32 v76, v76\n\t" \
"v_exp_f32 v77, v77\n\t" \
"v_exp_f32 v78, v78\n\t" \
"v_exp_f32 v79, v79\n\t" \
"v_exp_f32 v80, v80\n\t" \
"v_exp_f32 v81, v81\n\t" \
"s_waitcnt vmcnt(11)\n\t" \
"v_smfmac_f32_32x32x32_bf16 v[32:47], v[160:163], v[72:79], v7\n\t" \
"v_exp_f32 v82, v82\n\t" \
"v_exp_f32 v83, v83\n\t" \
"s_waitcnt vmcnt(10)\n\t" \
"v_mfma_f32_32x32x16_bf16 v[64:79], v[132:135], v[24:27], v[96:111]\n\t" \
"v_exp_f32 v84, v84\n\t" \
"v_exp_f32 v85, v85\n\t" \
"v_exp_f32 v86, v86\n\t" \
"s_add_u32 s63, s53, 1114112\n\t" \
"buffer_load_dwordx4 v[176:179], v11, s[36:39], s63 offen\n\t" \
"v_exp_f32 v87, v87\n\t" \
"v_exp_f32 v88, v88\n\t" \
"v_exp_f32 v89, v89\n\t" \
"v_smfmac_f32_32x32x32_bf16 v[48:63], v[144:147], v[80:87], v7\n\t" \
"s_mov_b64 exec, s[94:95]\n\t" \
"buffer_load_dwordx4 v[144:147], v6, s[28:31], s59 offen offset:1024 sc1\n\t" \
"s_mov_b64 exec, -1\n\t" \
"v_exp_f32 v90, v90\n\t" \
"v_exp_f32 v91, v91\n\t" \
"v_exp_f32 v92, v92\n\t" \
"v_exp_f32 v93, v93\n\t" \
"v_exp_f32 v94, v94\n\t" \
"v_exp_f32 v95, v95\n\t" \
"v_exp_f32 v64, v64\n\t" \
"v_exp_f32 v65, v65\n\t" \
"v_smfmac_f32_32x32x32_bf16 v[48:63], v[160:163], v[88:95], v7\n\t" \
"s_mov_b64 exec, s[94:95]\n\t" \
"buffer_load_dwordx4 v[160:163], v6, s[28:31], s59 offen offset:1536 sc1\n\t" \
"s_mov_b64 exec, -1\n\t" \
"s_add_u32 s59, s59, 2048\n\t" \
"v_exp_f32 v66, v66\n\t" \
"v_exp_f32 v67, v67\n\t" \
"v_mfma_f32_32x32x16_bf16 v[80:95], v[132:135], v[28:31], v[112:127]\n\t" \
"buffer_load_dwordx4 v[132:135], v1, s[28:31], s59 offen sc1\n\t" \
"v_exp_f32 v68, v68\n\t" \
"v_exp_f32 v69, v69\n\t" \
"v_exp_f32 v70, v70\n\t" \
"s_add_u32 s63, s53, 1122304\n\t" \
"buffer_load_dwordx4 v[180:183], v11, s[36:39], s63 offen\n\t" \
"v_exp_f32 v71, v71\n\t" \
"v_exp_f32 v72, v72\n\t" \
"v_exp_f32 v73, v73\n\t" \
"s_waitcnt vmcnt(14)\n\t" \
"v_smfmac_f32_32x32x32_bf16 v[32:47], v[148:151], v[64:71], v7\n\t" \
"v_exp_f32 v74, v74\n\t" \
"v_exp_f32 v75, v75\n\t" \
"v_exp_f32 v76, v76\n\t" \
"v_exp_f32 v77, v77\n\t" \
"v_exp_f32 v78, v78\n\t" \
"v_exp_f32 v79, v79\n\t" \
"v_exp_f32 v80, v80\n\t" \
"v_exp_f32 v81, v81\n\t" \
"s_waitcnt vmcnt(13)\n\t" \
"v_smfmac_f32_32x32x32_bf16 v[32:47], v[164:167], v[72:79], v7\n\t" \
"v_exp_f32 v82, v82\n\t" \
"v_exp_f32 v83, v83\n\t" \
"s_waitcnt vmcnt(12)\n\t" \
"v_mfma_f32_32x32x16_bf16 v[64:79], v[136:139], v[24:27], v[96:111]\n\t" \
"v_exp_f32 v84, v84\n\t" \
"v_exp_f32 v85, v85\n\t" \
"v_exp_f32 v86, v86\n\t" \
"s_add_u32 s63, s53, 1130496\n\t" \
"buffer_load_dwordx4 v[184:187], v11, s[36:39], s63 offen\n\t" \
"v_exp_f32 v87, v87\n\t" \
"v_exp_f32 v88, v88\n\t" \
"v_exp_f32 v89, v89\n\t" \
"v_smfmac_f32_32x32x32_bf16 v[48:63], v[148:151], v[80:87], v7\n\t" \
"s_mov_b64 exec, s[94:95]\n\t" \
"buffer_load_dwordx4 v[148:151], v6, s[28:31], s59 offen offset:1024 sc1\n\t" \
"s_mov_b64 exec, -1\n\t" \
"v_exp_f32 v90, v90\n\t" \
"v_exp_f32 v91, v91\n\t" \
"v_exp_f32 v92, v92\n\t" \
"v_exp_f32 v93, v93\n\t" \
"v_exp_f32 v94, v94\n\t" \
"v_exp_f32 v95, v95\n\t" \
"v_exp_f32 v64, v64\n\t" \
"v_exp_f32 v65, v65\n\t" \
"v_smfmac_f32_32x32x32_bf16 v[48:63], v[164:167], v[88:95], v7\n\t" \
"s_mov_b64 exec, s[94:95]\n\t" \
"buffer_load_dwordx4 v[164:167], v6, s[28:31], s59 offen offset:1536 sc1\n\t" \
"s_mov_b64 exec, -1\n\t" \
"s_add_u32 s59, s59, 2048\n\t" \
"v_exp_f32 v66, v66\n\t" \
"v_exp_f32 v67, v67\n\t" \
"v_mfma_f32_32x32x16_bf16 v[80:95], v[136:139], v[28:31], v[112:127]\n\t" \
"buffer_load_dwordx4 v[136:139], v1, s[28:31], s59 offen sc1\n\t" \
"v_exp_f32 v68, v68\n\t" \
"v_exp_f32 v69, v69\n\t" \
"v_exp_f32 v70, v70\n\t" \
"s_add_u32 s63, s53, 1138688\n\t" \
"buffer_load_dwordx4 v[188:191], v11, s[36:39], s63 offen\n\t" \
"v_exp_f32 v71, v71\n\t" \
"v_exp_f32 v72, v72\n\t" \
"v_exp_f32 v73, v73\n\t" \
"s_waitcnt vmcnt(16)\n\t" \
"v_smfmac_f32_32x32x32_bf16 v[32:47], v[152:155], v[64:71], v7\n\t" \
"v_exp_f32 v74, v74\n\t" \
"v_exp_f32 v75, v75\n\t" \
"v_exp_f32 v76, v76\n\t" \
"v_exp_f32 v77, v77\n\t" \
"v_exp_f32 v78, v78\n\t" \
"v_exp_f32 v79, v79\n\t" \
"v_exp_f32 v80, v80\n\t" \
"v_exp_f32 v81, v81\n\t" \
"s_waitcnt vmcnt(15)\n\t" \
"v_smfmac_f32_32x32x32_bf16 v[32:47], v[168:171], v[72:79], v7\n\t" \
"v_exp_f32 v82, v82\n\t" \
"v_exp_f32 v83, v83\n\t" \
"s_waitcnt vmcnt(14)\n\t" \
"v_mfma_f32_32x32x16_bf16 v[64:79], v[140:143], v[24:27], v[96:111]\n\t" \
"v_exp_f32 v84, v84\n\t" \
"v_exp_f32 v85, v85\n\t" \
"v_exp_f32 v86, v86\n\t" \
"s_add_u32 s63, s83, 1638400\n\t" \
"buffer_load_dwordx4 v[192:195], v11, s[36:39], s63 offen\n\t" \
"v_exp_f32 v87, v87\n\t" \
"v_exp_f32 v88, v88\n\t" \
"v_exp_f32 v89, v89\n\t" \
"v_smfmac_f32_32x32x32_bf16 v[48:63], v[152:155], v[80:87], v7\n\t" \
"s_mov_b64 exec, s[94:95]\n\t" \
"buffer_load_dwordx4 v[152:155], v6, s[28:31], s59 offen offset:1024 sc1\n\t" \
"s_mov_b64 exec, -1\n\t" \
"v_exp_f32 v90, v90\n\t" \
"v_exp_f32 v91, v91\n\t" \
"v_exp_f32 v92, v92\n\t" \
"v_exp_f32 v93, v93\n\t" \
"v_exp_f32 v94, v94\n\t" \
"v_exp_f32 v95, v95\n\t" \
"v_exp_f32 v64, v64\n\t" \
"v_exp_f32 v65, v65\n\t" \
"v_smfmac_f32_32x32x32_bf16 v[48:63], v[168:171], v[88:95], v7\n\t" \
"s_mov_b64 exec, s[94:95]\n\t" \
"buffer_load_dwordx4 v[168:171], v6, s[28:31], s59 offen offset:1536 sc1\n\t" \
"s_mov_b64 exec, -1\n\t" \
"s_add_u32 s59, s59, 2048\n\t" \
"v_exp_f32 v66, v66\n\t" \
"v_exp_f32 v67, v67\n\t" \
"v_mfma_f32_32x32x16_bf16 v[80:95], v[140:143], v[28:31], v[112:127]\n\t" \
"buffer_load_dwordx4 v[140:143], v1, s[28:31], s59 offen sc1\n\t" \
"v_exp_f32 v68, v68\n\t" \
"v_exp_f32 v69, v69\n\t" \
"v_exp_f32 v70, v70\n\t" \
"s_add_u32 s63, s83, 1646592\n\t" \
"buffer_load_dwordx4 v[196:199], v11, s[36:39], s63 offen\n\t" \
"v_exp_f32 v71, v71\n\t" \
"v_exp_f32 v72, v72\n\t" \
"v_exp_f32 v73, v73\n\t" \
"s_waitcnt vmcnt(18)\n\t" \
"v_smfmac_f32_32x32x32_bf16 v[32:47], v[156:159], v[64:71], v7\n\t" \
"v_exp_f32 v74, v74\n\t" \
"v_exp_f32 v75, v75\n\t" \
"v_exp_f32 v76, v76\n\t" \
"v_exp_f32 v77, v77\n\t" \
"v_exp_f32 v78, v78\n\t" \
"v_exp_f32 v79, v79\n\t" \
"v_exp_f32 v80, v80\n\t" \
"v_exp_f32 v81, v81\n\t" \
"s_waitcnt vmcnt(17)\n\t" \
"v_smfmac_f32_32x32x32_bf16 v[32:47], v[172:175], v[72:79], v7\n\t" \
"v_exp_f32 v82, v82\n\t" \
"v_exp_f32 v83, v83\n\t" \
"s_waitcnt vmcnt(16)\n\t" \
"v_mfma_f32_32x32x16_bf16 v[64:79], v[128:131], v[24:27], v[96:111]\n\t" \
"v_exp_f32 v84, v84\n\t" \
"v_exp_f32 v85, v85\n\t" \
"v_exp_f32 v86, v86\n\t" \
"s_add_u32 s63, s83, 1654784\n\t" \
"buffer_load_dwordx4 v[200:203], v11, s[36:39], s63 offen\n\t" \
"global_load_dword v20, v18, s[96:97]\n\t" \
"v_exp_f32 v87, v87\n\t" \
"v_exp_f32 v88, v88\n\t" \
"v_exp_f32 v89, v89\n\t" \
"v_smfmac_f32_32x32x32_bf16 v[48:63], v[156:159], v[80:87], v7\n\t" \
"s_mov_b64 exec, s[94:95]\n\t" \
"buffer_load_dwordx4 v[156:159], v6, s[28:31], s59 offen offset:1024 sc1\n\t" \
"s_mov_b64 exec, -1\n\t" \
"v_exp_f32 v90, v90\n\t" \
"v_exp_f32 v91, v91\n\t" \
"v_exp_f32 v92, v92\n\t" \
"v_exp_f32 v93, v93\n\t" \
"v_exp_f32 v94, v94\n\t" \
"v_exp_f32 v95, v95\n\t" \
"v_exp_f32 v64, v64\n\t" \
"v_exp_f32 v65, v65\n\t" \
"v_smfmac_f32_32x32x32_bf16 v[48:63], v[172:175], v[88:95], v7\n\t" \
"s_mov_b64 exec, s[94:95]\n\t" \
"buffer_load_dwordx4 v[172:175], v6, s[28:31], s59 offen offset:1536 sc1\n\t" \
"s_mov_b64 exec, -1\n\t" \
"s_add_u32 s59, s59, 2048\n\t" \
"v_exp_f32 v66, v66\n\t" \
"v_exp_f32 v67, v67\n\t" \
"v_mfma_f32_32x32x16_bf16 v[80:95], v[128:131], v[28:31], v[112:127]\n\t" \
"buffer_load_dwordx4 v[128:131], v1, s[28:31], s59 offen sc1\n\t" \
"v_exp_f32 v68, v68\n\t" \
"v_exp_f32 v69, v69\n\t" \
"v_exp_f32 v70, v70\n\t" \
"v_exp_f32 v71, v71\n\t" \
"v_exp_f32 v72, v72\n\t" \
"v_exp_f32 v73, v73\n\t" \
"s_waitcnt vmcnt(18)\n\t" \
"v_smfmac_f32_32x32x32_bf16 v[32:47], v[144:147], v[64:71], v7\n\t" \
"v_exp_f32 v74, v74\n\t" \
"v_exp_f32 v75, v75\n\t" \
"v_exp_f32 v76, v76\n\t" \
"v_exp_f32 v77, v77\n\t" \
"v_exp_f32 v78, v78\n\t" \
"v_exp_f32 v79, v79\n\t" \
"v_exp_f32 v80, v80\n\t" \
"v_exp_f32 v81, v81\n\t" \
"s_waitcnt vmcnt(17)\n\t" \
"v_smfmac_f32_32x32x32_bf16 v[32:47], v[160:163], v[72:79], v7\n\t" \
"v_exp_f32 v82, v82\n\t" \
"v_exp_f32 v83, v83\n\t" \
"s_waitcnt vmcnt(16)\n\t" \
"v_mfma_f32_32x32x16_bf16 v[64:79], v[132:135], v[24:27], v[96:111]\n\t" \
"v_exp_f32 v84, v84\n\t" \
"v_exp_f32 v85, v85\n\t" \
"v_exp_f32 v86, v86\n\t" \
"s_add_u32 s63, s83, 1662976\n\t" \
"buffer_load_dwordx4 v[204:207], v11, s[36:39], s63 offen\n\t" \
"v_exp_f32 v87, v87\n\t" \
"v_exp_f32 v88, v88\n\t" \
"v_exp_f32 v89, v89\n\t" \
"v_smfmac_f32_32x32x32_bf16 v[48:63], v[144:147], v[80:87], v7\n\t" \
"s_mov_b64 exec, s[94:95]\n\t" \
"buffer_load_dwordx4 v[144:147], v6, s[28:31], s59 offen offset:1024 sc1\n\t" \
"s_mov_b64 exec, -1\n\t" \
"v_exp_f32 v90, v90\n\t" \
"v_exp_f32 v91, v91\n\t" \
"v_exp_f32 v92, v92\n\t" \
"v_exp_f32 v93, v93\n\t" \
"v_exp_f32 v94, v94\n\t" \
"v_exp_f32 v95, v95\n\t" \
"v_exp_f32 v64, v64\n\t" \
"v_exp_f32 v65, v65\n\t" \
"v_smfmac_f32_32x32x32_bf16 v[48:63], v[160:163], v[88:95], v7\n\t" \
"s_mov_b64 exec, s[94:95]\n\t" \
"buffer_load_dwordx4 v[160:163], v6, s[28:31], s59 offen offset:1536 sc1\n\t" \
"s_mov_b64 exec, -1\n\t" \
"s_add_u32 s59, s59, 2048\n\t" \
"v_exp_f32 v66, v66\n\t" \
"v_exp_f32 v67, v67\n\t" \
"v_mfma_f32_32x32x16_bf16 v[80:95], v[132:135], v[28:31], v[112:127]\n\t" \
"buffer_load_dwordx4 v[132:135], v1, s[28:31], s59 offen sc1\n\t" \
"v_exp_f32 v68, v68\n\t" \
"v_exp_f32 v69, v69\n\t" \
"v_exp_f32 v70, v70\n\t" \
"s_add_u32 s63, s53, 360448\n\t" \
"buffer_load_dwordx4 v[208:211], v11, s[36:39], s63 offen\n\t" \
"v_exp_f32 v71, v71\n\t" \
"v_exp_f32 v72, v72\n\t" \
"v_exp_f32 v73, v73\n\t" \
"s_waitcnt vmcnt(18)\n\t" \
"v_smfmac_f32_32x32x32_bf16 v[32:47], v[148:151], v[64:71], v7\n\t" \
"v_exp_f32 v74, v74\n\t" \
"v_exp_f32 v75, v75\n\t" \
"v_exp_f32 v76, v76\n\t" \
"v_exp_f32 v77, v77\n\t" \
"v_exp_f32 v78, v78\n\t" \
"v_exp_f32 v79, v79\n\t" \
"v_exp_f32 v80, v80\n\t" \
"v_exp_f32 v81, v81\n\t" \
"s_waitcnt vmcnt(17)\n\t" \
"v_smfmac_f32_32x32x32_bf16 v[32:47], v[164:167], v[72:79], v7\n\t" \
"v_exp_f32 v82, v82\n\t" \
"v_exp_f32 v83, v83\n\t" \
"s_waitcnt vmcnt(16)\n\t" \
"v_mfma_f32_32x32x16_bf16 v[64:79], v[136:139], v[24:27], v[96:111]\n\t" \
"v_exp_f32 v84, v84\n\t" \
"v_exp_f32 v85, v85\n\t" \
"v_exp_f32 v86, v86\n\t" \
"s_add_u32 s63, s53, 368640\n\t" \
"buffer_load_dwordx4 v[212:215], v11, s[36:39], s63 offen\n\t" \
"v_exp_f32 v87, v87\n\t" \
"v_exp_f32 v88, v88\n\t" \
"v_exp_f32 v89, v89\n\t" \
"v_smfmac_f32_32x32x32_bf16 v[48:63], v[148:151], v[80:87], v7\n\t" \
"s_mov_b64 exec, s[94:95]\n\t" \
"buffer_load_dwordx4 v[148:151], v6, s[28:31], s59 offen offset:1024 sc1\n\t" \
"s_mov_b64 exec, -1\n\t" \
"v_exp_f32 v90, v90\n\t" \
"v_exp_f32 v91, v91\n\t" \
"v_exp_f32 v92, v92\n\t" \
"v_exp_f32 v93, v93\n\t" \
"v_exp_f32 v94, v94\n\t" \
"v_exp_f32 v95, v95\n\t" \
"v_exp_f32 v64, v64\n\t" \
"v_exp_f32 v65, v65\n\t" \
"v_smfmac_f32_32x32x32_bf16 v[48:63], v[164:167], v[88:95], v7\n\t" \
"s_mov_b64 exec, s[94:95]\n\t" \
"buffer_load_dwordx4 v[164:167], v6, s[28:31], s59 offen offset:1536 sc1\n\t" \
"s_mov_b64 exec, -1\n\t" \
"s_add_u32 s59, s59, 2048\n\t" \
"v_exp_f32 v66, v66\n\t" \
"v_exp_f32 v67, v67\n\t" \
"v_mfma_f32_32x32x16_bf16 v[80:95], v[136:139], v[28:31], v[112:127]\n\t" \
"buffer_load_dwordx4 v[136:139], v1, s[28:31], s59 offen sc1\n\t" \
"v_exp_f32 v68, v68\n\t" \
"v_exp_f32 v69, v69\n\t" \
"v_exp_f32 v70, v70\n\t" \
"s_add_u32 s63, s53, 376832\n\t" \
"buffer_load_dwordx4 v[216:219], v11, s[36:39], s63 offen\n\t" \
"v_exp_f32 v71, v71\n\t" \
"v_exp_f32 v72, v72\n\t" \
"v_exp_f32 v73, v73\n\t" \
"s_waitcnt vmcnt(18)\n\t" \
"v_smfmac_f32_32x32x32_bf16 v[32:47], v[152:155], v[64:71], v7\n\t" \
"v_exp_f32 v74, v74\n\t" \
"v_exp_f32 v75, v75\n\t" \
"v_exp_f32 v76, v76\n\t" \
"v_exp_f32 v77, v77\n\t" \
"v_exp_f32 v78, v78\n\t" \
"v_exp_f32 v79, v79\n\t" \
"v_exp_f32 v80, v80\n\t" \
"v_exp_f32 v81, v81\n\t" \
"s_waitcnt vmcnt(17)\n\t" \
"v_smfmac_f32_32x32x32_bf16 v[32:47], v[168:171], v[72:79], v7\n\t" \
"v_exp_f32 v82, v82\n\t" \
"v_exp_f32 v83, v83\n\t" \
"s_waitcnt vmcnt(16)\n\t" \
"v_mfma_f32_32x32x16_bf16 v[64:79], v[140:143], v[24:27], v[96:111]\n\t" \
"v_exp_f32 v84, v84\n\t" \
"v_exp_f32 v85, v85\n\t" \
"v_exp_f32 v86, v86\n\t" \
"s_add_u32 s63, s53, 385024\n\t" \
"buffer_load_dwordx4 v[220:223], v11, s[36:39], s63 offen\n\t" \
"v_exp_f32 v87, v87\n\t" \
"v_exp_f32 v88, v88\n\t" \
"v_exp_f32 v89, v89\n\t" \
"v_smfmac_f32_32x32x32_bf16 v[48:63], v[152:155], v[80:87], v7\n\t" \
"s_mov_b64 exec, s[94:95]\n\t" \
"buffer_load_dwordx4 v[152:155], v6, s[28:31], s59 offen offset:1024 sc1\n\t" \
"s_mov_b64 exec, -1\n\t" \
"v_exp_f32 v90, v90\n\t" \
"v_exp_f32 v91, v91\n\t" \
"v_exp_f32 v92, v92\n\t" \
"v_exp_f32 v93, v93\n\t" \
"v_exp_f32 v94, v94\n\t" \
"v_exp_f32 v95, v95\n\t" \
"v_exp_f32 v64, v64\n\t" \
"v_exp_f32 v65, v65\n\t" \
"v_smfmac_f32_32x32x32_bf16 v[48:63], v[168:171], v[88:95], v7\n\t" \
"s_mov_b64 exec, s[94:95]\n\t" \
"buffer_load_dwordx4 v[168:171], v6, s[28:31], s59 offen offset:1536 sc1\n\t" \
"s_mov_b64 exec, -1\n\t" \
"s_add_u32 s59, s59, 2048\n\t" \
"v_exp_f32 v66, v66\n\t" \
"v_exp_f32 v67, v67\n\t" \
"v_mfma_f32_32x32x16_bf16 v[80:95], v[140:143], v[28:31], v[112:127]\n\t" \
"buffer_load_dwordx4 v[140:143], v1, s[28:31], s59 offen sc1\n\t" \
"v_exp_f32 v68, v68\n\t" \
"v_exp_f32 v69, v69\n\t" \
"v_exp_f32 v70, v70\n\t" \
"s_add_u32 s63, s53, 884736\n\t" \
"buffer_load_dwordx4 v[224:227], v11, s[36:39], s63 offen\n\t" \
"global_load_dword v20, v18, s[96:97]\n\t" \
"v_exp_f32 v71, v71\n\t" \
"v_exp_f32 v72, v72\n\t" \
"v_exp_f32 v73, v73\n\t" \
"s_waitcnt vmcnt(18)\n\t" \
"v_smfmac_f32_32x32x32_bf16 v[32:47], v[156:159], v[64:71], v7\n\t" \
"v_exp_f32 v74, v74\n\t" \
"v_exp_f32 v75, v75\n\t" \
"v_exp_f32 v76, v76\n\t" \
"v_exp_f32 v77, v77\n\t" \
"v_exp_f32 v78, v78\n\t" \
"v_exp_f32 v79, v79\n\t" \
"v_exp_f32 v80, v80\n\t" \
"v_exp_f32 v81, v81\n\t" \
"s_waitcnt vmcnt(17)\n\t" \
"v_smfmac_f32_32x32x32_bf16 v[32:47], v[172:175], v[72:79], v7\n\t" \
"v_exp_f32 v82, v82\n\t" \
"v_exp_f32 v83, v83\n\t" \
"s_waitcnt vmcnt(16)\n\t" \
"v_mfma_f32_32x32x16_bf16 v[64:79], v[128:131], v[24:27], v[96:111]\n\t" \
"v_exp_f32 v84, v84\n\t" \
"v_exp_f32 v85, v85\n\t" \
"v_exp_f32 v86, v86\n\t" \
"s_add_u32 s63, s53, 892928\n\t" \
"buffer_load_dwordx4 v[228:231], v11, s[36:39], s63 offen\n\t" \
"v_exp_f32 v87, v87\n\t" \
"v_exp_f32 v88, v88\n\t" \
"v_exp_f32 v89, v89\n\t" \
"v_smfmac_f32_32x32x32_bf16 v[48:63], v[156:159], v[80:87], v7\n\t" \
"s_mov_b64 exec, s[94:95]\n\t" \
"buffer_load_dwordx4 v[156:159], v6, s[28:31], s59 offen offset:1024 sc1\n\t" \
"s_mov_b64 exec, -1\n\t" \
"v_exp_f32 v90, v90\n\t" \
"v_exp_f32 v91, v91\n\t" \
"v_exp_f32 v92, v92\n\t" \
"v_exp_f32 v93, v93\n\t" \
"v_exp_f32 v94, v94\n\t" \
"v_exp_f32 v95, v95\n\t" \
"v_exp_f32 v64, v64\n\t" \
"v_exp_f32 v65, v65\n\t" \
"v_smfmac_f32_32x32x32_bf16 v[48:63], v[172:175], v[88:95], v7\n\t" \
"s_mov_b64 exec, s[94:95]\n\t" \
"buffer_load_dwordx4 v[172:175], v6, s[28:31], s59 offen offset:1536 sc1\n\t" \
"s_mov_b64 exec, -1\n\t" \
"s_add_u32 s59, s59, 2048\n\t" \
"v_exp_f32 v66, v66\n\t" \
"v_exp_f32 v67, v67\n\t" \
"v_mfma_f32_32x32x16_bf16 v[80:95], v[128:131], v[28:31], v[112:127]\n\t" \
"buffer_load_dwordx4 v[128:131], v1, s[28:31], s59 offen sc1\n\t" \
"v_exp_f32 v68, v68\n\t" \
"v_exp_f32 v69, v69\n\t" \
"v_exp_f32 v70, v70\n\t" \
"v_exp_f32 v71, v71\n\t" \
"v_exp_f32 v72, v72\n\t" \
"v_exp_f32 v73, v73\n\t" \
"s_waitcnt vmcnt(18)\n\t" \
"v_smfmac_f32_32x32x32_bf16 v[32:47], v[144:147], v[64:71], v7\n\t" \
"v_exp_f32 v74, v74\n\t" \
"v_exp_f32 v75, v75\n\t" \
"v_exp_f32 v76, v76\n\t" \
"v_exp_f32 v77, v77\n\t" \
"v_exp_f32 v78, v78\n\t" \
"v_exp_f32 v79, v79\n\t" \
"v_exp_f32 v80, v80\n\t" \
"v_exp_f32 v81, v81\n\t" \
"s_waitcnt vmcnt(17)\n\t" \
"v_smfmac_f32_32x32x32_bf16 v[32:47], v[160:163], v[72:79], v7\n\t" \
"v_exp_f32 v82, v82\n\t" \
"v_exp_f32 v83, v83\n\t" \
"s_waitcnt vmcnt(16)\n\t" \
"v_mfma_f32_32x32x16_bf16 v[64:79], v[132:135], v[24:27], v[96:111]\n\t" \
"v_exp_f32 v84, v84\n\t" \
"v_exp_f32 v85, v85\n\t" \
"v_exp_f32 v86, v86\n\t" \
"s_add_u32 s63, s53, 901120\n\t" \
"buffer_load_dwordx4 v[232:235], v11, s[36:39], s63 offen\n\t" \
"v_exp_f32 v87, v87\n\t" \
"v_exp_f32 v88, v88\n\t" \
"v_exp_f32 v89, v89\n\t" \
"v_smfmac_f32_32x32x32_bf16 v[48:63], v[144:147], v[80:87], v7\n\t" \
"s_mov_b64 exec, s[94:95]\n\t" \
"buffer_load_dwordx4 v[144:147], v6, s[28:31], s59 offen offset:1024 sc1\n\t" \
"s_mov_b64 exec, -1\n\t" \
"v_exp_f32 v90, v90\n\t" \
"v_exp_f32 v91, v91\n\t" \
"v_exp_f32 v92, v92\n\t" \
"v_exp_f32 v93, v93\n\t" \
"v_exp_f32 v94, v94\n\t" \
"v_exp_f32 v95, v95\n\t" \
"v_exp_f32 v64, v64\n\t" \
"v_exp_f32 v65, v65\n\t" \
"v_smfmac_f32_32x32x32_bf16 v[48:63], v[160:163], v[88:95], v7\n\t" \
"s_mov_b64 exec, s[94:95]\n\t" \
"buffer_load_dwordx4 v[160:163], v6, s[28:31], s59 offen offset:1536 sc1\n\t" \
"s_mov_b64 exec, -1\n\t" \
"s_add_u32 s59, s59, 2048\n\t" \
"v_exp_f32 v66, v66\n\t" \
"v_exp_f32 v67, v67\n\t" \
"v_mfma_f32_32x32x16_bf16 v[80:95], v[132:135], v[28:31], v[112:127]\n\t" \
"buffer_load_dwordx4 v[132:135], v1, s[28:31], s59 offen sc1\n\t" \
"v_exp_f32 v68, v68\n\t" \
"v_exp_f32 v69, v69\n\t" \
"v_exp_f32 v70, v70\n\t" \
"s_add_u32 s63, s53, 909312\n\t" \
"buffer_load_dwordx4 v[236:239], v11, s[36:39], s63 offen\n\t" \
"v_exp_f32 v71, v71\n\t" \
"v_exp_f32 v72, v72\n\t" \
"v_exp_f32 v73, v73\n\t" \
"s_waitcnt vmcnt(18)\n\t" \
"v_smfmac_f32_32x32x32_bf16 v[32:47], v[148:151], v[64:71], v7\n\t" \
"v_exp_f32 v74, v74\n\t" \
"v_exp_f32 v75, v75\n\t" \
"v_exp_f32 v76, v76\n\t" \
"v_exp_f32 v77, v77\n\t" \
"v_exp_f32 v78, v78\n\t" \
"v_exp_f32 v79, v79\n\t" \
"v_exp_f32 v80, v80\n\t" \
"v_exp_f32 v81, v81\n\t" \
"s_waitcnt vmcnt(17)\n\t" \
"v_smfmac_f32_32x32x32_bf16 v[32:47], v[164:167], v[72:79], v7\n\t" \
"v_exp_f32 v82, v82\n\t" \
"v_exp_f32 v83, v83\n\t" \
"s_waitcnt vmcnt(16)\n\t" \
"v_mfma_f32_32x32x16_bf16 v[64:79], v[136:139], v[24:27], v[96:111]\n\t" \
"v_exp_f32 v84, v84\n\t" \
"v_exp_f32 v85, v85\n\t" \
"v_exp_f32 v86, v86\n\t" \
"s_add_u32 s63, s53, 1409024\n\t" \
"buffer_load_dwordx4 v[240:243], v11, s[36:39], s63 offen\n\t" \
"v_exp_f32 v87, v87\n\t" \
"v_exp_f32 v88, v88\n\t" \
"v_exp_f32 v89, v89\n\t" \
"v_smfmac_f32_32x32x32_bf16 v[48:63], v[148:151], v[80:87], v7\n\t" \
"s_mov_b64 exec, s[94:95]\n\t" \
"buffer_load_dwordx4 v[148:151], v6, s[28:31], s59 offen offset:1024 sc1\n\t" \
"s_mov_b64 exec, -1\n\t" \
"v_exp_f32 v90, v90\n\t" \
"v_exp_f32 v91, v91\n\t" \
"v_exp_f32 v92, v92\n\t" \
"v_exp_f32 v93, v93\n\t" \
"v_exp_f32 v94, v94\n\t" \
"v_exp_f32 v95, v95\n\t" \
"v_exp_f32 v64, v64\n\t" \
"v_exp_f32 v65, v65\n\t" \
"v_smfmac_f32_32x32x32_bf16 v[48:63], v[164:167], v[88:95], v7\n\t" \
"s_mov_b64 exec, s[94:95]\n\t" \
"buffer_load_dwordx4 v[164:167], v6, s[28:31], s59 offen offset:1536 sc1\n\t" \
"s_mov_b64 exec, -1\n\t" \
"s_add_u32 s59, s59, 2048\n\t" \
"v_exp_f32 v66, v66\n\t" \
"v_exp_f32 v67, v67\n\t" \
"v_mfma_f32_32x32x16_bf16 v[80:95], v[136:139], v[28:31], v[112:127]\n\t" \
"buffer_load_dwordx4 v[136:139], v1, s[28:31], s59 offen sc1\n\t" \
"v_exp_f32 v68, v68\n\t" \
"v_exp_f32 v69, v69\n\t" \
"v_exp_f32 v70, v70\n\t" \
"s_add_u32 s63, s53, 1417216\n\t" \
"buffer_load_dwordx4 v[244:247], v11, s[36:39], s63 offen\n\t" \
"v_exp_f32 v71, v71\n\t" \
"v_exp_f32 v72, v72\n\t" \
"v_exp_f32 v73, v73\n\t" \
"s_waitcnt vmcnt(18)\n\t" \
"v_smfmac_f32_32x32x32_bf16 v[32:47], v[152:155], v[64:71], v7\n\t" \
"v_exp_f32 v74, v74\n\t" \
"v_exp_f32 v75, v75\n\t" \
"v_exp_f32 v76, v76\n\t" \
"v_exp_f32 v77, v77\n\t" \
"v_exp_f32 v78, v78\n\t" \
"v_exp_f32 v79, v79\n\t" \
"v_exp_f32 v80, v80\n\t" \
"v_exp_f32 v81, v81\n\t" \
"s_waitcnt vmcnt(17)\n\t" \
"v_smfmac_f32_32x32x32_bf16 v[32:47], v[168:171], v[72:79], v7\n\t" \
"v_exp_f32 v82, v82\n\t" \
"v_exp_f32 v83, v83\n\t" \
"s_waitcnt vmcnt(16)\n\t" \
"v_mfma_f32_32x32x16_bf16 v[64:79], v[140:143], v[24:27], v[96:111]\n\t" \
"v_exp_f32 v84, v84\n\t" \
"v_exp_f32 v85, v85\n\t" \
"v_exp_f32 v86, v86\n\t" \
"s_add_u32 s63, s53, 1425408\n\t" \
"buffer_load_dwordx4 v[248:251], v11, s[36:39], s63 offen\n\t" \
"global_load_dword v20, v18, s[96:97]\n\t" \
"v_exp_f32 v87, v87\n\t" \
"v_exp_f32 v88, v88\n\t" \
"v_exp_f32 v89, v89\n\t" \
"v_smfmac_f32_32x32x32_bf16 v[48:63], v[152:155], v[80:87], v7\n\t" \
"s_mov_b64 exec, s[94:95]\n\t" \
"buffer_load_dwordx4 v[152:155], v6, s[28:31], s59 offen offset:1024 sc1\n\t" \
"s_mov_b64 exec, -1\n\t" \
"v_exp_f32 v90, v90\n\t" \
"v_exp_f32 v91, v91\n\t" \
"v_exp_f32 v92, v92\n\t" \
"v_exp_f32 v93, v93\n\t" \
"v_exp_f32 v94, v94\n\t" \
"v_exp_f32 v95, v95\n\t" \
"v_exp_f32 v64, v64\n\t" \
"v_exp_f32 v65, v65\n\t" \
"v_smfmac_f32_32x32x32_bf16 v[48:63], v[168:171], v[88:95], v7\n\t" \
"s_mov_b64 exec, s[94:95]\n\t" \
"buffer_load_dwordx4 v[168:171], v6, s[28:31], s59 offen offset:1536 sc1\n\t" \
"s_mov_b64 exec, -1\n\t" \
"s_add_u32 s59, s59, 2048\n\t" \
"v_exp_f32 v66, v66\n\t" \
"v_exp_f32 v67, v67\n\t" \
"v_mfma_f32_32x32x16_bf16 v[80:95], v[140:143], v[28:31], v[112:127]\n\t" \
"v_exp_f32 v68, v68\n\t" \
"v_exp_f32 v69, v69\n\t" \
"v_exp_f32 v70, v70\n\t" \
"s_add_u32 s63, s53, 1433600\n\t" \
"buffer_load_dwordx4 v[252:255], v11, s[36:39], s63 offen\n\t" \
"v_exp_f32 v71, v71\n\t" \
"v_exp_f32 v72, v72\n\t" \
"v_exp_f32 v73, v73\n\t" \
"s_waitcnt vmcnt(17)\n\t" \
"v_smfmac_f32_32x32x32_bf16 v[32:47], v[156:159], v[64:71], v7\n\t" \
"v_exp_f32 v74, v74\n\t" \
"v_exp_f32 v75, v75\n\t" \
"v_exp_f32 v76, v76\n\t" \
"v_exp_f32 v77, v77\n\t" \
"v_exp_f32 v78, v78\n\t" \
"v_exp_f32 v79, v79\n\t" \
"v_exp_f32 v80, v80\n\t" \
"v_exp_f32 v81, v81\n\t" \
"s_waitcnt vmcnt(16)\n\t" \
"v_smfmac_f32_32x32x32_bf16 v[32:47], v[172:175], v[72:79], v7\n\t" \
"v_exp_f32 v82, v82\n\t" \
"v_exp_f32 v83, v83\n\t" \
"s_waitcnt vmcnt(15)\n\t" \
"v_mfma_f32_32x32x16_bf16 v[64:79], v[128:131], v[24:27], v[96:111]\n\t" \
"v_exp_f32 v84, v84\n\t" \
"v_exp_f32 v85, v85\n\t" \
"v_exp_f32 v86, v86\n\t" \
"v_exp_f32 v87, v87\n\t" \
"v_exp_f32 v88, v88\n\t" \
"v_exp_f32 v89, v89\n\t" \
"v_smfmac_f32_32x32x32_bf16 v[48:63], v[156:159], v[80:87], v7\n\t" \
"v_exp_f32 v90, v90\n\t" \
"v_exp_f32 v91, v91\n\t" \
"v_exp_f32 v92, v92\n\t" \
"v_exp_f32 v93, v93\n\t" \
"v_exp_f32 v94, v94\n\t" \
"v_exp_f32 v95, v95\n\t" \
"v_exp_f32 v64, v64\n\t" \
"v_exp_f32 v65, v65\n\t" \
"v_smfmac_f32_32x32x32_bf16 v[48:63], v[172:175], v[88:95], v7\n\t" \
"v_exp_f32 v66, v66\n\t" \
"v_exp_f32 v67, v67\n\t" \
"v_mfma_f32_32x32x16_bf16 v[80:95], v[128:131], v[28:31], v[112:127]\n\t" \
"v_exp_f32 v68, v68\n\t" \
"v_exp_f32 v69, v69\n\t" \
"v_exp_f32 v70, v70\n\t" \
"v_exp_f32 v71, v71\n\t" \
"v_exp_f32 v72, v72\n\t" \
"v_exp_f32 v73, v73\n\t" \
"s_waitcnt vmcnt(13)\n\t" \
"v_smfmac_f32_32x32x32_bf16 v[32:47], v[144:147], v[64:71], v7\n\t" \
"v_exp_f32 v74, v74\n\t" \
"v_exp_f32 v75, v75\n\t" \
"v_exp_f32 v76, v76\n\t" \
"v_exp_f32 v77, v77\n\t" \
"v_exp_f32 v78, v78\n\t" \
"v_exp_f32 v79, v79\n\t" \
"v_exp_f32 v80, v80\n\t" \
"v_exp_f32 v81, v81\n\t" \
"s_waitcnt vmcnt(12)\n\t" \
"v_smfmac_f32_32x32x32_bf16 v[32:47], v[160:163], v[72:79], v7\n\t" \
"v_exp_f32 v82, v82\n\t" \
"v_exp_f32 v83, v83\n\t" \
"s_waitcnt vmcnt(11)\n\t" \
"v_mfma_f32_32x32x16_bf16 v[64:79], v[132:135], v[24:27], v[96:111]\n\t" \
"v_exp_f32 v84, v84\n\t" \
"v_exp_f32 v85, v85\n\t" \
"v_exp_f32 v86, v86\n\t" \
"s_mul_i32 s57, s26, 2560\n\t" \
"s_add_u32 s57, s57, 131072\n\t" \
"v_add_u32 v14, s57, v1\n\t" \
"v_lshlrev_b32 v15, 3, v0\n\t" \
"v_add_u32 v15, s57, v15\n\t" \
"v_exp_f32 v87, v87\n\t" \
"v_exp_f32 v88, v88\n\t" \
"v_exp_f32 v89, v89\n\t" \
"v_smfmac_f32_32x32x32_bf16 v[48:63], v[144:147], v[80:87], v7\n\t" \
"v_exp_f32 v90, v90\n\t" \
"v_exp_f32 v91, v91\n\t" \
"v_exp_f32 v92, v92\n\t" \
"v_exp_f32 v93, v93\n\t" \
"v_exp_f32 v94, v94\n\t" \
"v_exp_f32 v95, v95\n\t" \
"v_exp_f32 v64, v64\n\t" \
"v_exp_f32 v65, v65\n\t" \
"v_smfmac_f32_32x32x32_bf16 v[48:63], v[160:163], v[88:95], v7\n\t" \
"v_exp_f32 v66, v66\n\t" \
"v_exp_f32 v67, v67\n\t" \
"v_mfma_f32_32x32x16_bf16 v[80:95], v[132:135], v[28:31], v[112:127]\n\t" \
"v_exp_f32 v68, v68\n\t" \
"v_exp_f32 v69, v69\n\t" \
"v_exp_f32 v70, v70\n\t" \
"s_xor_b32 s58, s26, 4\n\t" \
"s_mul_i32 s58, s58, 2560\n\t" \
"s_add_u32 s58, s58, 131072\n\t" \
"s_sub_u32 s58, s58, s57\n\t" \
"v_add_u32 v16, s58, v14\n\t" \
"v_exp_f32 v71, v71\n\t" \
"v_exp_f32 v72, v72\n\t" \
"v_exp_f32 v73, v73\n\t" \
"s_waitcnt vmcnt(8)\n\t" \
"v_smfmac_f32_32x32x32_bf16 v[32:47], v[148:151], v[64:71], v7\n\t" \
"v_exp_f32 v74, v74\n\t" \
"v_exp_f32 v75, v75\n\t" \
"v_exp_f32 v76, v76\n\t" \
"v_exp_f32 v77, v77\n\t" \
"v_exp_f32 v78, v78\n\t" \
"v_exp_f32 v79, v79\n\t" \
"v_exp_f32 v80, v80\n\t" \
"v_exp_f32 v81, v81\n\t" \
"s_waitcnt vmcnt(7)\n\t" \
"v_smfmac_f32_32x32x32_bf16 v[32:47], v[164:167], v[72:79], v7\n\t" \
"v_exp_f32 v82, v82\n\t" \
"v_exp_f32 v83, v83\n\t" \
"s_waitcnt vmcnt(6)\n\t" \
"v_mfma_f32_32x32x16_bf16 v[64:79], v[136:139], v[24:27], v[96:111]\n\t" \
"v_exp_f32 v84, v84\n\t" \
"v_exp_f32 v85, v85\n\t" \
"v_exp_f32 v86, v86\n\t" \
"v_add_u32 v17, s58, v15\n\t" \
"s_lshl_b32 s57, s47, 1\n\t" \
"s_lshr_b32 s58, s26, 2\n\t" \
"s_add_u32 s57, s57, s58\n\t" \
"s_lshl_b32 s57, s57, 5\n\t" \
"v_exp_f32 v87, v87\n\t" \
"v_exp_f32 v88, v88\n\t" \
"v_exp_f32 v89, v89\n\t" \
"v_smfmac_f32_32x32x32_bf16 v[48:63], v[148:151], v[80:87], v7\n\t" \
"v_exp_f32 v90, v90\n\t" \
"v_exp_f32 v91, v91\n\t" \
"v_exp_f32 v92, v92\n\t" \
"v_exp_f32 v93, v93\n\t" \
"v_exp_f32 v94, v94\n\t" \
"v_exp_f32 v95, v95\n\t" \
"v_exp_f32 v64, v64\n\t" \
"v_exp_f32 v65, v65\n\t" \
"v_smfmac_f32_32x32x32_bf16 v[48:63], v[164:167], v[88:95], v7\n\t" \
"v_exp_f32 v66, v66\n\t" \
"v_exp_f32 v67, v67\n\t" \
"v_mfma_f32_32x32x16_bf16 v[80:95], v[136:139], v[28:31], v[112:127]\n\t" \
"v_exp_f32 v68, v68\n\t" \
"v_exp_f32 v69, v69\n\t" \
"v_exp_f32 v70, v70\n\t" \
"s_lshl_b32 s58, s46, 2\n\t" \
"s_add_u32 s57, s57, s58\n\t" \
"s_add_u32 s57, s57, s56\n\t" \
"s_lshl_b32 s57, s57, 10\n\t" \
"s_add_u32 s92, s57, 5242880\n\t" \
"v_exp_f32 v71, v71\n\t" \
"v_exp_f32 v72, v72\n\t" \
"v_exp_f32 v73, v73\n\t" \
"s_waitcnt vmcnt(2)\n\t" \
"v_smfmac_f32_32x32x32_bf16 v[32:47], v[152:155], v[64:71], v7\n\t" \
"v_exp_f32 v74, v74\n\t" \
"v_exp_f32 v75, v75\n\t" \
"v_exp_f32 v76, v76\n\t" \
"v_exp_f32 v77, v77\n\t" \
"v_exp_f32 v78, v78\n\t" \
"v_exp_f32 v79, v79\n\t" \
"v_exp_f32 v80, v80\n\t" \
"v_exp_f32 v81, v81\n\t" \
"s_waitcnt vmcnt(1)\n\t" \
"v_smfmac_f32_32x32x32_bf16 v[32:47], v[168:171], v[72:79], v7\n\t" \
"v_exp_f32 v82, v82\n\t" \
"v_exp_f32 v83, v83\n\t" \
"v_exp_f32 v84, v84\n\t" \
"v_exp_f32 v85, v85\n\t" \
"v_exp_f32 v86, v86\n\t" \
"v_exp_f32 v87, v87\n\t" \
"v_exp_f32 v88, v88\n\t" \
"v_exp_f32 v89, v89\n\t" \
"v_smfmac_f32_32x32x32_bf16 v[48:63], v[152:155], v[80:87], v7\n\t" \
"v_exp_f32 v90, v90\n\t" \
"v_exp_f32 v91, v91\n\t" \
"v_exp_f32 v92, v92\n\t" \
"v_exp_f32 v93, v93\n\t" \
"v_exp_f32 v94, v94\n\t" \
"v_exp_f32 v95, v95\n\t" \
"v_mov_b32 v41, v8\n\t" \
"ds_write_b128 v14, v[32:35]\n\t" \
"ds_write_b128 v14, v[36:39] offset:1024\n\t" \
"ds_write_b64 v15, v[40:41] offset:2048\n\t" \
"v_smfmac_f32_32x32x32_bf16 v[48:63], v[168:171], v[88:95], v7\n\t" \
"Latte_%=:\n\t" \
"v_mul_f32 v10, v32, v32\n\t" \
"v_mul_f32 v12, v33, v33\n\t" \
"v_fmac_f32 v10, v34, v34\n\t" \
"v_fmac_f32 v12, v35, v35\n\t" \
"v_fmac_f32 v10, v36, v36\n\t" \
"v_fmac_f32 v12, v37, v37\n\t" \
"v_fmac_f32 v10, v38, v38\n\t" \
"v_fmac_f32 v12, v39, v39\n\t" \
"v_fmac_f32 v10, v40, v40\n\t" \
"s_nop 2\n\t" \
"v_fmac_f32 v12, v48, v48\n\t" \
"v_fmac_f32 v10, v49, v49\n\t" \
"v_fmac_f32 v12, v50, v50\n\t" \
"v_fmac_f32 v10, v51, v51\n\t" \
"v_fmac_f32 v12, v52, v52\n\t" \
"v_fmac_f32 v10, v53, v53\n\t" \
"v_fmac_f32 v12, v54, v54\n\t" \
"v_fmac_f32 v10, v55, v55\n\t" \
"v_fmac_f32 v12, v56, v56\n\t" \
"v_add_f32 v10, v10, v12\n\t" \
"v_cmp_ngt_f32 vcc, 0x7f000000, v10\n\t" \
"s_setprio 0\n\t" \
"s_cmp_lg_u64 vcc, 0\n\t" \
"s_cbranch_scc1 Lslow_%=\n\t" \
"Lback_%=:\n\t" \
"s_waitcnt lgkmcnt(0)\n\t" \
"s_barrier\n\t" \
"ds_read_b64 v[40:41], v17 offset:2048\n\t" \
"ds_read_b128 v[32:35], v16\n\t" \
"ds_read_b128 v[36:39], v16 offset:1024\n\t" \
"s_waitcnt lgkmcnt(2)\n\t" \
"v_max_f32 v14, v9, v41\n\t" \
"v_sub_f32 v15, v9, v14\n\t" \
"v_sub_f32 v16, v41, v14\n\t" \
"v_exp_f32 v15, v15\n\t" \
"v_exp_f32 v16, v16\n\t" \
"s_nop 0\n\t" \
"v_mul_f32 v14, v56, v15\n\t" \
"v_fmac_f32 v14, v40, v16\n\t" \
"v_rcp_f32 v14, v14\n\t" \
"s_nop 0\n\t" \
"v_mul_f32 v15, v15, v14\n\t" \
"v_mul_f32 v16, v16, v14\n\t" \
"v_pk_mul_f32 v[48:49], v[48:49], v[14:15] op_sel:[0,1] op_sel_hi:[1,1]\n\t" \
"v_pk_mul_f32 v[50:51], v[50:51], v[14:15] op_sel:[0,1] op_sel_hi:[1,1]\n\t" \
"v_pk_mul_f32 v[52:53], v[52:53], v[14:15] op_sel:[0,1] op_sel_hi:[1,1]\n\t" \
"v_pk_mul_f32 v[54:55], v[54:55], v[14:15] op_sel:[0,1] op_sel_hi:[1,1]\n\t" \
"s_waitcnt lgkmcnt(0)\n\t" \
"v_pk_fma_f32 v[48:49], v[32:33], v[16:17], v[48:49] op_sel_hi:[1,0,1]\n\t" \
"v_pk_fma_f32 v[50:51], v[34:35], v[16:17], v[50:51] op_sel_hi:[1,0,1]\n\t" \
"v_pk_fma_f32 v[52:53], v[36:37], v[16:17], v[52:53] op_sel_hi:[1,0,1]\n\t" \
"v_pk_fma_f32 v[54:55], v[38:39], v[16:17], v[54:55] op_sel_hi:[1,0,1]\n\t" \
"v_cvt_pk_bf16_f32 v64, v48, v49\n\t" \
"v_cvt_pk_bf16_f32 v65, v50, v51\n\t" \
"v_cvt_pk_bf16_f32 v66, v52, v53\n\t" \
"v_cvt_pk_bf16_f32 v67, v54, v55\n\t" \
"buffer_store_dwordx4 v[64:67], v1, s[28:31], s92 offen sc1\n\t" \
"s_lshl_b32 s80, s48, 3\n\t" \
"s_and_b32 s57, s52, 1\n\t" \
"s_lshl_b32 s57, s57, 2\n\t" \
"s_xor_b32 s57, s57, s26\n\t" \
"s_add_u32 s80, s80, s57\n\t" \
"s_lshl_b32 s57, s51, 1\n\t" \
"s_add_u32 s54, s54, s57\n\t" \
"s_lshl_b32 s57, s47, 3\n\t" \
"s_add_u32 s57, s57, s46\n\t" \
"s_lshl_b32 s57, s57, 7\n\t" \
"s_add_u32 s57, s57, 98304\n\t" \
"s_waitcnt vmcnt(0)\n\t" \
"s_barrier\n\t" \
"s_cmp_lg_u32 s26, 0\n\t" \
"s_cbranch_scc1 Lnof2_%=\n\t" \
"v_mov_b32 v14, s44\n\t" \
"v_mov_b32 v15, s45\n\t" \
"v_mov_b32 v16, s44\n\t" \
"v_mov_b32 v17, s45\n\t" \
"s_mov_b64 exec, 0xff\n\t" \
"buffer_store_dwordx4 v[14:17], v1, s[28:31], s57 offen sc1\n\t" \
"s_mov_b64 exec, -1\n\t" \
"Lnof2_%=:\n\t" \
"s_cmp_ge_u32 s26, 4\n\t" \
"s_cbranch_scc1 Lpathb_%=\n\t" \
"v_add_u32 v2, s62, v1\n\t" \
"v_add_u32 v14, 0x20000, v2\n\t" \
"ds_write_b128 v14, v[176:179] offset:0\n\t" \
"ds_write_b128 v14, v[180:183] offset:1024\n\t" \
"ds_write_b128 v14, v[184:187] offset:2048\n\t" \
"ds_write_b128 v14, v[188:191] offset:3072\n\t" \
"s_and_b32 s59, s56, 1\n\t" \
"s_lshl_b32 s59, s59, 6\n\t" \
"s_lshl_b32 s57, s52, 7\n\t" \
"s_add_u32 s57, s57, s59\n\t" \
"v_and_b32 v14, 31, v0\n\t" \
"v_lshl_add_u32 v15, v14, 1, s57\n\t" \
"global_load_short_d16_hi v12, v15, s[16:17]\n\t" \
"v_lshrrev_b32 v14, 5, v0\n\t" \
"v_bfe_u32 v6, v0, 2, 2\n\t" \
"v_lshl_add_u32 v6, v14, 2, v6\n\t" \
"v_lshl_add_u32 v6, v6, 7, v3\n\t" \
"v_add_u32 v6, 0x10000, v6\n\t" \
"v_xor_b32 v7, 64, v6\n\t" \
"v_and_b32 v14, 2, v0\n\t" \
"v_cmp_eq_u32 vcc, 0, v14\n\t" \
"v_mov_b32 v14, 0x3f803f80\n\t" \
"v_and_b32 v25, 1, v0\n\t" \
"v_cndmask_b32 v16, 0, v14, vcc\n\t" \
"v_cndmask_b32 v17, 0, v14, vcc\n\t" \
"v_xor_b32 v18, v14, v16\n\t" \
"v_xor_b32 v19, v14, v16\n\t" \
"v_mul_u32_u24 v25, 0xaaaa, v25\n\t" \
"v_add_u32 v25, 0x4444, v25\n\t" \
"v_xor_b32 v14, s59, v6\n\t" \
"v_add_u32 v15, 16384, v14\n\t" \
"s_cmp_ge_u32 s56, 2\n\t" \
"s_cbranch_scc1 Lanocs_%=\n\t" \
"s_waitcnt lgkmcnt(0)\n\t" \
"v_mov_b32 v20, 0\n\t" \
"v_mov_b32 v21, 0\n\t" \
"v_mov_b32 v22, 0\n\t" \
"v_mov_b32 v23, 0\n\t" \
"ds_read_b64_tr_b16 v[32:33], v14 offset:0\n\t" \
"ds_read_b64_tr_b16 v[34:35], v14 offset:1024\n\t" \
"ds_read_b64_tr_b16 v[36:37], v14 offset:2048\n\t" \
"ds_read_b64_tr_b16 v[38:39], v14 offset:3072\n\t" \
"ds_read_b64_tr_b16 v[40:41], v14 offset:4096\n\t" \
"ds_read_b64_tr_b16 v[42:43], v14 offset:5120\n\t" \
"ds_read_b64_tr_b16 v[44:45], v14 offset:6144\n\t" \
"ds_read_b64_tr_b16 v[46:47], v14 offset:7168\n\t" \
"ds_read_b64_tr_b16 v[48:49], v14 offset:8192\n\t" \
"ds_read_b64_tr_b16 v[50:51], v14 offset:9216\n\t" \
"ds_read_b64_tr_b16 v[52:53], v14 offset:10240\n\t" \
"ds_read_b64_tr_b16 v[54:55], v14 offset:11264\n\t" \
"ds_read_b64_tr_b16 v[56:57], v14 offset:12288\n\t" \
"ds_read_b64_tr_b16 v[58:59], v14 offset:13312\n\t" \
"s_waitcnt lgkmcnt(10)\n\t" \
"v_smfmac_f32_16x16x64_bf16 v[20:23], v[16:19], v[32:39], v25\n\t" \
"ds_read_b64_tr_b16 v[60:61], v14 offset:14336\n\t" \
"ds_read_b64_tr_b16 v[62:63], v14 offset:15360\n\t" \
"ds_read_b64_tr_b16 v[32:33], v15 offset:0\n\t" \
"ds_read_b64_tr_b16 v[34:35], v15 offset:1024\n\t" \
"s_waitcnt lgkmcnt(10)\n\t" \
"v_smfmac_f32_16x16x64_bf16 v[20:23], v[16:19], v[40:47], v25\n\t" \
"ds_read_b64_tr_b16 v[36:37], v15 offset:2048\n\t" \
"ds_read_b64_tr_b16 v[38:39], v15 offset:3072\n\t" \
"ds_read_b64_tr_b16 v[40:41], v15 offset:4096\n\t" \
"ds_read_b64_tr_b16 v[42:43], v15 offset:5120\n\t" \
"s_waitcnt lgkmcnt(10)\n\t" \
"v_smfmac_f32_16x16x64_bf16 v[20:23], v[16:19], v[48:55], v25\n\t" \
"ds_read_b64_tr_b16 v[44:45], v15 offset:6144\n\t" \
"ds_read_b64_tr_b16 v[46:47], v15 offset:7168\n\t" \
"ds_read_b64_tr_b16 v[48:49], v15 offset:8192\n\t" \
"ds_read_b64_tr_b16 v[50:51], v15 offset:9216\n\t" \
"s_waitcnt lgkmcnt(10)\n\t" \
"v_smfmac_f32_16x16x64_bf16 v[20:23], v[16:19], v[56:63], v25\n\t" \
"ds_read_b64_tr_b16 v[52:53], v15 offset:10240\n\t" \
"ds_read_b64_tr_b16 v[54:55], v15 offset:11264\n\t" \
"ds_read_b64_tr_b16 v[56:57], v15 offset:12288\n\t" \
"ds_read_b64_tr_b16 v[58:59], v15 offset:13312\n\t" \
"s_waitcnt lgkmcnt(10)\n\t" \
"v_smfmac_f32_16x16x64_bf16 v[20:23], v[16:19], v[32:39], v25\n\t" \
"ds_read_b64_tr_b16 v[60:61], v15 offset:14336\n\t" \
"ds_read_b64_tr_b16 v[62:63], v15 offset:15360\n\t" \
"ds_read_b64_tr_b16 v[32:33], v15 offset:16384\n\t" \
"ds_read_b64_tr_b16 v[34:35], v15 offset:17408\n\t" \
"s_waitcnt lgkmcnt(10)\n\t" \
"v_smfmac_f32_16x16x64_bf16 v[20:23], v[16:19], v[40:47], v25\n\t" \
"ds_read_b64_tr_b16 v[36:37], v15 offset:18432\n\t" \
"ds_read_b64_tr_b16 v[38:39], v15 offset:19456\n\t" \
"ds_read_b64_tr_b16 v[40:41], v15 offset:20480\n\t" \
"ds_read_b64_tr_b16 v[42:43], v15 offset:21504\n\t" \
"s_waitcnt lgkmcnt(10)\n\t" \
"v_smfmac_f32_16x16x64_bf16 v[20:23], v[16:19], v[48:55], v25\n\t" \
"ds_read_b64_tr_b16 v[44:45], v15 offset:22528\n\t" \
"ds_read_b64_tr_b16 v[46:47], v15 offset:23552\n\t" \
"ds_read_b64_tr_b16 v[48:49], v15 offset:24576\n\t" \
"ds_read_b64_tr_b16 v[50:51], v15 offset:25600\n\t" \
"s_waitcnt lgkmcnt(10)\n\t" \
"v_smfmac_f32_16x16x64_bf16 v[20:23], v[16:19], v[56:63], v25\n\t" \
"ds_read_b64_tr_b16 v[52:53], v15 offset:26624\n\t" \
"ds_read_b64_tr_b16 v[54:55], v15 offset:27648\n\t" \
"ds_read_b64_tr_b16 v[56:57], v15 offset:28672\n\t" \
"ds_read_b64_tr_b16 v[58:59], v15 offset:29696\n\t" \
"s_waitcnt lgkmcnt(10)\n\t" \
"v_smfmac_f32_16x16x64_bf16 v[20:23], v[16:19], v[32:39], v25\n\t" \
"ds_read_b64_tr_b16 v[60:61], v15 offset:30720\n\t" \
"ds_read_b64_tr_b16 v[62:63], v15 offset:31744\n\t" \
"ds_read_b64_tr_b16 v[32:33], v15 offset:32768\n\t" \
"ds_read_b64_tr_b16 v[34:35], v15 offset:33792\n\t" \
"s_waitcnt lgkmcnt(10)\n\t" \
"v_smfmac_f32_16x16x64_bf16 v[20:23], v[16:19], v[40:47], v25\n\t" \
"ds_read_b64_tr_b16 v[36:37], v15 offset:34816\n\t" \
"ds_read_b64_tr_b16 v[38:39], v15 offset:35840\n\t" \
"ds_read_b64_tr_b16 v[40:41], v15 offset:36864\n\t" \
"ds_read_b64_tr_b16 v[42:43], v15 offset:37888\n\t" \
"s_waitcnt lgkmcnt(10)\n\t" \
"v_smfmac_f32_16x16x64_bf16 v[20:23], v[16:19], v[48:55], v25\n\t" \
"ds_read_b64_tr_b16 v[44:45], v15 offset:38912\n\t" \
"ds_read_b64_tr_b16 v[46:47], v15 offset:39936\n\t" \
"ds_read_b64_tr_b16 v[48:49], v15 offset:40960\n\t" \
"ds_read_b64_tr_b16 v[50:51], v15 offset:41984\n\t" \
"s_waitcnt lgkmcnt(10)\n\t" \
"v_smfmac_f32_16x16x64_bf16 v[20:23], v[16:19], v[56:63], v25\n\t" \
"ds_read_b64_tr_b16 v[52:53], v15 offset:43008\n\t" \
"ds_read_b64_tr_b16 v[54:55], v15 offset:44032\n\t" \
"ds_read_b64_tr_b16 v[56:57], v15 offset:45056\n\t" \
"ds_read_b64_tr_b16 v[58:59], v15 offset:46080\n\t" \
"s_waitcnt lgkmcnt(10)\n\t" \
"v_smfmac_f32_16x16x64_bf16 v[20:23], v[16:19], v[32:39], v25\n\t" \
"ds_read_b64_tr_b16 v[60:61], v15 offset:47104\n\t" \
"ds_read_b64_tr_b16 v[62:63], v15 offset:48128\n\t" \
"s_waitcnt lgkmcnt(8)\n\t" \
"v_smfmac_f32_16x16x64_bf16 v[20:23], v[16:19], v[40:47], v25\n\t" \
"s_waitcnt lgkmcnt(4)\n\t" \
"v_smfmac_f32_16x16x64_bf16 v[20:23], v[16:19], v[48:55], v25\n\t" \
"s_waitcnt lgkmcnt(0)\n\t" \
"v_smfmac_f32_16x16x64_bf16 v[20:23], v[16:19], v[56:63], v25\n\t" \
"s_nop 7\n\t" \
"v_add_f32 v20, v20, v21\n\t" \
"v_add_f32 v21, v22, v23\n\t" \
"Lanocs_%=:\n\t" \
"s_lshl_b32 s58, s59, 1\n\t" \
"s_add_u32 s58, s58, 147456\n\t" \
"v_and_b32 v14, 31, v0\n\t" \
"v_lshl_add_u32 v14, v14, 2, s58\n\t" \
"s_cmp_ge_u32 s56, 2\n\t" \
"s_cselect_b32 s58, 161792, s58\n\t" \
"v_cmp_gt_u32 vcc, 16, v0\n\t" \
"v_lshl_add_u32 v15, v0, 2, s58\n\t" \
"v_mov_b32 v24, 161792\n\t" \
"v_cndmask_b32 v15, v24, v15, vcc\n\t" \
"ds_read_b64_tr_b16 v[64:65], v6 offset:0\n\t" \
"ds_read_b64_tr_b16 v[66:67], v6 offset:1024\n\t" \
"ds_read_b64_tr_b16 v[68:69], v7 offset:0\n\t" \
"ds_read_b64_tr_b16 v[70:71], v7 offset:1024\n\t" \
"ds_read_b64_tr_b16 v[72:73], v6 offset:2048\n\t" \
"ds_read_b64_tr_b16 v[74:75], v6 offset:3072\n\t" \
"ds_read_b64_tr_b16 v[76:77], v7 offset:2048\n\t" \
"ds_read_b64_tr_b16 v[78:79], v7 offset:3072\n\t" \
"ds_read_b64_tr_b16 v[80:81], v6 offset:4096\n\t" \
"ds_read_b64_tr_b16 v[82:83], v6 offset:5120\n\t" \
"ds_read_b64_tr_b16 v[84:85], v7 offset:4096\n\t" \
"ds_read_b64_tr_b16 v[86:87], v7 offset:5120\n\t" \
"s_mul_i32 s77, s80, 114688\n\t" \
"s_lshl_b32 s57, s52, 12\n\t" \
"s_add_u32 s77, s77, s57\n\t" \
"s_add_u32 s77, s77, 9699328\n\t" \
"s_lshl_b32 s76, s80, 6\n\t" \
"s_lshr_b32 s57, s52, 1\n\t" \
"s_add_u32 s76, s76, s57\n\t" \
"s_lshl_b32 s76, s76, 7\n\t" \
"s_add_u32 s76, s76, 262144\n\t" \
"s_mov_b32 s67, 0xbdd2d3e8\n\t" \
"s_lshl_b32 s57, s80, 11\n\t" \
"s_add_u32 s57, s57, 131072\n\t" \
"s_lshl_b32 s58, s80, 15\n\t" \
"s_add_u32 s58, s58, 7340032\n\t" \
"s_waitcnt vmcnt(0)\n\t" \
"s_barrier\n\t" \
"s_sleep 2\n\t" \
"ds_write_b128 v2, v[192:195] offset:0\n\t" \
"ds_write_b128 v2, v[196:199] offset:1024\n\t" \
"ds_write_b128 v2, v[200:203] offset:2048\n\t" \
"ds_write_b128 v2, v[204:207] offset:3072\n\t" \
"ds_write_b128 v2, v[208:211] offset:16384\n\t" \
"ds_write_b128 v2, v[212:215] offset:17408\n\t" \
"ds_write_b128 v2, v[216:219] offset:18432\n\t" \
"ds_write_b128 v2, v[220:223] offset:19456\n\t" \
"ds_write_b128 v2, v[224:227] offset:32768\n\t" \
"ds_write_b128 v2, v[228:231] offset:33792\n\t" \
"ds_write_b128 v2, v[232:235] offset:34816\n\t" \
"ds_write_b128 v2, v[236:239] offset:35840\n\t" \
"ds_write_b128 v2, v[240:243] offset:49152\n\t" \
"ds_write_b128 v2, v[244:247] offset:50176\n\t" \
"ds_write_b128 v2, v[248:251] offset:51200\n\t" \
"ds_write_b128 v2, v[252:255] offset:52224\n\t" \
"s_barrier\n\t" \
"s_waitcnt lgkmcnt(12)\n\t" \
"s_add_u32 s63, s60, 819200\n\t" \
"buffer_load_dwordx4 v[192:195], v11, s[36:39], s63 offen\n\t" \
"s_add_u32 s63, s60, 827392\n\t" \
"buffer_load_dwordx4 v[196:199], v11, s[36:39], s63 offen\n\t" \
"s_add_u32 s63, s60, 835584\n\t" \
"buffer_load_dwordx4 v[200:203], v11, s[36:39], s63 offen\n\t" \
"s_add_u32 s63, s60, 843776\n\t" \
"buffer_load_dwordx4 v[204:207], v11, s[36:39], s63 offen\n\t" \
"s_waitcnt lgkmcnt(8)\n\t" \
"s_add_u32 s63, s60, 1343488\n\t" \
"buffer_load_dwordx4 v[208:211], v11, s[36:39], s63 offen\n\t" \
"s_add_u32 s63, s60, 1351680\n\t" \
"buffer_load_dwordx4 v[212:215], v11, s[36:39], s63 offen\n\t" \
"s_add_u32 s63, s60, 1359872\n\t" \
"buffer_load_dwordx4 v[216:219], v11, s[36:39], s63 offen\n\t" \
"s_add_u32 s63, s60, 1368064\n\t" \
"buffer_load_dwordx4 v[220:223], v11, s[36:39], s63 offen\n\t" \
"s_waitcnt lgkmcnt(4)\n\t" \
"s_add_u32 s63, s53, 65536\n\t" \
"buffer_load_dwordx4 v[224:227], v11, s[36:39], s63 offen\n\t" \
"s_add_u32 s63, s53, 73728\n\t" \
"buffer_load_dwordx4 v[228:231], v11, s[36:39], s63 offen\n\t" \
"s_add_u32 s63, s53, 81920\n\t" \
"buffer_load_dwordx4 v[232:235], v11, s[36:39], s63 offen\n\t" \
"s_add_u32 s63, s53, 90112\n\t" \
"buffer_load_dwordx4 v[236:239], v11, s[36:39], s63 offen\n\t" \
"s_waitcnt lgkmcnt(0)\n\t" \
"s_add_u32 s63, s53, 589824\n\t" \
"buffer_load_dwordx4 v[240:243], v11, s[36:39], s63 offen\n\t" \
"s_add_u32 s63, s53, 598016\n\t" \
"buffer_load_dwordx4 v[244:247], v11, s[36:39], s63 offen\n\t" \
"s_add_u32 s63, s53, 606208\n\t" \
"buffer_load_dwordx4 v[248:251], v11, s[36:39], s63 offen\n\t" \
"s_add_u32 s63, s53, 614400\n\t" \
"buffer_load_dwordx4 v[252:255], v11, s[36:39], s63 offen\n\t" \
"s_cmp_ge_u32 s49, 28\n\t" \
"s_cbranch_scc1 Lnom1_%=\n\t" \
"v_and_b32 v12, 0xffff0000, v12\n\t" \
"ds_write_b32 v14, v12 offset:512\n\t" \
"ds_write2_b32 v15, v20, v21 offset0:0 offset1:16\n\t" \
"v_lshrrev_b32 v14, 5, v0\n\t" \
"v_lshlrev_b32 v14, 4, v14\n\t" \
"v_add_u32 v10, 147456, v14\n\t" \
"v_mov_b32 v12, 0xc0135761\n\t" \
"s_waitcnt lgkmcnt(0)\n\t" \
"s_barrier\n\t" \
"s_lshl_b32 s59, s26, 8\n\t" \
"s_add_u32 s59, s59, 149504\n\t" \
"v_and_b32 v180, 15, v0\n\t" \
"v_lshl_add_u32 v180, v180, 4, s59\n\t" \
"v_and_b32 v181, 31, v0\n\t" \
"v_lshl_add_u32 v181, v181, 3, s59\n\t" \
"s_lshl_b32 s59, s80, 12\n\t" \
"s_add_u32 s59, s59, 9437184\n\t" \
"v_min_u32 v14, 15, v0\n\t" \
"v_lshlrev_b32 v14, 7, v14\n\t" \
"s_mov_b32 s65, 0x40000\n\t" \
"Lpoll3_%=:\n\t" \
"buffer_load_dwordx2 v[16:17], v14, s[28:31], s57 offen sc1\n\t" \
"s_waitcnt vmcnt(0)\n\t" \
"v_cmp_ne_u64 vcc, s[44:45], v[16:17]\n\t" \
"s_nop 0\n\t" \
"s_cmp_eq_u64 vcc, 0\n\t" \
"s_cbranch_scc1 Lpdone3_%=\n\t" \
"s_sleep 1\n\t" \
"s_sub_u32 s65, s65, 1\n\t" \
"s_cmp_lg_u32 s65, 0\n\t" \
"s_cbranch_scc1 Lpoll3_%=\n\t" \
"Lpdone3_%=:\n\t" \
"s_setprio 3\n\t" \
"buffer_load_dwordx4 v[96:99], v1, s[28:31], s58 offen offset:0 sc1\n\t" \
"buffer_load_dwordx4 v[100:103], v1, s[28:31], s58 offen offset:1024 sc1\n\t" \
"buffer_load_dwordx4 v[104:107], v1, s[28:31], s58 offen offset:2048 sc1\n\t" \
"buffer_load_dwordx4 v[108:111], v1, s[28:31], s58 offen offset:3072 sc1\n\t" \
"s_add_u32 s58, s58, 4096\n\t" \
"buffer_load_dwordx4 v[112:115], v1, s[28:31], s58 offen offset:0 sc1\n\t" \
"buffer_load_dwordx4 v[116:119], v1, s[28:31], s58 offen offset:1024 sc1\n\t" \
"buffer_load_dwordx4 v[120:123], v1, s[28:31], s58 offen offset:2048 sc1\n\t" \
"buffer_load_dwordx4 v[124:127], v1, s[28:31], s58 offen offset:3072 sc1\n\t" \
"s_add_u32 s58, s58, 4096\n\t" \
"buffer_load_dwordx4 v[128:131], v1, s[28:31], s58 offen offset:0 sc1\n\t" \
"buffer_load_dwordx4 v[132:135], v1, s[28:31], s58 offen offset:1024 sc1\n\t" \
"buffer_load_dwordx4 v[136:139], v1, s[28:31], s58 offen offset:2048 sc1\n\t" \
"buffer_load_dwordx4 v[140:143], v1, s[28:31], s58 offen offset:3072 sc1\n\t" \
"s_waitcnt vmcnt(11)\n\t" \
"v_mfma_f32_32x32x16_bf16 v[32:47], v[64:67], v[96:99], 0\n\t" \
"ds_read_b64_tr_b16 v[88:89], v6 offset:6144\n\t" \
"ds_read_b64_tr_b16 v[90:91], v6 offset:7168\n\t" \
"v_mfma_f32_32x32x16_bf16 v[48:63], v[68:71], v[96:99], 0\n\t" \
"ds_read_b64_tr_b16 v[92:93], v7 offset:6144\n\t" \
"ds_read_b64_tr_b16 v[94:95], v7 offset:7168\n\t" \
"s_add_u32 s58, s58, 4096\n\t" \
"buffer_load_dwordx4 v[144:147], v1, s[28:31], s58 offen offset:0 sc1\n\t" \
"s_waitcnt vmcnt(11)\n\t" \
"v_mfma_f32_32x32x16_bf16 v[32:47], v[72:75], v[100:103], v[32:47]\n\t" \
"ds_read_b64_tr_b16 v[64:65], v6 offset:8192\n\t" \
"ds_read_b64_tr_b16 v[66:67], v6 offset:9216\n\t" \
"v_mfma_f32_32x32x16_bf16 v[48:63], v[76:79], v[100:103], v[48:63]\n\t" \
"ds_read_b64_tr_b16 v[68:69], v7 offset:8192\n\t" \
"ds_read_b64_tr_b16 v[70:71], v7 offset:9216\n\t" \
"buffer_load_dwordx4 v[148:151], v1, s[28:31], s58 offen offset:1024 sc1\n\t" \
"s_waitcnt vmcnt(11)\n\t" \
"v_mfma_f32_32x32x16_bf16 v[32:47], v[80:83], v[104:107], v[32:47]\n\t" \
"ds_read_b64_tr_b16 v[72:73], v6 offset:10240\n\t" \
"ds_read_b64_tr_b16 v[74:75], v6 offset:11264\n\t" \
"v_mfma_f32_32x32x16_bf16 v[48:63], v[84:87], v[104:107], v[48:63]\n\t" \
"ds_read_b64_tr_b16 v[76:77], v7 offset:10240\n\t" \
"ds_read_b64_tr_b16 v[78:79], v7 offset:11264\n\t" \
"buffer_load_dwordx4 v[152:155], v1, s[28:31], s58 offen offset:2048 sc1\n\t" \
"s_waitcnt lgkmcnt(8) vmcnt(11)\n\t" \
"v_mfma_f32_32x32x16_bf16 v[32:47], v[88:91], v[108:111], v[32:47]\n\t" \
"ds_read_b64_tr_b16 v[80:81], v6 offset:12288\n\t" \
"ds_read_b64_tr_b16 v[82:83], v6 offset:13312\n\t" \
"v_mfma_f32_32x32x16_bf16 v[48:63], v[92:95], v[108:111], v[48:63]\n\t" \
"ds_read_b64_tr_b16 v[84:85], v7 offset:12288\n\t" \
"ds_read_b64_tr_b16 v[86:87], v7 offset:13312\n\t" \
"buffer_load_dwordx4 v[156:159], v1, s[28:31], s58 offen offset:3072 sc1\n\t" \
"s_waitcnt lgkmcnt(8) vmcnt(11)\n\t" \
"v_mfma_f32_32x32x16_bf16 v[32:47], v[64:67], v[112:115], v[32:47]\n\t" \
"ds_read_b64_tr_b16 v[88:89], v6 offset:14336\n\t" \
"ds_read_b64_tr_b16 v[90:91], v6 offset:15360\n\t" \
"v_mfma_f32_32x32x16_bf16 v[48:63], v[68:71], v[112:115], v[48:63]\n\t" \
"ds_read_b64_tr_b16 v[92:93], v7 offset:14336\n\t" \
"ds_read_b64_tr_b16 v[94:95], v7 offset:15360\n\t" \
"s_add_u32 s58, s58, 4096\n\t" \
"buffer_load_dwordx4 v[96:99], v1, s[28:31], s58 offen offset:0 sc1\n\t" \
"s_waitcnt lgkmcnt(8) vmcnt(11)\n\t" \
"v_mfma_f32_32x32x16_bf16 v[32:47], v[72:75], v[116:119], v[32:47]\n\t" \
"ds_read_b64_tr_b16 v[64:65], v6 offset:16384\n\t" \
"ds_read_b64_tr_b16 v[66:67], v6 offset:17408\n\t" \
"v_mfma_f32_32x32x16_bf16 v[48:63], v[76:79], v[116:119], v[48:63]\n\t" \
"ds_read_b64_tr_b16 v[68:69], v7 offset:16384\n\t" \
"ds_read_b64_tr_b16 v[70:71], v7 offset:17408\n\t" \
"buffer_load_dwordx4 v[100:103], v1, s[28:31], s58 offen offset:1024 sc1\n\t" \
"s_waitcnt lgkmcnt(8) vmcnt(11)\n\t" \
"v_mfma_f32_32x32x16_bf16 v[32:47], v[80:83], v[120:123], v[32:47]\n\t" \
"ds_read_b64_tr_b16 v[72:73], v6 offset:18432\n\t" \
"ds_read_b64_tr_b16 v[74:75], v6 offset:19456\n\t" \
"v_mfma_f32_32x32x16_bf16 v[48:63], v[84:87], v[120:123], v[48:63]\n\t" \
"ds_read_b64_tr_b16 v[76:77], v7 offset:18432\n\t" \
"ds_read_b64_tr_b16 v[78:79], v7 offset:19456\n\t" \
"buffer_load_dwordx4 v[104:107], v1, s[28:31], s58 offen offset:2048 sc1\n\t" \
"s_waitcnt lgkmcnt(8) vmcnt(11)\n\t" \
"v_mfma_f32_32x32x16_bf16 v[32:47], v[88:91], v[124:127], v[32:47]\n\t" \
"ds_read_b64_tr_b16 v[80:81], v6 offset:20480\n\t" \
"ds_read_b64_tr_b16 v[82:83], v6 offset:21504\n\t" \
"v_mfma_f32_32x32x16_bf16 v[48:63], v[92:95], v[124:127], v[48:63]\n\t" \
"ds_read_b64_tr_b16 v[84:85], v7 offset:20480\n\t" \
"ds_read_b64_tr_b16 v[86:87], v7 offset:21504\n\t" \
"buffer_load_dwordx4 v[108:111], v1, s[28:31], s58 offen offset:3072 sc1\n\t" \
"s_waitcnt lgkmcnt(8) vmcnt(11)\n\t" \
"v_mfma_f32_32x32x16_bf16 v[32:47], v[64:67], v[128:131], v[32:47]\n\t" \
"ds_read_b64_tr_b16 v[88:89], v6 offset:22528\n\t" \
"ds_read_b64_tr_b16 v[90:91], v6 offset:23552\n\t" \
"v_mfma_f32_32x32x16_bf16 v[48:63], v[68:71], v[128:131], v[48:63]\n\t" \
"ds_read_b64_tr_b16 v[92:93], v7 offset:22528\n\t" \
"ds_read_b64_tr_b16 v[94:95], v7 offset:23552\n\t" \
"s_add_u32 s58, s58, 4096\n\t" \
"buffer_load_dwordx4 v[112:115], v1, s[28:31], s58 offen offset:0 sc1\n\t" \
"s_waitcnt lgkmcnt(8) vmcnt(11)\n\t" \
"v_mfma_f32_32x32x16_bf16 v[32:47], v[72:75], v[132:135], v[32:47]\n\t" \
"ds_read_b64_tr_b16 v[64:65], v6 offset:24576\n\t" \
"ds_read_b64_tr_b16 v[66:67], v6 offset:25600\n\t" \
"v_mfma_f32_32x32x16_bf16 v[48:63], v[76:79], v[132:135], v[48:63]\n\t" \
"ds_read_b64_tr_b16 v[68:69], v7 offset:24576\n\t" \
"ds_read_b64_tr_b16 v[70:71], v7 offset:25600\n\t" \
"buffer_load_dwordx4 v[116:119], v1, s[28:31], s58 offen offset:1024 sc1\n\t" \
"s_waitcnt lgkmcnt(8) vmcnt(11)\n\t" \
"v_mfma_f32_32x32x16_bf16 v[32:47], v[80:83], v[136:139], v[32:47]\n\t" \
"ds_read_b64_tr_b16 v[72:73], v6 offset:26624\n\t" \
"ds_read_b64_tr_b16 v[74:75], v6 offset:27648\n\t" \
"v_mfma_f32_32x32x16_bf16 v[48:63], v[84:87], v[136:139], v[48:63]\n\t" \
"ds_read_b64_tr_b16 v[76:77], v7 offset:26624\n\t" \
"ds_read_b64_tr_b16 v[78:79], v7 offset:27648\n\t" \
"buffer_load_dwordx4 v[120:123], v1, s[28:31], s58 offen offset:2048 sc1\n\t" \
"s_waitcnt lgkmcnt(8) vmcnt(11)\n\t" \
"v_mfma_f32_32x32x16_bf16 v[32:47], v[88:91], v[140:143], v[32:47]\n\t" \
"ds_read_b64_tr_b16 v[80:81], v6 offset:28672\n\t" \
"ds_read_b64_tr_b16 v[82:83], v6 offset:29696\n\t" \
"v_mfma_f32_32x32x16_bf16 v[48:63], v[92:95], v[140:143], v[48:63]\n\t" \
"ds_read_b64_tr_b16 v[84:85], v7 offset:28672\n\t" \
"ds_read_b64_tr_b16 v[86:87], v7 offset:29696\n\t" \
"buffer_load_dwordx4 v[124:127], v1, s[28:31], s58 offen offset:3072 sc1\n\t" \
"s_waitcnt lgkmcnt(8) vmcnt(11)\n\t" \
"v_mfma_f32_32x32x16_bf16 v[32:47], v[64:67], v[144:147], v[32:47]\n\t" \
"ds_read_b64_tr_b16 v[88:89], v6 offset:30720\n\t" \
"ds_read_b64_tr_b16 v[90:91], v6 offset:31744\n\t" \
"v_mfma_f32_32x32x16_bf16 v[48:63], v[68:71], v[144:147], v[48:63]\n\t" \
"ds_read_b64_tr_b16 v[92:93], v7 offset:30720\n\t" \
"ds_read_b64_tr_b16 v[94:95], v7 offset:31744\n\t" \
"s_add_u32 s58, s58, 4096\n\t" \
"buffer_load_dwordx4 v[128:131], v1, s[28:31], s58 offen offset:0 sc1\n\t" \
"s_waitcnt lgkmcnt(8) vmcnt(11)\n\t" \
"v_mfma_f32_32x32x16_bf16 v[32:47], v[72:75], v[148:151], v[32:47]\n\t" \
"ds_read_b64_tr_b16 v[64:65], v6 offset:32768\n\t" \
"ds_read_b64_tr_b16 v[66:67], v6 offset:33792\n\t" \
"buffer_load_dwordx4 v[160:163], v1, s[28:31], s59 offen offset:0 sc1\n\t" \
"v_mfma_f32_32x32x16_bf16 v[48:63], v[76:79], v[148:151], v[48:63]\n\t" \
"ds_read_b64_tr_b16 v[68:69], v7 offset:32768\n\t" \
"ds_read_b64_tr_b16 v[70:71], v7 offset:33792\n\t" \
"buffer_load_dwordx4 v[132:135], v1, s[28:31], s58 offen offset:1024 sc1\n\t" \
"s_waitcnt lgkmcnt(8) vmcnt(12)\n\t" \
"v_mfma_f32_32x32x16_bf16 v[32:47], v[80:83], v[152:155], v[32:47]\n\t" \
"ds_read_b64_tr_b16 v[72:73], v6 offset:34816\n\t" \
"ds_read_b64_tr_b16 v[74:75], v6 offset:35840\n\t" \
"buffer_load_dwordx4 v[164:167], v1, s[28:31], s59 offen offset:1024 sc1\n\t" \
"v_mfma_f32_32x32x16_bf16 v[48:63], v[84:87], v[152:155], v[48:63]\n\t" \
"ds_read_b64_tr_b16 v[76:77], v7 offset:34816\n\t" \
"ds_read_b64_tr_b16 v[78:79], v7 offset:35840\n\t" \
"buffer_load_dwordx4 v[136:139], v1, s[28:31], s58 offen offset:2048 sc1\n\t" \
"s_waitcnt lgkmcnt(8) vmcnt(13)\n\t" \
"v_mfma_f32_32x32x16_bf16 v[32:47], v[88:91], v[156:159], v[32:47]\n\t" \
"ds_read_b64_tr_b16 v[80:81], v6 offset:36864\n\t" \
"ds_read_b64_tr_b16 v[82:83], v6 offset:37888\n\t" \
"buffer_load_dwordx4 v[168:171], v1, s[28:31], s59 offen offset:2048 sc1\n\t" \
"v_mfma_f32_32x32x16_bf16 v[48:63], v[92:95], v[156:159], v[48:63]\n\t" \
"ds_read_b64_tr_b16 v[84:85], v7 offset:36864\n\t" \
"ds_read_b64_tr_b16 v[86:87], v7 offset:37888\n\t" \
"buffer_load_dwordx4 v[140:143], v1, s[28:31], s58 offen offset:3072 sc1\n\t" \
"s_waitcnt lgkmcnt(8) vmcnt(14)\n\t" \
"v_mfma_f32_32x32x16_bf16 v[32:47], v[64:67], v[96:99], v[32:47]\n\t" \
"ds_read_b64_tr_b16 v[88:89], v6 offset:38912\n\t" \
"ds_read_b64_tr_b16 v[90:91], v6 offset:39936\n\t" \
"buffer_load_dwordx4 v[172:175], v1, s[28:31], s59 offen offset:3072 sc1\n\t" \
"v_mfma_f32_32x32x16_bf16 v[48:63], v[68:71], v[96:99], v[48:63]\n\t" \
"ds_read_b64_tr_b16 v[92:93], v7 offset:38912\n\t" \
"ds_read_b64_tr_b16 v[94:95], v7 offset:39936\n\t" \
"s_add_u32 s58, s58, 4096\n\t" \
"buffer_load_dwordx4 v[144:147], v1, s[28:31], s58 offen offset:0 sc1\n\t" \
"s_waitcnt lgkmcnt(8) vmcnt(15)\n\t" \
"v_mfma_f32_32x32x16_bf16 v[32:47], v[72:75], v[100:103], v[32:47]\n\t" \
"ds_read_b64_tr_b16 v[64:65], v6 offset:40960\n\t" \
"ds_read_b64_tr_b16 v[66:67], v6 offset:41984\n\t" \
"v_mfma_f32_32x32x16_bf16 v[48:63], v[76:79], v[100:103], v[48:63]\n\t" \
"ds_read_b64_tr_b16 v[68:69], v7 offset:40960\n\t" \
"ds_read_b64_tr_b16 v[70:71], v7 offset:41984\n\t" \
"buffer_load_dwordx4 v[148:151], v1, s[28:31], s58 offen offset:1024 sc1\n\t" \
"s_waitcnt lgkmcnt(8) vmcnt(15)\n\t" \
"v_mfma_f32_32x32x16_bf16 v[32:47], v[80:83], v[104:107], v[32:47]\n\t" \
"ds_read_b64_tr_b16 v[72:73], v6 offset:43008\n\t" \
"ds_read_b64_tr_b16 v[74:75], v6 offset:44032\n\t" \
"v_mfma_f32_32x32x16_bf16 v[48:63], v[84:87], v[104:107], v[48:63]\n\t" \
"ds_read_b64_tr_b16 v[76:77], v7 offset:43008\n\t" \
"ds_read_b64_tr_b16 v[78:79], v7 offset:44032\n\t" \
"buffer_load_dwordx4 v[152:155], v1, s[28:31], s58 offen offset:2048 sc1\n\t" \
"s_waitcnt lgkmcnt(8) vmcnt(15)\n\t" \
"v_mfma_f32_32x32x16_bf16 v[32:47], v[88:91], v[108:111], v[32:47]\n\t" \
"ds_read_b64_tr_b16 v[80:81], v6 offset:45056\n\t" \
"ds_read_b64_tr_b16 v[82:83], v6 offset:46080\n\t" \
"v_mfma_f32_32x32x16_bf16 v[48:63], v[92:95], v[108:111], v[48:63]\n\t" \
"ds_read_b64_tr_b16 v[84:85], v7 offset:45056\n\t" \
"ds_read_b64_tr_b16 v[86:87], v7 offset:46080\n\t" \
"buffer_load_dwordx4 v[156:159], v1, s[28:31], s58 offen offset:3072 sc1\n\t" \
"s_barrier\n\t" \
"s_waitcnt lgkmcnt(8) vmcnt(15)\n\t" \
"v_mfma_f32_32x32x16_bf16 v[32:47], v[64:67], v[112:115], v[32:47]\n\t" \
"ds_read_b64_tr_b16 v[88:89], v6 offset:47104\n\t" \
"ds_read_b64_tr_b16 v[90:91], v6 offset:48128\n\t" \
"v_mfma_f32_32x32x16_bf16 v[48:63], v[68:71], v[112:115], v[48:63]\n\t" \
"ds_read_b64_tr_b16 v[92:93], v7 offset:47104\n\t" \
"ds_read_b64_tr_b16 v[94:95], v7 offset:48128\n\t" \
"s_waitcnt lgkmcnt(8) vmcnt(14)\n\t" \
"v_mfma_f32_32x32x16_bf16 v[32:47], v[72:75], v[116:119], v[32:47]\n\t" \
"ds_read_b64_tr_b16 v[64:65], v6 offset:49152\n\t" \
"ds_read_b64_tr_b16 v[66:67], v6 offset:50176\n\t" \
"v_mfma_f32_32x32x16_bf16 v[48:63], v[76:79], v[116:119], v[48:63]\n\t" \
"ds_read_b64_tr_b16 v[68:69], v7 offset:49152\n\t" \
"ds_read_b64_tr_b16 v[70:71], v7 offset:50176\n\t" \
"s_waitcnt lgkmcnt(8) vmcnt(13)\n\t" \
"v_mfma_f32_32x32x16_bf16 v[32:47], v[80:83], v[120:123], v[32:47]\n\t" \
"ds_read_b64_tr_b16 v[72:73], v6 offset:51200\n\t" \
"ds_read_b64_tr_b16 v[74:75], v6 offset:52224\n\t" \
"ds_read_b128 v[16:19], v10 offset:0\n\t" \
"v_mfma_f32_32x32x16_bf16 v[48:63], v[84:87], v[120:123], v[48:63]\n\t" \
"ds_read_b64_tr_b16 v[76:77], v7 offset:51200\n\t" \
"ds_read_b64_tr_b16 v[78:79], v7 offset:52224\n\t" \
"ds_read_b128 v[20:23], v10 offset:512\n\t" \
"s_waitcnt lgkmcnt(10) vmcnt(12)\n\t" \
"v_mfma_f32_32x32x16_bf16 v[32:47], v[88:91], v[124:127], v[32:47]\n\t" \
"ds_read_b64_tr_b16 v[80:81], v6 offset:53248\n\t" \
"ds_read_b64_tr_b16 v[82:83], v6 offset:54272\n\t" \
"v_mfma_f32_32x32x16_bf16 v[48:63], v[92:95], v[124:127], v[48:63]\n\t" \
"ds_read_b64_tr_b16 v[84:85], v7 offset:53248\n\t" \
"ds_read_b64_tr_b16 v[86:87], v7 offset:54272\n\t" \
"ds_read_b128 v[24:27], v10 offset:32\n\t" \
"s_waitcnt lgkmcnt(11) vmcnt(11)\n\t" \
"v_mfma_f32_32x32x16_bf16 v[32:47], v[64:67], v[128:131], v[32:47]\n\t" \
"ds_read_b64_tr_b16 v[88:89], v6 offset:55296\n\t" \
"ds_read_b64_tr_b16 v[90:91], v6 offset:56320\n\t" \
"ds_read_b128 v[28:31], v10 offset:544\n\t" \
"v_mfma_f32_32x32x16_bf16 v[48:63], v[68:71], v[128:131], v[48:63]\n\t" \
"ds_read_b64_tr_b16 v[92:93], v7 offset:55296\n\t" \
"ds_read_b64_tr_b16 v[94:95], v7 offset:56320\n\t" \
"s_waitcnt lgkmcnt(11) vmcnt(9)\n\t" \
"v_mfma_f32_32x32x16_bf16 v[32:47], v[72:75], v[132:135], v[32:47]\n\t" \
"ds_read_b64_tr_b16 v[64:65], v6 offset:57344\n\t" \
"ds_read_b64_tr_b16 v[66:67], v6 offset:58368\n\t" \
"s_waitcnt vmcnt(4)\n\t" \
"v_add_f32 v160, v160, v164\n\t" \
"v_add_f32 v161, v161, v165\n\t" \
"v_add_f32 v162, v162, v166\n\t" \
"v_add_f32 v163, v163, v167\n\t" \
"v_mfma_f32_32x32x16_bf16 v[48:63], v[76:79], v[132:135], v[48:63]\n\t" \
"ds_read_b64_tr_b16 v[68:69], v7 offset:57344\n\t" \
"ds_read_b64_tr_b16 v[70:71], v7 offset:58368\n\t" \
"v_add_f32 v160, v160, v168\n\t" \
"v_add_f32 v161, v161, v169\n\t" \
"v_add_f32 v162, v162, v170\n\t" \
"v_add_f32 v163, v163, v171\n\t" \
"s_waitcnt lgkmcnt(10)\n\t" \
"v_mfma_f32_32x32x16_bf16 v[32:47], v[80:83], v[136:139], v[32:47]\n\t" \
"ds_read_b64_tr_b16 v[72:73], v6 offset:59392\n\t" \
"ds_read_b64_tr_b16 v[74:75], v6 offset:60416\n\t" \
"v_add_f32 v160, v160, v172\n\t" \
"v_add_f32 v161, v161, v173\n\t" \
"v_add_f32 v162, v162, v174\n\t" \
"v_add_f32 v163, v163, v175\n\t" \
"v_mfma_f32_32x32x16_bf16 v[48:63], v[84:87], v[136:139], v[48:63]\n\t" \
"ds_read_b64_tr_b16 v[76:77], v7 offset:59392\n\t" \
"ds_read_b64_tr_b16 v[78:79], v7 offset:60416\n\t" \
"v_mov_b32 v176, v160\n\t" \
"v_mov_b32 v177, v161\n\t" \
"v_mov_b32 v178, v162\n\t" \
"v_mov_b32 v179, v163\n\t" \
"v_permlane16_swap_b32 v176, v160\n\t" \
"s_waitcnt lgkmcnt(8)\n\t" \
"v_mfma_f32_32x32x16_bf16 v[32:47], v[88:91], v[140:143], v[32:47]\n\t" \
"ds_read_b64_tr_b16 v[80:81], v6 offset:61440\n\t" \
"ds_read_b64_tr_b16 v[82:83], v6 offset:62464\n\t" \
"v_permlane16_swap_b32 v177, v161\n\t" \
"v_permlane16_swap_b32 v178, v162\n\t" \
"v_permlane16_swap_b32 v179, v163\n\t" \
"v_add_f32 v160, v160, v176\n\t" \
"v_mfma_f32_32x32x16_bf16 v[48:63], v[92:95], v[140:143], v[48:63]\n\t" \
"ds_read_b64_tr_b16 v[84:85], v7 offset:61440\n\t" \
"ds_read_b64_tr_b16 v[86:87], v7 offset:62464\n\t" \
"v_add_f32 v161, v161, v177\n\t" \
"v_add_f32 v162, v162, v178\n\t" \
"v_add_f32 v163, v163, v179\n\t" \
"v_mov_b32 v176, v160\n\t" \
"s_waitcnt lgkmcnt(8) vmcnt(3)\n\t" \
"v_mfma_f32_32x32x16_bf16 v[32:47], v[64:67], v[144:147], v[32:47]\n\t" \
"ds_read_b64_tr_b16 v[88:89], v6 offset:63488\n\t" \
"ds_read_b64_tr_b16 v[90:91], v6 offset:64512\n\t" \
"v_mov_b32 v177, v161\n\t" \
"v_mov_b32 v178, v162\n\t" \
"v_mov_b32 v179, v163\n\t" \
"v_permlane32_swap_b32 v176, v160\n\t" \
"v_permlane32_swap_b32 v177, v161\n\t" \
"v_mfma_f32_32x32x16_bf16 v[48:63], v[68:71], v[144:147], v[48:63]\n\t" \
"ds_read_b64_tr_b16 v[92:93], v7 offset:63488\n\t" \
"ds_read_b64_tr_b16 v[94:95], v7 offset:64512\n\t" \
"v_permlane32_swap_b32 v178, v162\n\t" \
"v_permlane32_swap_b32 v179, v163\n\t" \
"v_add_f32 v160, v160, v176\n\t" \
"v_add_f32 v161, v161, v177\n\t" \
"s_waitcnt lgkmcnt(8) vmcnt(2)\n\t" \
"v_mfma_f32_32x32x16_bf16 v[32:47], v[72:75], v[148:151], v[32:47]\n\t" \
"v_add_f32 v162, v162, v178\n\t" \
"v_add_f32 v163, v163, v179\n\t" \
"ds_write_b128 v180, v[160:163]\n\t" \
"ds_read_b64 v[164:165], v181\n\t" \
"v_mfma_f32_32x32x16_bf16 v[48:63], v[76:79], v[148:151], v[48:63]\n\t" \
"s_waitcnt lgkmcnt(6) vmcnt(1)\n\t" \
"v_mfma_f32_32x32x16_bf16 v[32:47], v[80:83], v[152:155], v[32:47]\n\t" \
"s_waitcnt lgkmcnt(0)\n\t" \
"v_mul_f32 v164, 0x3b000000, v164\n\t" \
"v_mul_f32 v165, 0x3b000000, v165\n\t" \
"v_fma_f32 v165, -v164, v164, v165\n\t" \
"v_max_f32 v165, 0, v165\n\t" \
"v_mfma_f32_32x32x16_bf16 v[48:63], v[84:87], v[152:155], v[48:63]\n\t" \
"v_add_f32 v166, 0x3727c5ac, v165\n\t" \
"v_rsq_f32 v166, v166\n\t" \
"s_nop 0\n\t" \
"v_mul_f32 v165, v165, v166\n\t" \
"v_mul_f32 v165, v165, v166\n\t" \
"v_add_f32 v165, 0x3727c5ac, v165\n\t" \
"s_waitcnt vmcnt(0)\n\t" \
"v_mfma_f32_32x32x16_bf16 v[32:47], v[88:91], v[156:159], v[32:47]\n\t" \
"v_rsq_f32 v165, v165\n\t" \
"s_nop 0\n\t" \
"v_mul_f32 v8, v165, v166\n\t" \
"v_mul_f32 v9, v164, v8\n\t" \
"v_sub_f32 v9, 0, v9\n\t" \
"v_mfma_f32_32x32x16_bf16 v[48:63], v[92:95], v[156:159], v[48:63]\n\t" \
"s_setprio 0\n\t" \
"v_fmac_f32 v20, v9, v16\n\t" \
"v_fmac_f32 v21, v9, v17\n\t" \
"v_fmac_f32 v22, v9, v18\n\t" \
"v_fmac_f32 v23, v9, v19\n\t" \
"s_nop 0\n\t" \
"v_fmac_f32 v20, v8, v32\n\t" \
"v_fmac_f32 v21, v8, v33\n\t" \
"v_fmac_f32 v22, v8, v34\n\t" \
"v_fmac_f32 v23, v8, v35\n\t" \
"v_mul_f32 v16, v20, v20\n\t" \
"v_mul_f32 v17, v21, v21\n\t" \
"v_mul_f32 v18, v22, v22\n\t" \
"v_mul_f32 v19, v23, v23\n\t" \
"v_fma_f32 v16, v16, s67, v12\n\t" \
"v_fma_f32 v17, v17, s67, v12\n\t" \
"v_fma_f32 v18, v18, s67, v12\n\t" \
"v_fma_f32 v19, v19, s67, v12\n\t" \
"v_mul_f32 v16, v16, v20\n\t" \
"v_mul_f32 v17, v17, v21\n\t" \
"v_mul_f32 v18, v18, v22\n\t" \
"v_mul_f32 v19, v19, v23\n\t" \
"v_exp_f32 v16, v16\n\t" \
"v_exp_f32 v17, v17\n\t" \
"v_exp_f32 v18, v18\n\t" \
"v_exp_f32 v19, v19\n\t" \
"v_add_f32 v16, 1.0, v16\n\t" \
"v_add_f32 v17, 1.0, v17\n\t" \
"v_add_f32 v18, 1.0, v18\n\t" \
"v_add_f32 v19, 1.0, v19\n\t" \
"v_rcp_f32 v16, v16\n\t" \
"v_rcp_f32 v17, v17\n\t" \
"v_rcp_f32 v18, v18\n\t" \
"v_rcp_f32 v19, v19\n\t" \
"v_mul_f32 v20, v20, v16\n\t" \
"v_mul_f32 v21, v21, v17\n\t" \
"v_mul_f32 v22, v22, v18\n\t" \
"v_mul_f32 v23, v23, v19\n\t" \
"v_cvt_pk_bf16_f32 v32, v20, v21\n\t" \
"v_cvt_pk_bf16_f32 v33, v22, v23\n\t" \
"ds_read_b128 v[16:19], v10 offset:64\n\t" \
"ds_read_b128 v[20:23], v10 offset:576\n\t" \
"v_fmac_f32 v28, v9, v24\n\t" \
"v_fmac_f32 v29, v9, v25\n\t" \
"v_fmac_f32 v30, v9, v26\n\t" \
"v_fmac_f32 v31, v9, v27\n\t" \
"v_fmac_f32 v28, v8, v36\n\t" \
"v_fmac_f32 v29, v8, v37\n\t" \
"v_fmac_f32 v30, v8, v38\n\t" \
"v_fmac_f32 v31, v8, v39\n\t" \
"v_mul_f32 v24, v28, v28\n\t" \
"v_mul_f32 v25, v29, v29\n\t" \
"v_mul_f32 v26, v30, v30\n\t" \
"v_mul_f32 v27, v31, v31\n\t" \
"v_fma_f32 v24, v24, s67, v12\n\t" \
"v_fma_f32 v25, v25, s67, v12\n\t" \
"v_fma_f32 v26, v26, s67, v12\n\t" \
"v_fma_f32 v27, v27, s67, v12\n\t" \
"v_mul_f32 v24, v24, v28\n\t" \
"v_mul_f32 v25, v25, v29\n\t" \
"v_mul_f32 v26, v26, v30\n\t" \
"v_mul_f32 v27, v27, v31\n\t" \
"v_exp_f32 v24, v24\n\t" \
"v_exp_f32 v25, v25\n\t" \
"v_exp_f32 v26, v26\n\t" \
"v_exp_f32 v27, v27\n\t" \
"v_add_f32 v24, 1.0, v24\n\t" \
"v_add_f32 v25, 1.0, v25\n\t" \
"v_add_f32 v26, 1.0, v26\n\t" \
"v_add_f32 v27, 1.0, v27\n\t" \
"v_rcp_f32 v24, v24\n\t" \
"v_rcp_f32 v25, v25\n\t" \
"v_rcp_f32 v26, v26\n\t" \
"v_rcp_f32 v27, v27\n\t" \
"v_mul_f32 v28, v28, v24\n\t" \
"v_mul_f32 v29, v29, v25\n\t" \
"v_mul_f32 v30, v30, v26\n\t" \
"v_mul_f32 v31, v31, v27\n\t" \
"v_cvt_pk_bf16_f32 v34, v28, v29\n\t" \
"v_cvt_pk_bf16_f32 v35, v30, v31\n\t" \
"ds_read_b128 v[24:27], v10 offset:96\n\t" \
"ds_read_b128 v[28:31], v10 offset:608\n\t" \
"s_setprio 3\n\t" \
"buffer_store_dwordx4 v[32:35], v1, s[28:31], s77 offen offset:0 sc1\n\t" \
"s_setprio 0\n\t" \
"s_waitcnt lgkmcnt(2)\n\t" \
"v_fmac_f32 v20, v9, v16\n\t" \
"v_fmac_f32 v21, v9, v17\n\t" \
"v_fmac_f32 v22, v9, v18\n\t" \
"v_fmac_f32 v23, v9, v19\n\t" \
"v_fmac_f32 v20, v8, v40\n\t" \
"v_fmac_f32 v21, v8, v41\n\t" \
"v_fmac_f32 v22, v8, v42\n\t" \
"v_fmac_f32 v23, v8, v43\n\t" \
"v_mul_f32 v16, v20, v20\n\t" \
"v_mul_f32 v17, v21, v21\n\t" \
"v_mul_f32 v18, v22, v22\n\t" \
"v_mul_f32 v19, v23, v23\n\t" \
"v_fma_f32 v16, v16, s67, v12\n\t" \
"v_fma_f32 v17, v17, s67, v12\n\t" \
"v_fma_f32 v18, v18, s67, v12\n\t" \
"v_fma_f32 v19, v19, s67, v12\n\t" \
"v_mul_f32 v16, v16, v20\n\t" \
"v_mul_f32 v17, v17, v21\n\t" \
"v_mul_f32 v18, v18, v22\n\t" \
"v_mul_f32 v19, v19, v23\n\t" \
"v_exp_f32 v16, v16\n\t" \
"v_exp_f32 v17, v17\n\t" \
"v_exp_f32 v18, v18\n\t" \
"v_exp_f32 v19, v19\n\t" \
"v_add_f32 v16, 1.0, v16\n\t" \
"v_add_f32 v17, 1.0, v17\n\t" \
"v_add_f32 v18, 1.0, v18\n\t" \
"v_add_f32 v19, 1.0, v19\n\t" \
"v_rcp_f32 v16, v16\n\t" \
"v_rcp_f32 v17, v17\n\t" \
"v_rcp_f32 v18, v18\n\t" \
"v_rcp_f32 v19, v19\n\t" \
"v_mul_f32 v20, v20, v16\n\t" \
"v_mul_f32 v21, v21, v17\n\t" \
"v_mul_f32 v22, v22, v18\n\t" \
"v_mul_f32 v23, v23, v19\n\t" \
"v_cvt_pk_bf16_f32 v36, v20, v21\n\t" \
"v_cvt_pk_bf16_f32 v37, v22, v23\n\t" \
"ds_read_b128 v[16:19], v10 offset:128\n\t" \
"ds_read_b128 v[20:23], v10 offset:640\n\t" \
"s_waitcnt lgkmcnt(2)\n\t" \
"v_fmac_f32 v28, v9, v24\n\t" \
"v_fmac_f32 v29, v9, v25\n\t" \
"v_fmac_f32 v30, v9, v26\n\t" \
"v_fmac_f32 v31, v9, v27\n\t" \
"v_fmac_f32 v28, v8, v44\n\t" \
"v_fmac_f32 v29, v8, v45\n\t" \
"v_fmac_f32 v30, v8, v46\n\t" \
"v_fmac_f32 v31, v8, v47\n\t" \
"v_mul_f32 v24, v28, v28\n\t" \
"v_mul_f32 v25, v29, v29\n\t" \
"v_mul_f32 v26, v30, v30\n\t" \
"v_mul_f32 v27, v31, v31\n\t" \
"v_fma_f32 v24, v24, s67, v12\n\t" \
"v_fma_f32 v25, v25, s67, v12\n\t" \
"v_fma_f32 v26, v26, s67, v12\n\t" \
"v_fma_f32 v27, v27, s67, v12\n\t" \
"v_mul_f32 v24, v24, v28\n\t" \
"v_mul_f32 v25, v25, v29\n\t" \
"v_mul_f32 v26, v26, v30\n\t" \
"v_mul_f32 v27, v27, v31\n\t" \
"v_exp_f32 v24, v24\n\t" \
"v_exp_f32 v25, v25\n\t" \
"v_exp_f32 v26, v26\n\t" \
"v_exp_f32 v27, v27\n\t" \
"v_add_f32 v24, 1.0, v24\n\t" \
"v_add_f32 v25, 1.0, v25\n\t" \
"v_add_f32 v26, 1.0, v26\n\t" \
"v_add_f32 v27, 1.0, v27\n\t" \
"v_rcp_f32 v24, v24\n\t" \
"v_rcp_f32 v25, v25\n\t" \
"v_rcp_f32 v26, v26\n\t" \
"v_rcp_f32 v27, v27\n\t" \
"v_mul_f32 v28, v28, v24\n\t" \
"v_mul_f32 v29, v29, v25\n\t" \
"v_mul_f32 v30, v30, v26\n\t" \
"v_mul_f32 v31, v31, v27\n\t" \
"v_cvt_pk_bf16_f32 v38, v28, v29\n\t" \
"v_cvt_pk_bf16_f32 v39, v30, v31\n\t" \
"ds_read_b128 v[24:27], v10 offset:160\n\t" \
"ds_read_b128 v[28:31], v10 offset:672\n\t" \
"s_setprio 3\n\t" \
"buffer_store_dwordx4 v[36:39], v1, s[28:31], s77 offen offset:1024 sc1\n\t" \
"s_setprio 0\n\t" \
"s_waitcnt lgkmcnt(2)\n\t" \
"v_fmac_f32 v20, v9, v16\n\t" \
"v_fmac_f32 v21, v9, v17\n\t" \
"v_fmac_f32 v22, v9, v18\n\t" \
"v_fmac_f32 v23, v9, v19\n\t" \
"v_fmac_f32 v20, v8, v48\n\t" \
"v_fmac_f32 v21, v8, v49\n\t" \
"v_fmac_f32 v22, v8, v50\n\t" \
"v_fmac_f32 v23, v8, v51\n\t" \
"v_mul_f32 v16, v20, v20\n\t" \
"v_mul_f32 v17, v21, v21\n\t" \
"v_mul_f32 v18, v22, v22\n\t" \
"v_mul_f32 v19, v23, v23\n\t" \
"v_fma_f32 v16, v16, s67, v12\n\t" \
"v_fma_f32 v17, v17, s67, v12\n\t" \
"v_fma_f32 v18, v18, s67, v12\n\t" \
"v_fma_f32 v19, v19, s67, v12\n\t" \
"v_mul_f32 v16, v16, v20\n\t" \
"v_mul_f32 v17, v17, v21\n\t" \
"v_mul_f32 v18, v18, v22\n\t" \
"v_mul_f32 v19, v19, v23\n\t" \
"v_exp_f32 v16, v16\n\t" \
"v_exp_f32 v17, v17\n\t" \
"v_exp_f32 v18, v18\n\t" \
"v_exp_f32 v19, v19\n\t" \
"v_add_f32 v16, 1.0, v16\n\t" \
"v_add_f32 v17, 1.0, v17\n\t" \
"v_add_f32 v18, 1.0, v18\n\t" \
"v_add_f32 v19, 1.0, v19\n\t" \
"v_rcp_f32 v16, v16\n\t" \
"v_rcp_f32 v17, v17\n\t" \
"v_rcp_f32 v18, v18\n\t" \
"v_rcp_f32 v19, v19\n\t" \
"v_mul_f32 v20, v20, v16\n\t" \
"v_mul_f32 v21, v21, v17\n\t" \
"v_mul_f32 v22, v22, v18\n\t" \
"v_mul_f32 v23, v23, v19\n\t" \
"v_cvt_pk_bf16_f32 v48, v20, v21\n\t" \
"v_cvt_pk_bf16_f32 v49, v22, v23\n\t" \
"ds_read_b128 v[16:19], v10 offset:192\n\t" \
"ds_read_b128 v[20:23], v10 offset:704\n\t" \
"s_waitcnt lgkmcnt(2)\n\t" \
"v_fmac_f32 v28, v9, v24\n\t" \
"v_fmac_f32 v29, v9, v25\n\t" \
"v_fmac_f32 v30, v9, v26\n\t" \
"v_fmac_f32 v31, v9, v27\n\t" \
"v_fmac_f32 v28, v8, v52\n\t" \
"v_fmac_f32 v29, v8, v53\n\t" \
"v_fmac_f32 v30, v8, v54\n\t" \
"v_fmac_f32 v31, v8, v55\n\t" \
"v_mul_f32 v24, v28, v28\n\t" \
"v_mul_f32 v25, v29, v29\n\t" \
"v_mul_f32 v26, v30, v30\n\t" \
"v_mul_f32 v27, v31, v31\n\t" \
"v_fma_f32 v24, v24, s67, v12\n\t" \
"v_fma_f32 v25, v25, s67, v12\n\t" \
"v_fma_f32 v26, v26, s67, v12\n\t" \
"v_fma_f32 v27, v27, s67, v12\n\t" \
"v_mul_f32 v24, v24, v28\n\t" \
"v_mul_f32 v25, v25, v29\n\t" \
"v_mul_f32 v26, v26, v30\n\t" \
"v_mul_f32 v27, v27, v31\n\t" \
"v_exp_f32 v24, v24\n\t" \
"v_exp_f32 v25, v25\n\t" \
"v_exp_f32 v26, v26\n\t" \
"v_exp_f32 v27, v27\n\t" \
"v_add_f32 v24, 1.0, v24\n\t" \
"v_add_f32 v25, 1.0, v25\n\t" \
"v_add_f32 v26, 1.0, v26\n\t" \
"v_add_f32 v27, 1.0, v27\n\t" \
"v_rcp_f32 v24, v24\n\t" \
"v_rcp_f32 v25, v25\n\t" \
"v_rcp_f32 v26, v26\n\t" \
"v_rcp_f32 v27, v27\n\t" \
"v_mul_f32 v28, v28, v24\n\t" \
"v_mul_f32 v29, v29, v25\n\t" \
"v_mul_f32 v30, v30, v26\n\t" \
"v_mul_f32 v31, v31, v27\n\t" \
"v_cvt_pk_bf16_f32 v50, v28, v29\n\t" \
"v_cvt_pk_bf16_f32 v51, v30, v31\n\t" \
"s_waitcnt vmcnt(0)\n\t" \
"v_mov_b32 v40, s44\n\t" \
"v_mov_b32 v41, s45\n\t" \
"v_mov_b32 v42, s44\n\t" \
"v_mov_b32 v43, s45\n\t" \
"s_mov_b64 exec, 0xff\n\t" \
"s_setprio 3\n\t" \
"buffer_store_dwordx4 v[40:43], v1, s[28:31], s76 offen sc1\n\t" \
"s_setprio 0\n\t" \
"s_mov_b64 exec, -1\n\t" \
"s_add_u32 s76, s76, 2048\n\t" \
"ds_read_b128 v[24:27], v10 offset:224\n\t" \
"ds_read_b128 v[28:31], v10 offset:736\n\t" \
"s_setprio 3\n\t" \
"buffer_store_dwordx4 v[48:51], v1, s[28:31], s77 offen offset:2048 sc1\n\t" \
"s_setprio 0\n\t" \
"s_waitcnt lgkmcnt(2)\n\t" \
"v_fmac_f32 v20, v9, v16\n\t" \
"v_fmac_f32 v21, v9, v17\n\t" \
"v_fmac_f32 v22, v9, v18\n\t" \
"v_fmac_f32 v23, v9, v19\n\t" \
"v_fmac_f32 v20, v8, v56\n\t" \
"v_fmac_f32 v21, v8, v57\n\t" \
"v_fmac_f32 v22, v8, v58\n\t" \
"v_fmac_f32 v23, v8, v59\n\t" \
"v_mul_f32 v16, v20, v20\n\t" \
"v_mul_f32 v17, v21, v21\n\t" \
"v_mul_f32 v18, v22, v22\n\t" \
"v_mul_f32 v19, v23, v23\n\t" \
"v_fma_f32 v16, v16, s67, v12\n\t" \
"v_fma_f32 v17, v17, s67, v12\n\t" \
"v_fma_f32 v18, v18, s67, v12\n\t" \
"v_fma_f32 v19, v19, s67, v12\n\t" \
"v_mul_f32 v16, v16, v20\n\t" \
"v_mul_f32 v17, v17, v21\n\t" \
"v_mul_f32 v18, v18, v22\n\t" \
"v_mul_f32 v19, v19, v23\n\t" \
"v_exp_f32 v16, v16\n\t" \
"v_exp_f32 v17, v17\n\t" \
"v_exp_f32 v18, v18\n\t" \
"v_exp_f32 v19, v19\n\t" \
"v_add_f32 v16, 1.0, v16\n\t" \
"v_add_f32 v17, 1.0, v17\n\t" \
"v_add_f32 v18, 1.0, v18\n\t" \
"v_add_f32 v19, 1.0, v19\n\t" \
"v_rcp_f32 v16, v16\n\t" \
"v_rcp_f32 v17, v17\n\t" \
"v_rcp_f32 v18, v18\n\t" \
"v_rcp_f32 v19, v19\n\t" \
"v_mul_f32 v20, v20, v16\n\t" \
"v_mul_f32 v21, v21, v17\n\t" \
"v_mul_f32 v22, v22, v18\n\t" \
"v_mul_f32 v23, v23, v19\n\t" \
"v_cvt_pk_bf16_f32 v52, v20, v21\n\t" \
"v_cvt_pk_bf16_f32 v53, v22, v23\n\t" \
"s_waitcnt lgkmcnt(0)\n\t" \
"v_fmac_f32 v28, v9, v24\n\t" \
"v_fmac_f32 v29, v9, v25\n\t" \
"v_fmac_f32 v30, v9, v26\n\t" \
"v_fmac_f32 v31, v9, v27\n\t" \
"v_fmac_f32 v28, v8, v60\n\t" \
"v_fmac_f32 v29, v8, v61\n\t" \
"v_fmac_f32 v30, v8, v62\n\t" \
"v_fmac_f32 v31, v8, v63\n\t" \
"v_mul_f32 v24, v28, v28\n\t" \
"v_mul_f32 v25, v29, v29\n\t" \
"v_mul_f32 v26, v30, v30\n\t" \
"v_mul_f32 v27, v31, v31\n\t" \
"v_fma_f32 v24, v24, s67, v12\n\t" \
"v_fma_f32 v25, v25, s67, v12\n\t" \
"v_fma_f32 v26, v26, s67, v12\n\t" \
"v_fma_f32 v27, v27, s67, v12\n\t" \
"v_mul_f32 v24, v24, v28\n\t" \
"v_mul_f32 v25, v25, v29\n\t" \
"v_mul_f32 v26, v26, v30\n\t" \
"v_mul_f32 v27, v27, v31\n\t" \
"v_exp_f32 v24, v24\n\t" \
"v_exp_f32 v25, v25\n\t" \
"v_exp_f32 v26, v26\n\t" \
"v_exp_f32 v27, v27\n\t" \
"v_add_f32 v24, 1.0, v24\n\t" \
"v_add_f32 v25, 1.0, v25\n\t" \
"v_add_f32 v26, 1.0, v26\n\t" \
"v_add_f32 v27, 1.0, v27\n\t" \
"v_rcp_f32 v24, v24\n\t" \
"v_rcp_f32 v25, v25\n\t" \
"v_rcp_f32 v26, v26\n\t" \
"v_rcp_f32 v27, v27\n\t" \
"v_mul_f32 v28, v28, v24\n\t" \
"v_mul_f32 v29, v29, v25\n\t" \
"v_mul_f32 v30, v30, v26\n\t" \
"v_mul_f32 v31, v31, v27\n\t" \
"v_cvt_pk_bf16_f32 v54, v28, v29\n\t" \
"v_cvt_pk_bf16_f32 v55, v30, v31\n\t" \
"s_setprio 3\n\t" \
"buffer_store_dwordx4 v[52:55], v1, s[28:31], s77 offen offset:3072 sc1\n\t" \
"s_setprio 0\n\t" \
"s_branch Lm1done_%=\n\t" \
"Lnom1_%=:\n\t" \
"s_mov_b32 s38, 1835008\n\t" \
"s_memtime s[68:69]\n\t" \
"s_waitcnt lgkmcnt(0)\n\t" \
"s_add_u32 m0, s62, 131072\n\t" \
"s_add_u32 s63, s60, 0\n\t" \
"buffer_load_dwordx4 v11, s[36:39], s63 offen lds\n\t" \
"s_add_u32 m0, s62, 132096\n\t" \
"s_add_u32 s63, s60, 8192\n\t" \
"buffer_load_dwordx4 v11, s[36:39], s63 offen lds\n\t" \
"s_add_u32 m0, s62, 133120\n\t" \
"s_add_u32 s63, s60, 16384\n\t" \
"buffer_load_dwordx4 v11, s[36:39], s63 offen lds\n\t" \
"s_add_u32 m0, s62, 134144\n\t" \
"s_add_u32 s63, s60, 24576\n\t" \
"buffer_load_dwordx4 v11, s[36:39], s63 offen lds\n\t" \
"s_add_u32 m0, s62, 0\n\t" \
"s_add_u32 s63, s60, 524288\n\t" \
"buffer_load_dwordx4 v11, s[36:39], s63 offen lds\n\t" \
"s_add_u32 m0, s62, 1024\n\t" \
"s_add_u32 s63, s60, 532480\n\t" \
"buffer_load_dwordx4 v11, s[36:39], s63 offen lds\n\t" \
"s_add_u32 m0, s62, 2048\n\t" \
"s_add_u32 s63, s60, 540672\n\t" \
"buffer_load_dwordx4 v11, s[36:39], s63 offen lds\n\t" \
"s_add_u32 m0, s62, 3072\n\t" \
"s_add_u32 s63, s60, 548864\n\t" \
"buffer_load_dwordx4 v11, s[36:39], s63 offen lds\n\t" \
"s_add_u32 m0, s62, 16384\n\t" \
"s_add_u32 s63, s60, 1048576\n\t" \
"buffer_load_dwordx4 v11, s[36:39], s63 offen lds\n\t" \
"s_add_u32 m0, s62, 17408\n\t" \
"s_add_u32 s63, s60, 1056768\n\t" \
"buffer_load_dwordx4 v11, s[36:39], s63 offen lds\n\t" \
"s_add_u32 m0, s62, 18432\n\t" \
"s_add_u32 s63, s60, 1064960\n\t" \
"buffer_load_dwordx4 v11, s[36:39], s63 offen lds\n\t" \
"s_add_u32 m0, s62, 19456\n\t" \
"s_add_u32 s63, s60, 1073152\n\t" \
"buffer_load_dwordx4 v11, s[36:39], s63 offen lds\n\t" \
"s_add_u32 m0, s62, 32768\n\t" \
"s_add_u32 s63, s66, 1572864\n\t" \
"buffer_load_dwordx4 v11, s[36:39], s63 offen lds\n\t" \
"s_add_u32 m0, s62, 33792\n\t" \
"s_add_u32 s63, s66, 1581056\n\t" \
"buffer_load_dwordx4 v11, s[36:39], s63 offen lds\n\t" \
"s_add_u32 m0, s62, 34816\n\t" \
"s_add_u32 s63, s66, 1589248\n\t" \
"buffer_load_dwordx4 v11, s[36:39], s63 offen lds\n\t" \
"s_add_u32 m0, s62, 35840\n\t" \
"s_add_u32 s63, s66, 1597440\n\t" \
"buffer_load_dwordx4 v11, s[36:39], s63 offen lds\n\t" \
"s_add_u32 m0, s62, 49152\n\t" \
"s_add_u32 s63, s60, 294912\n\t" \
"buffer_load_dwordx4 v11, s[36:39], s63 offen lds\n\t" \
"s_add_u32 m0, s62, 50176\n\t" \
"s_add_u32 s63, s60, 303104\n\t" \
"buffer_load_dwordx4 v11, s[36:39], s63 offen lds\n\t" \
"s_add_u32 m0, s62, 51200\n\t" \
"s_add_u32 s63, s60, 311296\n\t" \
"buffer_load_dwordx4 v11, s[36:39], s63 offen lds\n\t" \
"s_add_u32 m0, s62, 52224\n\t" \
"s_add_u32 s63, s60, 319488\n\t" \
"buffer_load_dwordx4 v11, s[36:39], s63 offen lds\n\t" \
"s_add_u32 s63, s60, 819200\n\t" \
"buffer_load_dwordx4 v[192:195], v11, s[36:39], s63 offen\n\t" \
"s_add_u32 s63, s60, 827392\n\t" \
"buffer_load_dwordx4 v[196:199], v11, s[36:39], s63 offen\n\t" \
"s_add_u32 s63, s60, 835584\n\t" \
"buffer_load_dwordx4 v[200:203], v11, s[36:39], s63 offen\n\t" \
"s_add_u32 s63, s60, 843776\n\t" \
"buffer_load_dwordx4 v[204:207], v11, s[36:39], s63 offen\n\t" \
"s_add_u32 s63, s60, 1343488\n\t" \
"buffer_load_dwordx4 v[208:211], v11, s[36:39], s63 offen\n\t" \
"s_add_u32 s63, s60, 1351680\n\t" \
"buffer_load_dwordx4 v[212:215], v11, s[36:39], s63 offen\n\t" \
"s_add_u32 s63, s60, 1359872\n\t" \
"buffer_load_dwordx4 v[216:219], v11, s[36:39], s63 offen\n\t" \
"s_add_u32 s63, s60, 1368064\n\t" \
"buffer_load_dwordx4 v[220:223], v11, s[36:39], s63 offen\n\t" \
"s_add_u32 s63, s53, 65536\n\t" \
"buffer_load_dwordx4 v[224:227], v11, s[36:39], s63 offen\n\t" \
"s_add_u32 s63, s53, 73728\n\t" \
"buffer_load_dwordx4 v[228:231], v11, s[36:39], s63 offen\n\t" \
"s_add_u32 s63, s53, 81920\n\t" \
"buffer_load_dwordx4 v[232:235], v11, s[36:39], s63 offen\n\t" \
"s_add_u32 s63, s53, 90112\n\t" \
"buffer_load_dwordx4 v[236:239], v11, s[36:39], s63 offen\n\t" \
"s_add_u32 s63, s53, 589824\n\t" \
"buffer_load_dwordx4 v[240:243], v11, s[36:39], s63 offen\n\t" \
"s_add_u32 s63, s53, 598016\n\t" \
"buffer_load_dwordx4 v[244:247], v11, s[36:39], s63 offen\n\t" \
"s_add_u32 s63, s53, 606208\n\t" \
"buffer_load_dwordx4 v[248:251], v11, s[36:39], s63 offen\n\t" \
"s_add_u32 s63, s53, 614400\n\t" \
"buffer_load_dwordx4 v[252:255], v11, s[36:39], s63 offen\n\t" \
"s_waitcnt vmcnt(0) lgkmcnt(0)\n\t" \
"s_barrier\n\t" \
"s_barrier\n\t" \
"Lnw_%=:\n\t" \
"s_sleep 2\n\t" \
"s_memtime s[70:71]\n\t" \
"s_waitcnt lgkmcnt(0)\n\t" \
"s_sub_u32 s70, s70, s68\n\t" \
"s_cmp_lt_u32 s70, 9000\n\t" \
"s_cbranch_scc1 Lnw_%=\n\t" \
"s_mov_b32 s76, 1073741824\n\t" \
"Lm1done_%=:\n\t" \
"s_lshl_b32 s63, s55, 12\n\t" \
"v_add_u32 v26, s63, v6\n\t" \
"v_add_u32 v27, s63, v7\n\t" \
"v_subrev_u32 v24, 0x10000, v26\n\t" \
"v_add_u32 v28, 0x10000, v26\n\t" \
"v_subrev_u32 v25, 0x10000, v27\n\t" \
"v_add_u32 v29, 0x10000, v27\n\t" \
"ds_read_b64_tr_b16 v[64:65], v28 offset:0\n\t" \
"ds_read_b64_tr_b16 v[66:67], v28 offset:1024\n\t" \
"ds_read_b64_tr_b16 v[68:69], v29 offset:0\n\t" \
"ds_read_b64_tr_b16 v[70:71], v29 offset:1024\n\t" \
"ds_read_b64_tr_b16 v[72:73], v28 offset:2048\n\t" \
"ds_read_b64_tr_b16 v[74:75], v28 offset:3072\n\t" \
"ds_read_b64_tr_b16 v[76:77], v29 offset:2048\n\t" \
"ds_read_b64_tr_b16 v[78:79], v29 offset:3072\n\t" \
"ds_read_b64_tr_b16 v[80:81], v28 offset:8192\n\t" \
"ds_read_b64_tr_b16 v[82:83], v28 offset:9216\n\t" \
"ds_read_b64_tr_b16 v[84:85], v29 offset:8192\n\t" \
"ds_read_b64_tr_b16 v[86:87], v29 offset:9216\n\t" \
"v_add_u32 v2, 0x10000, v2\n\t" \
"s_lshl_b32 s57, s54, 6\n\t" \
"s_add_u32 s57, s57, s55\n\t" \
"s_lshl_b32 s57, s57, 7\n\t" \
"s_add_u32 s57, s57, 262144\n\t" \
"s_mul_i32 s61, s54, 114688\n\t" \
"s_lshl_b32 s58, s55, 13\n\t" \
"s_add_u32 s61, s61, s58\n\t" \
"s_add_u32 s61, s61, 9699328\n\t" \
"s_and_b32 s58, s54, 4\n\t" \
"s_lshl_b32 s58, s58, 10\n\t" \
"s_sub_u32 s59, s61, s58\n\t" \
"s_add_u32 s61, s61, s58\n\t" \
"v_min_u32 v13, 6, v0\n\t" \
"v_lshlrev_b32 v13, 8, v13\n\t" \
"s_barrier\n\t" \
"ds_write_b128 v2, v[192:195] offset:0\n\t" \
"ds_write_b128 v2, v[196:199] offset:1024\n\t" \
"ds_write_b128 v2, v[200:203] offset:2048\n\t" \
"ds_write_b128 v2, v[204:207] offset:3072\n\t" \
"ds_write_b128 v2, v[208:211] offset:16384\n\t" \
"ds_write_b128 v2, v[212:215] offset:17408\n\t" \
"buffer_load_dwordx2 v[14:15], v13, s[28:31], s57 offen sc1\n\t" \
"ds_write_b128 v2, v[216:219] offset:18432\n\t" \
"ds_write_b128 v2, v[220:223] offset:19456\n\t" \
"ds_write_b128 v2, v[224:227] offset:32768\n\t" \
"ds_write_b128 v2, v[228:231] offset:33792\n\t" \
"ds_write_b128 v2, v[232:235] offset:34816\n\t" \
"ds_write_b128 v2, v[236:239] offset:35840\n\t" \
"s_waitcnt vmcnt(1)\n\t" \
"v_mov_b32 v20, s44\n\t" \
"v_mov_b32 v21, s45\n\t" \
"v_mov_b32 v22, s44\n\t" \
"v_mov_b32 v23, s45\n\t" \
"s_mov_b64 exec, 0xff\n\t" \
"buffer_store_dwordx4 v[20:23], v1, s[28:31], s76 offen sc1\n\t" \
"s_mov_b64 exec, -1\n\t" \
"s_add_u32 s76, s76, 2048\n\t" \
"s_waitcnt vmcnt(1)\n\t" \
"v_cmp_ne_u64 vcc, s[44:45], v[14:15]\n\t" \
"s_nop 0\n\t" \
"s_cmp_lg_u64 vcc, 0\n\t" \
"s_cbranch_scc1 Lspin0_%=\n\t" \
"Lspun0_%=:\n\t" \
"s_add_u32 s58, s61, 0\n\t" \
"buffer_load_dwordx4 v[96:99], v1, s[28:31], s58 offen offset:0 sc1\n\t" \
"ds_write_b128 v2, v[240:243] offset:49152\n\t" \
"buffer_load_dwordx4 v[100:103], v1, s[28:31], s58 offen offset:1024 sc1\n\t" \
"s_add_u32 s58, s61, 16384\n\t" \
"buffer_load_dwordx4 v[104:107], v1, s[28:31], s58 offen offset:0 sc1\n\t" \
"buffer_load_dwordx4 v[108:111], v1, s[28:31], s58 offen offset:1024 sc1\n\t" \
"ds_write_b128 v2, v[244:247] offset:50176\n\t" \
"s_add_u32 s58, s61, 32768\n\t" \
"buffer_load_dwordx4 v[112:115], v1, s[28:31], s58 offen offset:0 sc1\n\t" \
"buffer_load_dwordx4 v[116:119], v1, s[28:31], s58 offen offset:1024 sc1\n\t" \
"s_add_u32 s58, s61, 49152\n\t" \
"buffer_load_dwordx4 v[120:123], v1, s[28:31], s58 offen offset:0 sc1\n\t" \
"ds_write_b128 v2, v[248:251] offset:51200\n\t" \
"buffer_load_dwordx4 v[124:127], v1, s[28:31], s58 offen offset:1024 sc1\n\t" \
"s_add_u32 s58, s61, 65536\n\t" \
"buffer_load_dwordx4 v[128:131], v1, s[28:31], s58 offen offset:0 sc1\n\t" \
"buffer_load_dwordx4 v[132:135], v1, s[28:31], s58 offen offset:1024 sc1\n\t" \
"ds_write_b128 v2, v[252:255] offset:52224\n\t" \
"s_add_u32 s58, s61, 81920\n\t" \
"buffer_load_dwordx4 v[136:139], v1, s[28:31], s58 offen offset:0 sc1\n\t" \
"buffer_load_dwordx4 v[140:143], v1, s[28:31], s58 offen offset:1024 sc1\n\t" \
"s_add_u32 s57, s57, 2048\n\t" \
"buffer_load_dwordx2 v[14:15], v13, s[28:31], s57 offen sc1\n\t" \
"s_waitcnt lgkmcnt(14) vmcnt(12)\n\t" \
"v_mfma_f32_32x32x16_bf16 v[32:47], v[64:67], v[96:99], 0\n\t" \
"ds_read_b64_tr_b16 v[88:89], v28 offset:10240\n\t" \
"ds_read_b64_tr_b16 v[90:91], v28 offset:11264\n\t" \
"v_mfma_f32_32x32x16_bf16 v[48:63], v[68:71], v[96:99], 0\n\t" \
"ds_read_b64_tr_b16 v[92:93], v29 offset:10240\n\t" \
"ds_read_b64_tr_b16 v[94:95], v29 offset:11264\n\t" \
"s_add_u32 s58, s61, 98304\n\t" \
"buffer_load_dwordx4 v[144:147], v1, s[28:31], s58 offen offset:0 sc1\n\t" \
"s_waitcnt vmcnt(12)\n\t" \
"v_mfma_f32_32x32x16_bf16 v[32:47], v[72:75], v[100:103], v[32:47]\n\t" \
"ds_read_b64_tr_b16 v[64:65], v24 offset:0\n\t" \
"ds_read_b64_tr_b16 v[66:67], v24 offset:1024\n\t" \
"v_mfma_f32_32x32x16_bf16 v[48:63], v[76:79], v[100:103], v[48:63]\n\t" \
"ds_read_b64_tr_b16 v[68:69], v25 offset:0\n\t" \
"ds_read_b64_tr_b16 v[70:71], v25 offset:1024\n\t" \
"buffer_load_dwordx4 v[148:151], v1, s[28:31], s58 offen offset:1024 sc1\n\t" \
"s_waitcnt vmcnt(12)\n\t" \
"v_mfma_f32_32x32x16_bf16 v[32:47], v[80:83], v[104:107], v[32:47]\n\t" \
"ds_read_b64_tr_b16 v[72:73], v24 offset:2048\n\t" \
"ds_read_b64_tr_b16 v[74:75], v24 offset:3072\n\t" \
"v_mfma_f32_32x32x16_bf16 v[48:63], v[84:87], v[104:107], v[48:63]\n\t" \
"ds_read_b64_tr_b16 v[76:77], v25 offset:2048\n\t" \
"ds_read_b64_tr_b16 v[78:79], v25 offset:3072\n\t" \
"s_waitcnt lgkmcnt(8) vmcnt(11)\n\t" \
"v_mfma_f32_32x32x16_bf16 v[32:47], v[88:91], v[108:111], v[32:47]\n\t" \
"ds_read_b64_tr_b16 v[80:81], v24 offset:8192\n\t" \
"ds_read_b64_tr_b16 v[82:83], v24 offset:9216\n\t" \
"v_mfma_f32_32x32x16_bf16 v[48:63], v[92:95], v[108:111], v[48:63]\n\t" \
"ds_read_b64_tr_b16 v[84:85], v25 offset:8192\n\t" \
"ds_read_b64_tr_b16 v[86:87], v25 offset:9216\n\t" \
"s_waitcnt lgkmcnt(8) vmcnt(10)\n\t" \
"v_mfma_f32_32x32x16_bf16 v[32:47], v[64:67], v[112:115], v[32:47]\n\t" \
"ds_read_b64_tr_b16 v[88:89], v24 offset:10240\n\t" \
"ds_read_b64_tr_b16 v[90:91], v24 offset:11264\n\t" \
"v_mfma_f32_32x32x16_bf16 v[48:63], v[68:71], v[112:115], v[48:63]\n\t" \
"ds_read_b64_tr_b16 v[92:93], v25 offset:10240\n\t" \
"ds_read_b64_tr_b16 v[94:95], v25 offset:11264\n\t" \
"s_waitcnt vmcnt(2)\n\t" \
"v_cmp_ne_u64 vcc, s[44:45], v[14:15]\n\t" \
"s_nop 0\n\t" \
"s_cmp_lg_u64 vcc, 0\n\t" \
"s_cbranch_scc1 Lspin1_%=\n\t" \
"Lspun1_%=:\n\t" \
"s_add_u32 s58, s61, 2048\n\t" \
"buffer_load_dwordx4 v[152:155], v1, s[28:31], s58 offen offset:0 sc1\n\t" \
"buffer_load_dwordx4 v[156:159], v1, s[28:31], s58 offen offset:1024 sc1\n\t" \
"s_waitcnt lgkmcnt(8)\n\t" \
"v_mfma_f32_32x32x16_bf16 v[32:47], v[72:75], v[116:119], v[32:47]\n\t" \
"ds_read_b64_tr_b16 v[64:65], v24 offset:16384\n\t" \
"ds_read_b64_tr_b16 v[66:67], v24 offset:17408\n\t" \
"v_mfma_f32_32x32x16_bf16 v[48:63], v[76:79], v[116:119], v[48:63]\n\t" \
"ds_read_b64_tr_b16 v[68:69], v25 offset:16384\n\t" \
"ds_read_b64_tr_b16 v[70:71], v25 offset:17408\n\t" \
"s_add_u32 s58, s61, 18432\n\t" \
"buffer_load_dwordx4 v[160:163], v1, s[28:31], s58 offen offset:0 sc1\n\t" \
"buffer_load_dwordx4 v[164:167], v1, s[28:31], s58 offen offset:1024 sc1\n\t" \
"s_waitcnt lgkmcnt(8)\n\t" \
"v_mfma_f32_32x32x16_bf16 v[32:47], v[80:83], v[120:123], v[32:47]\n\t" \
"ds_read_b64_tr_b16 v[72:73], v24 offset:18432\n\t" \
"ds_read_b64_tr_b16 v[74:75], v24 offset:19456\n\t" \
"v_mfma_f32_32x32x16_bf16 v[48:63], v[84:87], v[120:123], v[48:63]\n\t" \
"ds_read_b64_tr_b16 v[76:77], v25 offset:18432\n\t" \
"ds_read_b64_tr_b16 v[78:79], v25 offset:19456\n\t" \
"s_add_u32 s58, s61, 34816\n\t" \
"buffer_load_dwordx4 v[168:171], v1, s[28:31], s58 offen offset:0 sc1\n\t" \
"s_waitcnt lgkmcnt(8)\n\t" \
"v_mfma_f32_32x32x16_bf16 v[32:47], v[88:91], v[124:127], v[32:47]\n\t" \
"ds_read_b64_tr_b16 v[80:81], v24 offset:24576\n\t" \
"ds_read_b64_tr_b16 v[82:83], v24 offset:25600\n\t" \
"v_mfma_f32_32x32x16_bf16 v[48:63], v[92:95], v[124:127], v[48:63]\n\t" \
"ds_read_b64_tr_b16 v[84:85], v25 offset:24576\n\t" \
"ds_read_b64_tr_b16 v[86:87], v25 offset:25600\n\t" \
"buffer_load_dwordx4 v[172:175], v1, s[28:31], s58 offen offset:1024 sc1\n\t" \
"s_waitcnt lgkmcnt(8)\n\t" \
"v_mfma_f32_32x32x16_bf16 v[32:47], v[64:67], v[128:131], v[32:47]\n\t" \
"ds_read_b64_tr_b16 v[88:89], v24 offset:26624\n\t" \
"ds_read_b64_tr_b16 v[90:91], v24 offset:27648\n\t" \
"v_mfma_f32_32x32x16_bf16 v[48:63], v[68:71], v[128:131], v[48:63]\n\t" \
"ds_read_b64_tr_b16 v[92:93], v25 offset:26624\n\t" \
"ds_read_b64_tr_b16 v[94:95], v25 offset:27648\n\t" \
"s_add_u32 s58, s61, 51200\n\t" \
"buffer_load_dwordx4 v[176:179], v1, s[28:31], s58 offen offset:0 sc1\n\t" \
"s_waitcnt lgkmcnt(8)\n\t" \
"v_mfma_f32_32x32x16_bf16 v[32:47], v[72:75], v[132:135], v[32:47]\n\t" \
"ds_read_b64_tr_b16 v[64:65], v24 offset:32768\n\t" \
"ds_read_b64_tr_b16 v[66:67], v24 offset:33792\n\t" \
"v_mfma_f32_32x32x16_bf16 v[48:63], v[76:79], v[132:135], v[48:63]\n\t" \
"ds_read_b64_tr_b16 v[68:69], v25 offset:32768\n\t" \
"ds_read_b64_tr_b16 v[70:71], v25 offset:33792\n\t" \
"buffer_load_dwordx4 v[180:183], v1, s[28:31], s58 offen offset:1024 sc1\n\t" \
"s_waitcnt lgkmcnt(8)\n\t" \
"v_mfma_f32_32x32x16_bf16 v[32:47], v[80:83], v[136:139], v[32:47]\n\t" \
"ds_read_b64_tr_b16 v[72:73], v24 offset:34816\n\t" \
"ds_read_b64_tr_b16 v[74:75], v24 offset:35840\n\t" \
"v_mfma_f32_32x32x16_bf16 v[48:63], v[84:87], v[136:139], v[48:63]\n\t" \
"ds_read_b64_tr_b16 v[76:77], v25 offset:34816\n\t" \
"ds_read_b64_tr_b16 v[78:79], v25 offset:35840\n\t" \
"s_add_u32 s58, s61, 67584\n\t" \
"buffer_load_dwordx4 v[184:187], v1, s[28:31], s58 offen offset:0 sc1\n\t" \
"s_waitcnt lgkmcnt(8)\n\t" \
"v_mfma_f32_32x32x16_bf16 v[32:47], v[88:91], v[140:143], v[32:47]\n\t" \
"ds_read_b64_tr_b16 v[80:81], v24 offset:40960\n\t" \
"ds_read_b64_tr_b16 v[82:83], v24 offset:41984\n\t" \
"v_mfma_f32_32x32x16_bf16 v[48:63], v[92:95], v[140:143], v[48:63]\n\t" \
"ds_read_b64_tr_b16 v[84:85], v25 offset:40960\n\t" \
"ds_read_b64_tr_b16 v[86:87], v25 offset:41984\n\t" \
"buffer_load_dwordx4 v[188:191], v1, s[28:31], s58 offen offset:1024 sc1\n\t" \
"s_waitcnt lgkmcnt(8) vmcnt(11)\n\t" \
"v_mfma_f32_32x32x16_bf16 v[32:47], v[64:67], v[144:147], v[32:47]\n\t" \
"ds_read_b64_tr_b16 v[88:89], v24 offset:43008\n\t" \
"ds_read_b64_tr_b16 v[90:91], v24 offset:44032\n\t" \
"v_mfma_f32_32x32x16_bf16 v[48:63], v[68:71], v[144:147], v[48:63]\n\t" \
"ds_read_b64_tr_b16 v[92:93], v25 offset:43008\n\t" \
"ds_read_b64_tr_b16 v[94:95], v25 offset:44032\n\t" \
"s_add_u32 s58, s61, 83968\n\t" \
"buffer_load_dwordx4 v[96:99], v1, s[28:31], s58 offen offset:0 sc1\n\t" \
"s_waitcnt lgkmcnt(8) vmcnt(11)\n\t" \
"v_mfma_f32_32x32x16_bf16 v[32:47], v[72:75], v[148:151], v[32:47]\n\t" \
"ds_read_b64_tr_b16 v[64:65], v24 offset:49152\n\t" \
"ds_read_b64_tr_b16 v[66:67], v24 offset:50176\n\t" \
"v_mfma_f32_32x32x16_bf16 v[48:63], v[76:79], v[148:151], v[48:63]\n\t" \
"ds_read_b64_tr_b16 v[68:69], v25 offset:49152\n\t" \
"ds_read_b64_tr_b16 v[70:71], v25 offset:50176\n\t" \
"buffer_load_dwordx4 v[100:103], v1, s[28:31], s58 offen offset:1024 sc1\n\t" \
"s_add_u32 s57, s57, 2048\n\t" \
"buffer_load_dwordx2 v[14:15], v13, s[28:31], s57 offen sc1\n\t" \
"s_waitcnt lgkmcnt(8) vmcnt(12)\n\t" \
"v_mfma_f32_32x32x16_bf16 v[32:47], v[80:83], v[152:155], v[32:47]\n\t" \
"ds_read_b64_tr_b16 v[72:73], v24 offset:51200\n\t" \
"ds_read_b64_tr_b16 v[74:75], v24 offset:52224\n\t" \
"v_mfma_f32_32x32x16_bf16 v[48:63], v[84:87], v[152:155], v[48:63]\n\t" \
"ds_read_b64_tr_b16 v[76:77], v25 offset:51200\n\t" \
"ds_read_b64_tr_b16 v[78:79], v25 offset:52224\n\t" \
"s_add_u32 s58, s61, 100352\n\t" \
"buffer_load_dwordx4 v[104:107], v1, s[28:31], s58 offen offset:0 sc1\n\t" \
"s_waitcnt lgkmcnt(8) vmcnt(12)\n\t" \
"v_mfma_f32_32x32x16_bf16 v[32:47], v[88:91], v[156:159], v[32:47]\n\t" \
"ds_read_b64_tr_b16 v[80:81], v24 offset:57344\n\t" \
"ds_read_b64_tr_b16 v[82:83], v24 offset:58368\n\t" \
"v_mfma_f32_32x32x16_bf16 v[48:63], v[92:95], v[156:159], v[48:63]\n\t" \
"ds_read_b64_tr_b16 v[84:85], v25 offset:57344\n\t" \
"ds_read_b64_tr_b16 v[86:87], v25 offset:58368\n\t" \
"buffer_load_dwordx4 v[108:111], v1, s[28:31], s58 offen offset:1024 sc1\n\t" \
"s_barrier\n\t" \
"s_waitcnt lgkmcnt(8) vmcnt(12)\n\t" \
"v_mfma_f32_32x32x16_bf16 v[32:47], v[64:67], v[160:163], v[32:47]\n\t" \
"ds_read_b64_tr_b16 v[88:89], v24 offset:59392\n\t" \
"ds_read_b64_tr_b16 v[90:91], v24 offset:60416\n\t" \
"v_mfma_f32_32x32x16_bf16 v[48:63], v[68:71], v[160:163], v[48:63]\n\t" \
"ds_read_b64_tr_b16 v[92:93], v25 offset:59392\n\t" \
"ds_read_b64_tr_b16 v[94:95], v25 offset:60416\n\t" \
"s_waitcnt lgkmcnt(8) vmcnt(11)\n\t" \
"v_mfma_f32_32x32x16_bf16 v[32:47], v[72:75], v[164:167], v[32:47]\n\t" \
"ds_read_b64_tr_b16 v[64:65], v26 offset:0\n\t" \
"ds_read_b64_tr_b16 v[66:67], v26 offset:1024\n\t" \
"v_mfma_f32_32x32x16_bf16 v[48:63], v[76:79], v[164:167], v[48:63]\n\t" \
"ds_read_b64_tr_b16 v[68:69], v27 offset:0\n\t" \
"ds_read_b64_tr_b16 v[70:71], v27 offset:1024\n\t" \
"s_waitcnt lgkmcnt(8) vmcnt(10)\n\t" \
"v_mfma_f32_32x32x16_bf16 v[32:47], v[80:83], v[168:171], v[32:47]\n\t" \
"ds_read_b64_tr_b16 v[72:73], v26 offset:2048\n\t" \
"ds_read_b64_tr_b16 v[74:75], v26 offset:3072\n\t" \
"v_mfma_f32_32x32x16_bf16 v[48:63], v[84:87], v[168:171], v[48:63]\n\t" \
"ds_read_b64_tr_b16 v[76:77], v27 offset:2048\n\t" \
"ds_read_b64_tr_b16 v[78:79], v27 offset:3072\n\t" \
"s_waitcnt vmcnt(2)\n\t" \
"v_cmp_ne_u64 vcc, s[44:45], v[14:15]\n\t" \
"s_nop 0\n\t" \
"s_cmp_lg_u64 vcc, 0\n\t" \
"s_cbranch_scc1 Lspin2_%=\n\t" \
"Lspun2_%=:\n\t" \
"s_add_u32 s58, s59, 4096\n\t" \
"buffer_load_dwordx4 v[112:115], v1, s[28:31], s58 offen offset:0 sc1\n\t" \
"buffer_load_dwordx4 v[116:119], v1, s[28:31], s58 offen offset:1024 sc1\n\t" \
"s_waitcnt lgkmcnt(8)\n\t" \
"v_mfma_f32_32x32x16_bf16 v[32:47], v[88:91], v[172:175], v[32:47]\n\t" \
"ds_read_b64_tr_b16 v[80:81], v26 offset:8192\n\t" \
"ds_read_b64_tr_b16 v[82:83], v26 offset:9216\n\t" \
"v_mfma_f32_32x32x16_bf16 v[48:63], v[92:95], v[172:175], v[48:63]\n\t" \
"ds_read_b64_tr_b16 v[84:85], v27 offset:8192\n\t" \
"ds_read_b64_tr_b16 v[86:87], v27 offset:9216\n\t" \
"s_add_u32 s58, s59, 20480\n\t" \
"buffer_load_dwordx4 v[120:123], v1, s[28:31], s58 offen offset:0 sc1\n\t" \
"buffer_load_dwordx4 v[124:127], v1, s[28:31], s58 offen offset:1024 sc1\n\t" \
"s_waitcnt lgkmcnt(8)\n\t" \
"v_mfma_f32_32x32x16_bf16 v[32:47], v[64:67], v[176:179], v[32:47]\n\t" \
"ds_read_b64_tr_b16 v[88:89], v26 offset:10240\n\t" \
"ds_read_b64_tr_b16 v[90:91], v26 offset:11264\n\t" \
"v_mfma_f32_32x32x16_bf16 v[48:63], v[68:71], v[176:179], v[48:63]\n\t" \
"ds_read_b64_tr_b16 v[92:93], v27 offset:10240\n\t" \
"ds_read_b64_tr_b16 v[94:95], v27 offset:11264\n\t" \
"s_add_u32 s58, s59, 36864\n\t" \
"buffer_load_dwordx4 v[128:131], v1, s[28:31], s58 offen offset:0 sc1\n\t" \
"s_waitcnt lgkmcnt(8)\n\t" \
"v_mfma_f32_32x32x16_bf16 v[32:47], v[72:75], v[180:183], v[32:47]\n\t" \
"ds_read_b64_tr_b16 v[64:65], v26 offset:16384\n\t" \
"ds_read_b64_tr_b16 v[66:67], v26 offset:17408\n\t" \
"v_mfma_f32_32x32x16_bf16 v[48:63], v[76:79], v[180:183], v[48:63]\n\t" \
"ds_read_b64_tr_b16 v[68:69], v27 offset:16384\n\t" \
"ds_read_b64_tr_b16 v[70:71], v27 offset:17408\n\t" \
"buffer_load_dwordx4 v[132:135], v1, s[28:31], s58 offen offset:1024 sc1\n\t" \
"s_waitcnt lgkmcnt(8)\n\t" \
"v_mfma_f32_32x32x16_bf16 v[32:47], v[80:83], v[184:187], v[32:47]\n\t" \
"ds_read_b64_tr_b16 v[72:73], v26 offset:18432\n\t" \
"ds_read_b64_tr_b16 v[74:75], v26 offset:19456\n\t" \
"v_mfma_f32_32x32x16_bf16 v[48:63], v[84:87], v[184:187], v[48:63]\n\t" \
"ds_read_b64_tr_b16 v[76:77], v27 offset:18432\n\t" \
"ds_read_b64_tr_b16 v[78:79], v27 offset:19456\n\t" \
"s_add_u32 s58, s59, 53248\n\t" \
"buffer_load_dwordx4 v[136:139], v1, s[28:31], s58 offen offset:0 sc1\n\t" \
"s_waitcnt lgkmcnt(8)\n\t" \
"v_mfma_f32_32x32x16_bf16 v[32:47], v[88:91], v[188:191], v[32:47]\n\t" \
"ds_read_b64_tr_b16 v[80:81], v26 offset:24576\n\t" \
"ds_read_b64_tr_b16 v[82:83], v26 offset:25600\n\t" \
"v_mfma_f32_32x32x16_bf16 v[48:63], v[92:95], v[188:191], v[48:63]\n\t" \
"ds_read_b64_tr_b16 v[84:85], v27 offset:24576\n\t" \
"ds_read_b64_tr_b16 v[86:87], v27 offset:25600\n\t" \
"buffer_load_dwordx4 v[140:143], v1, s[28:31], s58 offen offset:1024 sc1\n\t" \
"s_waitcnt lgkmcnt(8)\n\t" \
"v_mfma_f32_32x32x16_bf16 v[32:47], v[64:67], v[96:99], v[32:47]\n\t" \
"ds_read_b64_tr_b16 v[88:89], v26 offset:26624\n\t" \
"ds_read_b64_tr_b16 v[90:91], v26 offset:27648\n\t" \
"v_mfma_f32_32x32x16_bf16 v[48:63], v[68:71], v[96:99], v[48:63]\n\t" \
"ds_read_b64_tr_b16 v[92:93], v27 offset:26624\n\t" \
"ds_read_b64_tr_b16 v[94:95], v27 offset:27648\n\t" \
"s_add_u32 s58, s59, 69632\n\t" \
"buffer_load_dwordx4 v[144:147], v1, s[28:31], s58 offen offset:0 sc1\n\t" \
"s_waitcnt lgkmcnt(8)\n\t" \
"v_mfma_f32_32x32x16_bf16 v[32:47], v[72:75], v[100:103], v[32:47]\n\t" \
"ds_read_b64_tr_b16 v[64:65], v26 offset:32768\n\t" \
"ds_read_b64_tr_b16 v[66:67], v26 offset:33792\n\t" \
"v_mfma_f32_32x32x16_bf16 v[48:63], v[76:79], v[100:103], v[48:63]\n\t" \
"ds_read_b64_tr_b16 v[68:69], v27 offset:32768\n\t" \
"ds_read_b64_tr_b16 v[70:71], v27 offset:33792\n\t" \
"buffer_load_dwordx4 v[148:151], v1, s[28:31], s58 offen offset:1024 sc1\n\t" \
"s_waitcnt lgkmcnt(8) vmcnt(11)\n\t" \
"v_mfma_f32_32x32x16_bf16 v[32:47], v[80:83], v[104:107], v[32:47]\n\t" \
"ds_read_b64_tr_b16 v[72:73], v26 offset:34816\n\t" \
"ds_read_b64_tr_b16 v[74:75], v26 offset:35840\n\t" \
"v_mfma_f32_32x32x16_bf16 v[48:63], v[84:87], v[104:107], v[48:63]\n\t" \
"ds_read_b64_tr_b16 v[76:77], v27 offset:34816\n\t" \
"ds_read_b64_tr_b16 v[78:79], v27 offset:35840\n\t" \
"s_add_u32 s58, s59, 86016\n\t" \
"buffer_load_dwordx4 v[152:155], v1, s[28:31], s58 offen offset:0 sc1\n\t" \
"s_waitcnt lgkmcnt(8) vmcnt(11)\n\t" \
"v_mfma_f32_32x32x16_bf16 v[32:47], v[88:91], v[108:111], v[32:47]\n\t" \
"ds_read_b64_tr_b16 v[80:81], v26 offset:40960\n\t" \
"ds_read_b64_tr_b16 v[82:83], v26 offset:41984\n\t" \
"v_mfma_f32_32x32x16_bf16 v[48:63], v[92:95], v[108:111], v[48:63]\n\t" \
"ds_read_b64_tr_b16 v[84:85], v27 offset:40960\n\t" \
"ds_read_b64_tr_b16 v[86:87], v27 offset:41984\n\t" \
"buffer_load_dwordx4 v[156:159], v1, s[28:31], s58 offen offset:1024 sc1\n\t" \
"s_add_u32 s57, s57, 2048\n\t" \
"buffer_load_dwordx2 v[14:15], v13, s[28:31], s57 offen sc1\n\t" \
"s_waitcnt lgkmcnt(8) vmcnt(12)\n\t" \
"v_mfma_f32_32x32x16_bf16 v[32:47], v[64:67], v[112:115], v[32:47]\n\t" \
"ds_read_b64_tr_b16 v[88:89], v26 offset:43008\n\t" \
"ds_read_b64_tr_b16 v[90:91], v26 offset:44032\n\t" \
"v_mfma_f32_32x32x16_bf16 v[48:63], v[68:71], v[112:115], v[48:63]\n\t" \
"ds_read_b64_tr_b16 v[92:93], v27 offset:43008\n\t" \
"ds_read_b64_tr_b16 v[94:95], v27 offset:44032\n\t" \
"s_add_u32 s58, s59, 102400\n\t" \
"buffer_load_dwordx4 v[160:163], v1, s[28:31], s58 offen offset:0 sc1\n\t" \
"s_waitcnt lgkmcnt(8) vmcnt(12)\n\t" \
"v_mfma_f32_32x32x16_bf16 v[32:47], v[72:75], v[116:119], v[32:47]\n\t" \
"ds_read_b64_tr_b16 v[64:65], v26 offset:49152\n\t" \
"ds_read_b64_tr_b16 v[66:67], v26 offset:50176\n\t" \
"v_mfma_f32_32x32x16_bf16 v[48:63], v[76:79], v[116:119], v[48:63]\n\t" \
"ds_read_b64_tr_b16 v[68:69], v27 offset:49152\n\t" \
"ds_read_b64_tr_b16 v[70:71], v27 offset:50176\n\t" \
"buffer_load_dwordx4 v[164:167], v1, s[28:31], s58 offen offset:1024 sc1\n\t" \
"s_waitcnt lgkmcnt(8) vmcnt(12)\n\t" \
"v_mfma_f32_32x32x16_bf16 v[32:47], v[80:83], v[120:123], v[32:47]\n\t" \
"ds_read_b64_tr_b16 v[72:73], v26 offset:51200\n\t" \
"ds_read_b64_tr_b16 v[74:75], v26 offset:52224\n\t" \
"v_mfma_f32_32x32x16_bf16 v[48:63], v[84:87], v[120:123], v[48:63]\n\t" \
"ds_read_b64_tr_b16 v[76:77], v27 offset:51200\n\t" \
"ds_read_b64_tr_b16 v[78:79], v27 offset:52224\n\t" \
"s_waitcnt lgkmcnt(8) vmcnt(11)\n\t" \
"v_mfma_f32_32x32x16_bf16 v[32:47], v[88:91], v[124:127], v[32:47]\n\t" \
"ds_read_b64_tr_b16 v[80:81], v26 offset:57344\n\t" \
"ds_read_b64_tr_b16 v[82:83], v26 offset:58368\n\t" \
"v_mfma_f32_32x32x16_bf16 v[48:63], v[92:95], v[124:127], v[48:63]\n\t" \
"ds_read_b64_tr_b16 v[84:85], v27 offset:57344\n\t" \
"ds_read_b64_tr_b16 v[86:87], v27 offset:58368\n\t" \
"s_waitcnt lgkmcnt(8) vmcnt(10)\n\t" \
"v_mfma_f32_32x32x16_bf16 v[32:47], v[64:67], v[128:131], v[32:47]\n\t" \
"ds_read_b64_tr_b16 v[88:89], v26 offset:59392\n\t" \
"ds_read_b64_tr_b16 v[90:91], v26 offset:60416\n\t" \
"v_mfma_f32_32x32x16_bf16 v[48:63], v[68:71], v[128:131], v[48:63]\n\t" \
"ds_read_b64_tr_b16 v[92:93], v27 offset:59392\n\t" \
"ds_read_b64_tr_b16 v[94:95], v27 offset:60416\n\t" \
"s_waitcnt vmcnt(2)\n\t" \
"v_cmp_ne_u64 vcc, s[44:45], v[14:15]\n\t" \
"s_nop 0\n\t" \
"s_cmp_lg_u64 vcc, 0\n\t" \
"s_cbranch_scc1 Lspin3_%=\n\t" \
"Lspun3_%=:\n\t" \
"s_add_u32 s58, s59, 6144\n\t" \
"buffer_load_dwordx4 v[168:171], v1, s[28:31], s58 offen offset:0 sc1\n\t" \
"buffer_load_dwordx4 v[172:175], v1, s[28:31], s58 offen offset:1024 sc1\n\t" \
"s_barrier\n\t" \
"s_waitcnt lgkmcnt(8)\n\t" \
"v_mfma_f32_32x32x16_bf16 v[32:47], v[72:75], v[132:135], v[32:47]\n\t" \
"ds_read_b64_tr_b16 v[64:65], v28 offset:16384\n\t" \
"ds_read_b64_tr_b16 v[66:67], v28 offset:17408\n\t" \
"v_mfma_f32_32x32x16_bf16 v[48:63], v[76:79], v[132:135], v[48:63]\n\t" \
"ds_read_b64_tr_b16 v[68:69], v29 offset:16384\n\t" \
"ds_read_b64_tr_b16 v[70:71], v29 offset:17408\n\t" \
"s_add_u32 s58, s59, 22528\n\t" \
"buffer_load_dwordx4 v[176:179], v1, s[28:31], s58 offen offset:0 sc1\n\t" \
"buffer_load_dwordx4 v[180:183], v1, s[28:31], s58 offen offset:1024 sc1\n\t" \
"s_waitcnt lgkmcnt(8)\n\t" \
"v_mfma_f32_32x32x16_bf16 v[32:47], v[80:83], v[136:139], v[32:47]\n\t" \
"ds_read_b64_tr_b16 v[72:73], v28 offset:18432\n\t" \
"ds_read_b64_tr_b16 v[74:75], v28 offset:19456\n\t" \
"v_mfma_f32_32x32x16_bf16 v[48:63], v[84:87], v[136:139], v[48:63]\n\t" \
"ds_read_b64_tr_b16 v[76:77], v29 offset:18432\n\t" \
"ds_read_b64_tr_b16 v[78:79], v29 offset:19456\n\t" \
"s_add_u32 s58, s59, 38912\n\t" \
"buffer_load_dwordx4 v[184:187], v1, s[28:31], s58 offen offset:0 sc1\n\t" \
"s_waitcnt lgkmcnt(8)\n\t" \
"v_mfma_f32_32x32x16_bf16 v[32:47], v[88:91], v[140:143], v[32:47]\n\t" \
"ds_read_b64_tr_b16 v[80:81], v28 offset:24576\n\t" \
"ds_read_b64_tr_b16 v[82:83], v28 offset:25600\n\t" \
"v_mfma_f32_32x32x16_bf16 v[48:63], v[92:95], v[140:143], v[48:63]\n\t" \
"ds_read_b64_tr_b16 v[84:85], v29 offset:24576\n\t" \
"ds_read_b64_tr_b16 v[86:87], v29 offset:25600\n\t" \
"buffer_load_dwordx4 v[188:191], v1, s[28:31], s58 offen offset:1024 sc1\n\t" \
"s_waitcnt lgkmcnt(8)\n\t" \
"v_mfma_f32_32x32x16_bf16 v[32:47], v[64:67], v[144:147], v[32:47]\n\t" \
"ds_read_b64_tr_b16 v[88:89], v28 offset:26624\n\t" \
"ds_read_b64_tr_b16 v[90:91], v28 offset:27648\n\t" \
"v_mfma_f32_32x32x16_bf16 v[48:63], v[68:71], v[144:147], v[48:63]\n\t" \
"ds_read_b64_tr_b16 v[92:93], v29 offset:26624\n\t" \
"ds_read_b64_tr_b16 v[94:95], v29 offset:27648\n\t" \
"s_add_u32 s58, s59, 55296\n\t" \
"buffer_load_dwordx4 v[96:99], v1, s[28:31], s58 offen offset:0 sc1\n\t" \
"s_waitcnt lgkmcnt(8)\n\t" \
"v_mfma_f32_32x32x16_bf16 v[32:47], v[72:75], v[148:151], v[32:47]\n\t" \
"ds_read_b64_tr_b16 v[64:65], v28 offset:0\n\t" \
"ds_read_b64_tr_b16 v[66:67], v28 offset:1024\n\t" \
"v_mfma_f32_32x32x16_bf16 v[48:63], v[76:79], v[148:151], v[48:63]\n\t" \
"ds_read_b64_tr_b16 v[68:69], v29 offset:0\n\t" \
"ds_read_b64_tr_b16 v[70:71], v29 offset:1024\n\t" \
"buffer_load_dwordx4 v[100:103], v1, s[28:31], s58 offen offset:1024 sc1\n\t" \
"s_waitcnt lgkmcnt(8)\n\t" \
"v_mfma_f32_32x32x16_bf16 v[32:47], v[80:83], v[152:155], v[32:47]\n\t" \
"ds_read_b64_tr_b16 v[72:73], v28 offset:2048\n\t" \
"ds_read_b64_tr_b16 v[74:75], v28 offset:3072\n\t" \
"v_mfma_f32_32x32x16_bf16 v[48:63], v[84:87], v[152:155], v[48:63]\n\t" \
"ds_read_b64_tr_b16 v[76:77], v29 offset:2048\n\t" \
"ds_read_b64_tr_b16 v[78:79], v29 offset:3072\n\t" \
"s_add_u32 s58, s59, 71680\n\t" \
"buffer_load_dwordx4 v[104:107], v1, s[28:31], s58 offen offset:0 sc1\n\t" \
"s_waitcnt lgkmcnt(8)\n\t" \
"v_mfma_f32_32x32x16_bf16 v[32:47], v[88:91], v[156:159], v[32:47]\n\t" \
"ds_read_b64_tr_b16 v[80:81], v28 offset:8192\n\t" \
"ds_read_b64_tr_b16 v[82:83], v28 offset:9216\n\t" \
"v_mfma_f32_32x32x16_bf16 v[48:63], v[92:95], v[156:159], v[48:63]\n\t" \
"ds_read_b64_tr_b16 v[84:85], v29 offset:8192\n\t" \
"ds_read_b64_tr_b16 v[86:87], v29 offset:9216\n\t" \
"buffer_load_dwordx4 v[108:111], v1, s[28:31], s58 offen offset:1024 sc1\n\t" \
"s_waitcnt lgkmcnt(8) vmcnt(11)\n\t" \
"v_mfma_f32_32x32x16_bf16 v[32:47], v[64:67], v[160:163], v[32:47]\n\t" \
"ds_read_b64_tr_b16 v[88:89], v28 offset:10240\n\t" \
"ds_read_b64_tr_b16 v[90:91], v28 offset:11264\n\t" \
"v_mfma_f32_32x32x16_bf16 v[48:63], v[68:71], v[160:163], v[48:63]\n\t" \
"ds_read_b64_tr_b16 v[92:93], v29 offset:10240\n\t" \
"ds_read_b64_tr_b16 v[94:95], v29 offset:11264\n\t" \
"s_add_u32 s58, s59, 88064\n\t" \
"buffer_load_dwordx4 v[112:115], v1, s[28:31], s58 offen offset:0 sc1\n\t" \
"s_waitcnt lgkmcnt(8) vmcnt(11)\n\t" \
"v_mfma_f32_32x32x16_bf16 v[32:47], v[72:75], v[164:167], v[32:47]\n\t" \
"ds_read_b64_tr_b16 v[64:65], v24 offset:0\n\t" \
"ds_read_b64_tr_b16 v[66:67], v24 offset:1024\n\t" \
"v_mfma_f32_32x32x16_bf16 v[48:63], v[76:79], v[164:167], v[48:63]\n\t" \
"ds_read_b64_tr_b16 v[68:69], v25 offset:0\n\t" \
"ds_read_b64_tr_b16 v[70:71], v25 offset:1024\n\t" \
"buffer_load_dwordx4 v[116:119], v1, s[28:31], s58 offen offset:1024 sc1\n\t" \
"s_waitcnt lgkmcnt(8) vmcnt(11)\n\t" \
"v_mfma_f32_32x32x16_bf16 v[32:47], v[80:83], v[168:171], v[32:47]\n\t" \
"ds_read_b64_tr_b16 v[72:73], v24 offset:2048\n\t" \
"ds_read_b64_tr_b16 v[74:75], v24 offset:3072\n\t" \
"v_mfma_f32_32x32x16_bf16 v[48:63], v[84:87], v[168:171], v[48:63]\n\t" \
"ds_read_b64_tr_b16 v[76:77], v25 offset:2048\n\t" \
"ds_read_b64_tr_b16 v[78:79], v25 offset:3072\n\t" \
"s_add_u32 s58, s59, 104448\n\t" \
"buffer_load_dwordx4 v[120:123], v1, s[28:31], s58 offen offset:0 sc1\n\t" \
"s_waitcnt lgkmcnt(8) vmcnt(11)\n\t" \
"v_mfma_f32_32x32x16_bf16 v[32:47], v[88:91], v[172:175], v[32:47]\n\t" \
"ds_read_b64_tr_b16 v[80:81], v24 offset:8192\n\t" \
"ds_read_b64_tr_b16 v[82:83], v24 offset:9216\n\t" \
"v_mfma_f32_32x32x16_bf16 v[48:63], v[92:95], v[172:175], v[48:63]\n\t" \
"ds_read_b64_tr_b16 v[84:85], v25 offset:8192\n\t" \
"ds_read_b64_tr_b16 v[86:87], v25 offset:9216\n\t" \
"buffer_load_dwordx4 v[124:127], v1, s[28:31], s58 offen offset:1024 sc1\n\t" \
"s_waitcnt lgkmcnt(8) vmcnt(11)\n\t" \
"v_mfma_f32_32x32x16_bf16 v[32:47], v[64:67], v[176:179], v[32:47]\n\t" \
"ds_read_b64_tr_b16 v[88:89], v24 offset:10240\n\t" \
"ds_read_b64_tr_b16 v[90:91], v24 offset:11264\n\t" \
"v_mfma_f32_32x32x16_bf16 v[48:63], v[68:71], v[176:179], v[48:63]\n\t" \
"ds_read_b64_tr_b16 v[92:93], v25 offset:10240\n\t" \
"ds_read_b64_tr_b16 v[94:95], v25 offset:11264\n\t" \
"s_waitcnt lgkmcnt(8) vmcnt(10)\n\t" \
"v_mfma_f32_32x32x16_bf16 v[32:47], v[72:75], v[180:183], v[32:47]\n\t" \
"ds_read_b64_tr_b16 v[64:65], v24 offset:16384\n\t" \
"ds_read_b64_tr_b16 v[66:67], v24 offset:17408\n\t" \
"s_lshl_b32 s58, s55, 14\n\t" \
"s_add_u32 s58, s58, 49152\n\t" \
"v_and_b32 v12, 31, v0\n\t" \
"v_mfma_f32_32x32x16_bf16 v[48:63], v[76:79], v[180:183], v[48:63]\n\t" \
"ds_read_b64_tr_b16 v[68:69], v25 offset:16384\n\t" \
"ds_read_b64_tr_b16 v[70:71], v25 offset:17408\n\t" \
"v_lshrrev_b32 v13, 5, v0\n\t" \
"v_and_b32 v18, 7, v0\n\t" \
"v_xor_b32 v13, v13, v18\n\t" \
"s_waitcnt lgkmcnt(8) vmcnt(9)\n\t" \
"v_mfma_f32_32x32x16_bf16 v[32:47], v[80:83], v[184:187], v[32:47]\n\t" \
"ds_read_b64_tr_b16 v[72:73], v24 offset:18432\n\t" \
"ds_read_b64_tr_b16 v[74:75], v24 offset:19456\n\t" \
"v_lshlrev_b32 v13, 4, v13\n\t" \
"v_lshl_add_u32 v13, v12, 8, v13\n\t" \
"v_mfma_f32_32x32x16_bf16 v[48:63], v[84:87], v[184:187], v[48:63]\n\t" \
"ds_read_b64_tr_b16 v[76:77], v25 offset:18432\n\t" \
"ds_read_b64_tr_b16 v[78:79], v25 offset:19456\n\t" \
"s_and_b32 s57, s56, 1\n\t" \
"s_lshl_b32 s57, s57, 13\n\t" \
"s_add_u32 s57, s57, s58\n\t" \
"s_waitcnt lgkmcnt(8) vmcnt(8)\n\t" \
"v_mfma_f32_32x32x16_bf16 v[32:47], v[88:91], v[188:191], v[32:47]\n\t" \
"ds_read_b64_tr_b16 v[80:81], v24 offset:24576\n\t" \
"ds_read_b64_tr_b16 v[82:83], v24 offset:25600\n\t" \
"v_add_u32 v18, s57, v13\n\t" \
"v_xor_b32 v19, 32, v18\n\t" \
"v_mfma_f32_32x32x16_bf16 v[48:63], v[92:95], v[188:191], v[48:63]\n\t" \
"ds_read_b64_tr_b16 v[84:85], v25 offset:24576\n\t" \
"ds_read_b64_tr_b16 v[86:87], v25 offset:25600\n\t" \
"v_xor_b32 v20, 64, v18\n\t" \
"v_xor_b32 v21, 96, v18\n\t" \
"v_lshrrev_b32 v14, 3, v0\n\t" \
"s_waitcnt lgkmcnt(8) vmcnt(7)\n\t" \
"v_mfma_f32_32x32x16_bf16 v[32:47], v[64:67], v[96:99], v[32:47]\n\t" \
"ds_read_b64_tr_b16 v[88:89], v24 offset:26624\n\t" \
"ds_read_b64_tr_b16 v[90:91], v24 offset:27648\n\t" \
"v_and_b32 v15, 7, v0\n\t" \
"v_lshlrev_b32 v16, 1, v15\n\t" \
"v_mfma_f32_32x32x16_bf16 v[48:63], v[68:71], v[96:99], v[48:63]\n\t" \
"ds_read_b64_tr_b16 v[92:93], v25 offset:26624\n\t" \
"ds_read_b64_tr_b16 v[94:95], v25 offset:27648\n\t" \
"v_xor_b32 v16, v16, v14\n\t" \
"v_lshlrev_b32 v16, 4, v16\n\t" \
"v_lshl_add_u32 v16, v14, 8, v16\n\t" \
"s_waitcnt lgkmcnt(8) vmcnt(6)\n\t" \
"v_mfma_f32_32x32x16_bf16 v[32:47], v[72:75], v[100:103], v[32:47]\n\t" \
"ds_read_b64_tr_b16 v[64:65], v24 offset:32768\n\t" \
"ds_read_b64_tr_b16 v[66:67], v24 offset:33792\n\t" \
"s_lshl_b32 s57, s26, 11\n\t" \
"s_add_u32 s57, s57, 49152\n\t" \
"v_add_u32 v16, s57, v16\n\t" \
"v_mfma_f32_32x32x16_bf16 v[48:63], v[76:79], v[100:103], v[48:63]\n\t" \
"ds_read_b64_tr_b16 v[68:69], v25 offset:32768\n\t" \
"ds_read_b64_tr_b16 v[70:71], v25 offset:33792\n\t" \
"v_xor_b32 v17, 16, v16\n\t" \
"v_lshlrev_b32 v15, 4, v15\n\t" \
"s_waitcnt lgkmcnt(8) vmcnt(5)\n\t" \
"v_mfma_f32_32x32x16_bf16 v[32:47], v[80:83], v[104:107], v[32:47]\n\t" \
"ds_read_b64_tr_b16 v[72:73], v24 offset:34816\n\t" \
"ds_read_b64_tr_b16 v[74:75], v24 offset:35840\n\t" \
"v_lshl_add_u32 v14, v14, 10, v15\n\t" \
"s_lshl_b32 s57, s51, 16\n\t" \
"s_lshl_b32 s58, s26, 13\n\t" \
"v_mfma_f32_32x32x16_bf16 v[48:63], v[84:87], v[104:107], v[48:63]\n\t" \
"ds_read_b64_tr_b16 v[76:77], v25 offset:34816\n\t" \
"ds_read_b64_tr_b16 v[78:79], v25 offset:35840\n\t" \
"s_add_u32 s57, s57, s58\n\t" \
"s_lshl_b32 s58, s50, 7\n\t" \
"s_waitcnt lgkmcnt(8) vmcnt(4)\n\t" \
"v_mfma_f32_32x32x16_bf16 v[32:47], v[88:91], v[108:111], v[32:47]\n\t" \
"ds_read_b64_tr_b16 v[80:81], v24 offset:40960\n\t" \
"ds_read_b64_tr_b16 v[82:83], v24 offset:41984\n\t" \
"s_add_u32 s63, s57, s58\n\t" \
"s_mov_b32 s40, s22\n\t" \
"s_and_b32 s41, s23, 0xffff\n\t" \
"v_mfma_f32_32x32x16_bf16 v[48:63], v[92:95], v[108:111], v[48:63]\n\t" \
"ds_read_b64_tr_b16 v[84:85], v25 offset:40960\n\t" \
"ds_read_b64_tr_b16 v[86:87], v25 offset:41984\n\t" \
"s_mov_b32 s42, 2097152\n\t" \
"s_mov_b32 s43, 0x20000\n\t" \
"s_waitcnt lgkmcnt(8) vmcnt(3)\n\t" \
"v_mfma_f32_32x32x16_bf16 v[32:47], v[64:67], v[112:115], v[32:47]\n\t" \
"ds_read_b64_tr_b16 v[88:89], v24 offset:43008\n\t" \
"ds_read_b64_tr_b16 v[90:91], v24 offset:44032\n\t" \
"ds_read_b64_tr_b16 v[92:93], v25 offset:43008\n\t" \
"ds_read_b64_tr_b16 v[94:95], v25 offset:44032\n\t" \
"s_waitcnt lgkmcnt(8) vmcnt(2)\n\t" \
"v_mfma_f32_32x32x16_bf16 v[32:47], v[72:75], v[116:119], v[32:47]\n\t" \
"s_waitcnt lgkmcnt(4) vmcnt(1)\n\t" \
"v_mfma_f32_32x32x16_bf16 v[32:47], v[80:83], v[120:123], v[32:47]\n\t" \
"s_waitcnt lgkmcnt(0) vmcnt(0)\n\t" \
"v_mfma_f32_32x32x16_bf16 v[32:47], v[88:91], v[124:127], v[32:47]\n\t" \
"v_mfma_f32_32x32x16_bf16 v[48:63], v[68:71], v[112:115], v[48:63]\n\t" \
"v_mfma_f32_32x32x16_bf16 v[48:63], v[76:79], v[116:119], v[48:63]\n\t" \
"s_nop 9\n\t" \
"ds_write_b128 v18, v[32:35] offset:0\n\t" \
"v_mfma_f32_32x32x16_bf16 v[48:63], v[84:87], v[120:123], v[48:63]\n\t" \
"ds_write_b128 v19, v[36:39] offset:0\n\t" \
"v_mfma_f32_32x32x16_bf16 v[48:63], v[92:95], v[124:127], v[48:63]\n\t" \
"ds_write_b128 v20, v[40:43] offset:0\n\t" \
"ds_write_b128 v21, v[44:47] offset:0\n\t" \
"s_nop 9\n\t" \
"ds_write_b128 v18, v[48:51] offset:128\n\t" \
"ds_write_b128 v19, v[52:55] offset:128\n\t" \
"ds_write_b128 v20, v[56:59] offset:128\n\t" \
"ds_write_b128 v21, v[60:63] offset:128\n\t" \
"s_branch Lfinal_%=\n\t" \
"Lpathb_%=:\n\t" \
"s_setprio 2\n\t" \
"v_and_b32 v12, 31, v0\n\t" \
"v_lshrrev_b32 v14, 5, v0\n\t" \
"v_lshlrev_b32 v14, 3, v14\n\t" \
"s_lshl_b32 s64, s50, 7\n\t" \
"s_lshl_b32 s57, s55, 6\n\t" \
"s_add_u32 s64, s64, s57\n\t" \
"s_lshl_b32 s59, s54, 15\n\t" \
"s_add_u32 s59, s59, s64\n\t" \
"v_lshrrev_b32 v16, 4, v0\n\t" \
"v_xor_b32 v16, v16, v0\n\t" \
"v_and_b32 v16, 3, v16\n\t" \
"v_lshlrev_b32 v16, 4, v16\n\t" \
"v_lshrrev_b32 v17, 2, v0\n\t" \
"v_lshl_add_u32 v16, v17, 10, v16\n\t" \
"s_add_u32 m0, s62, 147456\n\t" \
"s_add_u32 s63, s59, 0\n\t" \
"buffer_load_dwordx4 v16, s[32:35], s63 offen lds\n\t" \
"s_add_u32 m0, s62, 148480\n\t" \
"s_add_u32 s63, s59, 16384\n\t" \
"buffer_load_dwordx4 v16, s[32:35], s63 offen lds\n\t" \
"v_bfe_u32 v17, v0, 2, 2\n\t" \
"v_lshlrev_b32 v17, 4, v17\n\t" \
"v_lshl_add_u32 v17, v12, 6, v17\n\t" \
"v_add_u32 v17, v17, v14\n\t" \
"s_add_u32 s57, s62, 147456\n\t" \
"v_add_u32 v32, s57, v17\n\t" \
"v_xor_b32 v33, 16, v32\n\t" \
"v_xor_b32 v34, 32, v32\n\t" \
"v_xor_b32 v35, 48, v32\n\t" \
"v_add_u32 v14, s64, v14\n\t" \
"global_load_dwordx2 v[24:25], v14, s[20:21] offset:0\n\t" \
"global_load_dwordx2 v[26:27], v14, s[20:21] offset:16\n\t" \
"global_load_dwordx2 v[28:29], v14, s[20:21] offset:32\n\t" \
"global_load_dwordx2 v[30:31], v14, s[20:21] offset:48\n\t" \
"v_lshrrev_b32 v12, 5, v0\n\t" \
"v_bfe_u32 v8, v0, 2, 2\n\t" \
"v_lshl_add_u32 v8, v12, 2, v8\n\t" \
"v_lshl_add_u32 v8, v8, 7, v3\n\t" \
"s_lshl_b32 s57, s55, 6\n\t" \
"v_xor_b32 v8, s57, v8\n\t" \
"s_lshl_b32 s57, s55, 15\n\t" \
"v_add_u32 v8, s57, v8\n\t" \
"v_xor_b32 v7, 64, v8\n\t" \
"v_add_u32 v6, s62, v1\n\t" \
"v_add_u32 v2, 0x10000, v6\n\t" \
"v_add_u32 v3, 0x20000, v6\n\t" \
"v_add_u32 v9, 147456, v6\n\t" \
"v_xor_b32 v10, 0x2000, v9\n\t" \
"s_waitcnt vmcnt(4)\n\t" \
"ds_read_b64 v[16:17], v32\n\t" \
"ds_read_b64 v[18:19], v33\n\t" \
"ds_read_b64 v[20:21], v34\n\t" \
"ds_read_b64 v[22:23], v35\n\t" \
"s_waitcnt lgkmcnt(0)\n\t" \
"ds_read_b64_tr_b16 v[32:33], v7 offset:0\n\t" \
"ds_read_b64_tr_b16 v[34:35], v7 offset:1024\n\t" \
"ds_read_b64_tr_b16 v[36:37], v8 offset:0\n\t" \
"ds_read_b64_tr_b16 v[38:39], v8 offset:1024\n\t" \
"ds_read_b64_tr_b16 v[40:41], v7 offset:2048\n\t" \
"ds_read_b64_tr_b16 v[42:43], v7 offset:3072\n\t" \
"ds_read_b64_tr_b16 v[44:45], v8 offset:2048\n\t" \
"ds_read_b64_tr_b16 v[46:47], v8 offset:3072\n\t" \
"ds_read_b64_tr_b16 v[48:49], v7 offset:4096\n\t" \
"ds_read_b64_tr_b16 v[50:51], v7 offset:5120\n\t" \
"ds_read_b64_tr_b16 v[52:53], v8 offset:4096\n\t" \
"ds_read_b64_tr_b16 v[54:55], v8 offset:5120\n\t" \
"v_lshlrev_b32 v80, 16, v16\n\t" \
"v_and_b32 v81, 0xffff0000, v16\n\t" \
"v_lshlrev_b32 v82, 16, v17\n\t" \
"v_and_b32 v83, 0xffff0000, v17\n\t" \
"v_lshlrev_b32 v84, 16, v18\n\t" \
"v_and_b32 v85, 0xffff0000, v18\n\t" \
"v_lshlrev_b32 v86, 16, v19\n\t" \
"v_and_b32 v87, 0xffff0000, v19\n\t" \
"v_lshlrev_b32 v88, 16, v20\n\t" \
"v_and_b32 v89, 0xffff0000, v20\n\t" \
"v_lshlrev_b32 v90, 16, v21\n\t" \
"v_and_b32 v91, 0xffff0000, v21\n\t" \
"v_lshlrev_b32 v92, 16, v22\n\t" \
"v_and_b32 v93, 0xffff0000, v22\n\t" \
"v_lshlrev_b32 v94, 16, v23\n\t" \
"v_and_b32 v95, 0xffff0000, v23\n\t" \
"s_lshl_b32 s57, s51, 1\n\t" \
"s_add_u32 s57, s57, s55\n\t" \
"s_lshl_b32 s57, s57, 9\n\t" \
"s_add_u32 s57, s57, 98304\n\t" \
"s_lshl_b32 s58, s54, 15\n\t" \
"s_lshl_b32 s63, s55, 14\n\t" \
"s_add_u32 s58, s58, s63\n\t" \
"s_add_u32 s58, s58, 5242880\n\t" \
"s_lshl_b32 s76, s54, 4\n\t" \
"s_lshl_b32 s77, s50, 1\n\t" \
"s_add_u32 s76, s76, s77\n\t" \
"s_add_u32 s76, s76, s55\n\t" \
"s_lshl_b32 s77, s76, 11\n\t" \
"s_add_u32 s77, s77, 7340032\n\t" \
"s_lshl_b32 s78, s76, 8\n\t" \
"s_add_u32 s78, s78, 9437184\n\t" \
"v_lshlrev_b32 v13, 3, v0\n\t" \
"s_lshl_b32 s76, s76, 7\n\t" \
"s_add_u32 s76, s76, 131072\n\t" \
"v_min_u32 v15, 23, v0\n\t" \
"s_mul_i32 s63, s50, 1536\n\t" \
"s_add_u32 s63, s63, 4096\n\t" \
"v_lshl_add_u32 v15, v15, 6, s63\n\t" \
"v_min_u32 v12, 3, v0\n\t" \
"v_lshlrev_b32 v12, 7, v12\n\t" \
"s_mov_b32 s65, 0x40000\n\t" \
"Lpoll8_%=:\n\t" \
"buffer_load_dwordx2 v[16:17], v12, s[28:31], s57 offen sc1\n\t" \
"s_waitcnt vmcnt(0)\n\t" \
"v_cmp_ne_u64 vcc, s[44:45], v[16:17]\n\t" \
"s_nop 0\n\t" \
"s_cmp_eq_u64 vcc, 0\n\t" \
"s_cbranch_scc1 Lpdone8_%=\n\t" \
"s_sleep 1\n\t" \
"s_sub_u32 s65, s65, 1\n\t" \
"s_cmp_lg_u32 s65, 0\n\t" \
"s_cbranch_scc1 Lpoll8_%=\n\t" \
"Lpdone8_%=:\n\t" \
"buffer_load_dwordx4 v[112:115], v1, s[28:31], s58 offen offset:0 sc1\n\t" \
"buffer_load_dwordx4 v[116:119], v1, s[28:31], s58 offen offset:1024 sc1\n\t" \
"buffer_load_dwordx4 v[120:123], v1, s[28:31], s58 offen offset:2048 sc1\n\t" \
"buffer_load_dwordx4 v[124:127], v1, s[28:31], s58 offen offset:3072 sc1\n\t" \
"s_add_u32 s58, s58, 4096\n\t" \
"buffer_load_dwordx4 v[128:131], v1, s[28:31], s58 offen offset:0 sc1\n\t" \
"buffer_load_dwordx4 v[132:135], v1, s[28:31], s58 offen offset:1024 sc1\n\t" \
"buffer_load_dwordx4 v[136:139], v1, s[28:31], s58 offen offset:2048 sc1\n\t" \
"buffer_load_dwordx4 v[140:143], v1, s[28:31], s58 offen offset:3072 sc1\n\t" \
"s_add_u32 s58, s58, 4096\n\t" \
"buffer_load_dwordx4 v[144:147], v1, s[28:31], s58 offen offset:0 sc1\n\t" \
"buffer_load_dwordx4 v[148:151], v1, s[28:31], s58 offen offset:1024 sc1\n\t" \
"buffer_load_dwordx4 v[152:155], v1, s[28:31], s58 offen offset:2048 sc1\n\t" \
"buffer_load_dwordx4 v[156:159], v1, s[28:31], s58 offen offset:3072 sc1\n\t" \
"s_waitcnt lgkmcnt(8) vmcnt(11)\n\t" \
"v_mfma_f32_32x32x16_bf16 v[64:79], v[32:35], v[112:115], 0\n\t" \
"ds_read_b64_tr_b16 v[56:57], v7 offset:6144\n\t" \
"ds_read_b64_tr_b16 v[58:59], v7 offset:7168\n\t" \
"v_mfma_f32_32x32x16_bf16 v[80:95], v[36:39], v[112:115], v[80:95]\n\t" \
"ds_read_b64_tr_b16 v[60:61], v8 offset:6144\n\t" \
"ds_read_b64_tr_b16 v[62:63], v8 offset:7168\n\t" \
"s_add_u32 s58, s58, 4096\n\t" \
"buffer_load_dwordx4 v[160:163], v1, s[28:31], s58 offen offset:0 sc1\n\t" \
"s_waitcnt lgkmcnt(8) vmcnt(11)\n\t" \
"v_mfma_f32_32x32x16_bf16 v[64:79], v[40:43], v[116:119], v[64:79]\n\t" \
"ds_read_b64_tr_b16 v[32:33], v7 offset:8192\n\t" \
"ds_read_b64_tr_b16 v[34:35], v7 offset:9216\n\t" \
"v_mfma_f32_32x32x16_bf16 v[80:95], v[44:47], v[116:119], v[80:95]\n\t" \
"ds_read_b64_tr_b16 v[36:37], v8 offset:8192\n\t" \
"ds_read_b64_tr_b16 v[38:39], v8 offset:9216\n\t" \
"buffer_load_dwordx4 v[164:167], v1, s[28:31], s58 offen offset:1024 sc1\n\t" \
"s_waitcnt lgkmcnt(8) vmcnt(11)\n\t" \
"v_mfma_f32_32x32x16_bf16 v[64:79], v[48:51], v[120:123], v[64:79]\n\t" \
"ds_read_b64_tr_b16 v[40:41], v7 offset:10240\n\t" \
"ds_read_b64_tr_b16 v[42:43], v7 offset:11264\n\t" \
"v_mfma_f32_32x32x16_bf16 v[80:95], v[52:55], v[120:123], v[80:95]\n\t" \
"ds_read_b64_tr_b16 v[44:45], v8 offset:10240\n\t" \
"ds_read_b64_tr_b16 v[46:47], v8 offset:11264\n\t" \
"buffer_load_dwordx4 v[168:171], v1, s[28:31], s58 offen offset:2048 sc1\n\t" \
"s_waitcnt lgkmcnt(8) vmcnt(11)\n\t" \
"v_mfma_f32_32x32x16_bf16 v[64:79], v[56:59], v[124:127], v[64:79]\n\t" \
"ds_read_b64_tr_b16 v[48:49], v7 offset:12288\n\t" \
"ds_read_b64_tr_b16 v[50:51], v7 offset:13312\n\t" \
"v_mfma_f32_32x32x16_bf16 v[80:95], v[60:63], v[124:127], v[80:95]\n\t" \
"ds_read_b64_tr_b16 v[52:53], v8 offset:12288\n\t" \
"ds_read_b64_tr_b16 v[54:55], v8 offset:13312\n\t" \
"buffer_load_dwordx4 v[172:175], v1, s[28:31], s58 offen offset:3072 sc1\n\t" \
"s_waitcnt lgkmcnt(8) vmcnt(11)\n\t" \
"v_mfma_f32_32x32x16_bf16 v[64:79], v[32:35], v[128:131], v[64:79]\n\t" \
"ds_read_b64_tr_b16 v[56:57], v7 offset:14336\n\t" \
"ds_read_b64_tr_b16 v[58:59], v7 offset:15360\n\t" \
"v_mfma_f32_32x32x16_bf16 v[80:95], v[36:39], v[128:131], v[80:95]\n\t" \
"ds_read_b64_tr_b16 v[60:61], v8 offset:14336\n\t" \
"ds_read_b64_tr_b16 v[62:63], v8 offset:15360\n\t" \
"s_waitcnt lgkmcnt(8) vmcnt(10)\n\t" \
"v_mfma_f32_32x32x16_bf16 v[64:79], v[40:43], v[132:135], v[64:79]\n\t" \
"ds_read_b64_tr_b16 v[32:33], v7 offset:16384\n\t" \
"ds_read_b64_tr_b16 v[34:35], v7 offset:17408\n\t" \
"v_mfma_f32_32x32x16_bf16 v[80:95], v[44:47], v[132:135], v[80:95]\n\t" \
"ds_read_b64_tr_b16 v[36:37], v8 offset:16384\n\t" \
"ds_read_b64_tr_b16 v[38:39], v8 offset:17408\n\t" \
"s_waitcnt lgkmcnt(8) vmcnt(9)\n\t" \
"v_mfma_f32_32x32x16_bf16 v[64:79], v[48:51], v[136:139], v[64:79]\n\t" \
"ds_read_b64_tr_b16 v[40:41], v7 offset:18432\n\t" \
"ds_read_b64_tr_b16 v[42:43], v7 offset:19456\n\t" \
"v_mfma_f32_32x32x16_bf16 v[80:95], v[52:55], v[136:139], v[80:95]\n\t" \
"ds_read_b64_tr_b16 v[44:45], v8 offset:18432\n\t" \
"ds_read_b64_tr_b16 v[46:47], v8 offset:19456\n\t" \
"s_waitcnt lgkmcnt(8) vmcnt(8)\n\t" \
"v_mfma_f32_32x32x16_bf16 v[64:79], v[56:59], v[140:143], v[64:79]\n\t" \
"ds_read_b64_tr_b16 v[48:49], v7 offset:20480\n\t" \
"ds_read_b64_tr_b16 v[50:51], v7 offset:21504\n\t" \
"v_mfma_f32_32x32x16_bf16 v[80:95], v[60:63], v[140:143], v[80:95]\n\t" \
"ds_read_b64_tr_b16 v[52:53], v8 offset:20480\n\t" \
"ds_read_b64_tr_b16 v[54:55], v8 offset:21504\n\t" \
"s_waitcnt lgkmcnt(8) vmcnt(7)\n\t" \
"v_mfma_f32_32x32x16_bf16 v[64:79], v[32:35], v[144:147], v[64:79]\n\t" \
"ds_read_b64_tr_b16 v[56:57], v7 offset:22528\n\t" \
"ds_read_b64_tr_b16 v[58:59], v7 offset:23552\n\t" \
"v_mfma_f32_32x32x16_bf16 v[80:95], v[36:39], v[144:147], v[80:95]\n\t" \
"ds_read_b64_tr_b16 v[60:61], v8 offset:22528\n\t" \
"ds_read_b64_tr_b16 v[62:63], v8 offset:23552\n\t" \
"s_waitcnt lgkmcnt(8) vmcnt(6)\n\t" \
"v_mfma_f32_32x32x16_bf16 v[64:79], v[40:43], v[148:151], v[64:79]\n\t" \
"ds_read_b64_tr_b16 v[32:33], v7 offset:24576\n\t" \
"ds_read_b64_tr_b16 v[34:35], v7 offset:25600\n\t" \
"v_mfma_f32_32x32x16_bf16 v[80:95], v[44:47], v[148:151], v[80:95]\n\t" \
"ds_read_b64_tr_b16 v[36:37], v8 offset:24576\n\t" \
"ds_read_b64_tr_b16 v[38:39], v8 offset:25600\n\t" \
"s_waitcnt lgkmcnt(8) vmcnt(5)\n\t" \
"v_mfma_f32_32x32x16_bf16 v[64:79], v[48:51], v[152:155], v[64:79]\n\t" \
"ds_read_b64_tr_b16 v[40:41], v7 offset:26624\n\t" \
"ds_read_b64_tr_b16 v[42:43], v7 offset:27648\n\t" \
"v_mfma_f32_32x32x16_bf16 v[80:95], v[52:55], v[152:155], v[80:95]\n\t" \
"ds_read_b64_tr_b16 v[44:45], v8 offset:26624\n\t" \
"ds_read_b64_tr_b16 v[46:47], v8 offset:27648\n\t" \
"s_waitcnt lgkmcnt(8) vmcnt(4)\n\t" \
"v_mfma_f32_32x32x16_bf16 v[64:79], v[56:59], v[156:159], v[64:79]\n\t" \
"ds_read_b64_tr_b16 v[48:49], v7 offset:28672\n\t" \
"ds_read_b64_tr_b16 v[50:51], v7 offset:29696\n\t" \
"v_mfma_f32_32x32x16_bf16 v[80:95], v[60:63], v[156:159], v[80:95]\n\t" \
"ds_read_b64_tr_b16 v[52:53], v8 offset:28672\n\t" \
"ds_read_b64_tr_b16 v[54:55], v8 offset:29696\n\t" \
"s_waitcnt lgkmcnt(8) vmcnt(3)\n\t" \
"v_mfma_f32_32x32x16_bf16 v[64:79], v[32:35], v[160:163], v[64:79]\n\t" \
"ds_read_b64_tr_b16 v[56:57], v7 offset:30720\n\t" \
"ds_read_b64_tr_b16 v[58:59], v7 offset:31744\n\t" \
"v_mfma_f32_32x32x16_bf16 v[80:95], v[36:39], v[160:163], v[80:95]\n\t" \
"ds_read_b64_tr_b16 v[60:61], v8 offset:30720\n\t" \
"ds_read_b64_tr_b16 v[62:63], v8 offset:31744\n\t" \
"s_waitcnt lgkmcnt(8) vmcnt(2)\n\t" \
"v_mfma_f32_32x32x16_bf16 v[64:79], v[40:43], v[164:167], v[64:79]\n\t" \
"v_mfma_f32_32x32x16_bf16 v[80:95], v[44:47], v[164:167], v[80:95]\n\t" \
"s_waitcnt lgkmcnt(4) vmcnt(1)\n\t" \
"v_mfma_f32_32x32x16_bf16 v[64:79], v[48:51], v[168:171], v[64:79]\n\t" \
"v_mfma_f32_32x32x16_bf16 v[80:95], v[52:55], v[168:171], v[80:95]\n\t" \
"s_waitcnt lgkmcnt(0) vmcnt(0)\n\t" \
"v_mfma_f32_32x32x16_bf16 v[64:79], v[56:59], v[172:175], v[64:79]\n\t" \
"v_mfma_f32_32x32x16_bf16 v[80:95], v[60:63], v[172:175], v[80:95]\n\t" \
"s_nop 10\n\t" \
"ds_write_b128 v9, v[64:67] offset:0\n\t" \
"ds_write_b128 v9, v[68:71] offset:1024\n\t" \
"ds_write_b128 v9, v[72:75] offset:2048\n\t" \
"ds_write_b128 v9, v[76:79] offset:3072\n\t" \
"s_waitcnt lgkmcnt(0)\n\t" \
"s_barrier\n\t" \
"ds_read_b128 v[64:67], v10 offset:0\n\t" \
"ds_read_b128 v[68:71], v10 offset:1024\n\t" \
"ds_read_b128 v[72:75], v10 offset:2048\n\t" \
"ds_read_b128 v[76:79], v10 offset:3072\n\t" \
"s_waitcnt lgkmcnt(3)\n\t" \
"v_pk_add_f32 v[80:81], v[80:81], v[64:65]\n\t" \
"v_pk_add_f32 v[82:83], v[82:83], v[66:67]\n\t" \
"v_cvt_pk_bf16_f32 v96, v80, v81\n\t" \
"v_cvt_pk_bf16_f32 v97, v82, v83\n\t" \
"s_waitcnt lgkmcnt(2)\n\t" \
"v_pk_add_f32 v[84:85], v[84:85], v[68:69]\n\t" \
"v_pk_add_f32 v[86:87], v[86:87], v[70:71]\n\t" \
"v_cvt_pk_bf16_f32 v98, v84, v85\n\t" \
"v_cvt_pk_bf16_f32 v99, v86, v87\n\t" \
"buffer_store_dwordx4 v[96:99], v1, s[28:31], s77 offen offset:0 sc1\n\t" \
"s_waitcnt lgkmcnt(1)\n\t" \
"v_pk_add_f32 v[88:89], v[88:89], v[72:73]\n\t" \
"v_pk_add_f32 v[90:91], v[90:91], v[74:75]\n\t" \
"v_cvt_pk_bf16_f32 v100, v88, v89\n\t" \
"v_cvt_pk_bf16_f32 v101, v90, v91\n\t" \
"s_waitcnt lgkmcnt(0)\n\t" \
"v_pk_add_f32 v[92:93], v[92:93], v[76:77]\n\t" \
"v_pk_add_f32 v[94:95], v[94:95], v[78:79]\n\t" \
"v_cvt_pk_bf16_f32 v102, v92, v93\n\t" \
"v_cvt_pk_bf16_f32 v103, v94, v95\n\t" \
"buffer_store_dwordx4 v[100:103], v1, s[28:31], s77 offen offset:1024 sc1\n\t" \
"v_pk_add_f32 v[16:17], v[80:81], v[82:83]\n\t" \
"v_pk_mul_f32 v[18:19], v[80:81], v[80:81]\n\t" \
"v_pk_fma_f32 v[18:19], v[82:83], v[82:83], v[18:19]\n\t" \
"v_pk_add_f32 v[16:17], v[16:17], v[84:85]\n\t" \
"v_pk_fma_f32 v[18:19], v[84:85], v[84:85], v[18:19]\n\t" \
"v_pk_add_f32 v[16:17], v[16:17], v[86:87]\n\t" \
"v_pk_fma_f32 v[18:19], v[86:87], v[86:87], v[18:19]\n\t" \
"v_pk_add_f32 v[16:17], v[16:17], v[88:89]\n\t" \
"v_pk_fma_f32 v[18:19], v[88:89], v[88:89], v[18:19]\n\t" \
"v_pk_add_f32 v[16:17], v[16:17], v[90:91]\n\t" \
"v_pk_fma_f32 v[18:19], v[90:91], v[90:91], v[18:19]\n\t" \
"v_pk_add_f32 v[16:17], v[16:17], v[92:93]\n\t" \
"v_pk_fma_f32 v[18:19], v[92:93], v[92:93], v[18:19]\n\t" \
"v_pk_add_f32 v[16:17], v[16:17], v[94:95]\n\t" \
"v_pk_fma_f32 v[18:19], v[94:95], v[94:95], v[18:19]\n\t" \
"v_add_f32 v14, v16, v17\n\t" \
"v_add_f32 v15, v18, v19\n\t" \
"v_mov_b32 v16, v14\n\t" \
"v_mov_b32 v17, v15\n\t" \
"s_nop 0\n\t" \
"v_permlane32_swap_b32 v16, v14\n\t" \
"v_permlane32_swap_b32 v17, v15\n\t" \
"v_add_f32 v14, v14, v16\n\t" \
"v_add_f32 v15, v15, v17\n\t" \
"s_mov_b32 exec_hi, 0\n\t" \
"buffer_store_dwordx2 v[14:15], v13, s[28:31], s78 offen sc1\n\t" \
"s_mov_b32 exec_hi, -1\n\t" \
"s_barrier\n\t" \
"s_waitcnt vmcnt(0)\n\t" \
"v_mov_b32 v16, s44\n\t" \
"v_mov_b32 v17, s45\n\t" \
"v_mov_b32 v18, s44\n\t" \
"v_mov_b32 v19, s45\n\t" \
"s_mov_b64 exec, 0xff\n\t" \
"buffer_store_dwordx4 v[16:19], v1, s[28:31], s76 offen sc1\n\t" \
"s_mov_b64 exec, -1\n\t" \
"v_lshlrev_b32 v14, 16, v24\n\t" \
"v_and_b32 v15, 0xffff0000, v24\n\t" \
"v_add_f32 v80, v80, v14\n\t" \
"v_add_f32 v81, v81, v15\n\t" \
"v_lshlrev_b32 v14, 16, v25\n\t" \
"v_and_b32 v15, 0xffff0000, v25\n\t" \
"v_add_f32 v82, v82, v14\n\t" \
"v_add_f32 v83, v83, v15\n\t" \
"v_lshlrev_b32 v14, 16, v26\n\t" \
"v_and_b32 v15, 0xffff0000, v26\n\t" \
"v_add_f32 v84, v84, v14\n\t" \
"v_add_f32 v85, v85, v15\n\t" \
"v_lshlrev_b32 v14, 16, v27\n\t" \
"v_and_b32 v15, 0xffff0000, v27\n\t" \
"v_add_f32 v86, v86, v14\n\t" \
"v_add_f32 v87, v87, v15\n\t" \
"v_lshlrev_b32 v14, 16, v28\n\t" \
"v_and_b32 v15, 0xffff0000, v28\n\t" \
"v_add_f32 v88, v88, v14\n\t" \
"v_add_f32 v89, v89, v15\n\t" \
"v_lshlrev_b32 v14, 16, v29\n\t" \
"v_and_b32 v15, 0xffff0000, v29\n\t" \
"v_add_f32 v90, v90, v14\n\t" \
"v_add_f32 v91, v91, v15\n\t" \
"v_lshlrev_b32 v14, 16, v30\n\t" \
"v_and_b32 v15, 0xffff0000, v30\n\t" \
"v_add_f32 v92, v92, v14\n\t" \
"v_add_f32 v93, v93, v15\n\t" \
"v_lshlrev_b32 v14, 16, v31\n\t" \
"v_and_b32 v15, 0xffff0000, v31\n\t" \
"v_add_f32 v94, v94, v14\n\t" \
"v_add_f32 v95, v95, v15\n\t" \
"s_setprio 0\n\t" \
"s_cmp_ge_u32 s49, 28\n\t" \
"s_cbranch_scc1 Lbnom1_%=\n\t" \
"s_mul_i32 s57, s55, 32832\n\t" \
"v_xor_b32 v12, s57, v8\n\t" \
"v_add_u32 v12, 0x10000, v12\n\t" \
"v_xor_b32 v13, 64, v12\n\t" \
"ds_read_b64_tr_b16 v[96:97], v12 offset:0\n\t" \
"ds_read_b64_tr_b16 v[98:99], v12 offset:1024\n\t" \
"ds_read_b64_tr_b16 v[100:101], v13 offset:0\n\t" \
"ds_read_b64_tr_b16 v[102:103], v13 offset:1024\n\t" \
"ds_read_b64_tr_b16 v[104:105], v12 offset:2048\n\t" \
"ds_read_b64_tr_b16 v[106:107], v12 offset:3072\n\t" \
"ds_read_b64_tr_b16 v[108:109], v13 offset:2048\n\t" \
"ds_read_b64_tr_b16 v[110:111], v13 offset:3072\n\t" \
"ds_read_b64_tr_b16 v[112:113], v12 offset:4096\n\t" \
"ds_read_b64_tr_b16 v[114:115], v12 offset:5120\n\t" \
"ds_read_b64_tr_b16 v[116:117], v13 offset:4096\n\t" \
"ds_read_b64_tr_b16 v[118:119], v13 offset:5120\n\t" \
"v_lshrrev_b32 v14, 5, v0\n\t" \
"v_lshlrev_b32 v14, 4, v14\n\t" \
"v_add_u32 v7, 147456, v14\n\t" \
"v_mov_b32 v10, 0xc0135761\n\t" \
"s_mul_i32 s77, s80, 114688\n\t" \
"s_lshl_b32 s57, s52, 12\n\t" \
"s_add_u32 s77, s77, s57\n\t" \
"s_add_u32 s77, s77, 9699328\n\t" \
"s_lshl_b32 s76, s80, 6\n\t" \
"s_lshr_b32 s57, s52, 1\n\t" \
"s_add_u32 s76, s76, s57\n\t" \
"s_lshl_b32 s76, s76, 7\n\t" \
"s_add_u32 s76, s76, 266240\n\t" \
"s_mov_b32 s67, 0xbdd2d3e8\n\t" \
"s_lshl_b32 s57, s80, 11\n\t" \
"s_add_u32 s57, s57, 131072\n\t" \
"s_lshl_b32 s58, s80, 15\n\t" \
"s_add_u32 s58, s58, 7340032\n\t" \
"s_mov_b32 s68, s67\n\t" \
"s_mov_b32 s69, s67\n\t" \
"s_mov_b32 s70, 1.0\n\t" \
"s_mov_b32 s71, 1.0\n\t" \
"s_lshl_b32 s59, s26, 8\n\t" \
"s_add_u32 s59, s59, 149504\n\t" \
"v_and_b32 v52, 15, v0\n\t" \
"v_lshl_add_u32 v52, v52, 4, s59\n\t" \
"v_and_b32 v53, 31, v0\n\t" \
"v_lshl_add_u32 v53, v53, 3, s59\n\t" \
"s_lshl_b32 s59, s80, 12\n\t" \
"s_add_u32 s59, s59, 9437184\n\t" \
"s_barrier\n\t" \
"ds_read_b128 v[64:67], v7 offset:512\n\t" \
"ds_read_b128 v[68:71], v7 offset:544\n\t" \
"ds_read_b128 v[72:75], v7 offset:576\n\t" \
"ds_read_b128 v[76:79], v7 offset:608\n\t" \
"ds_read_b128 v[16:19], v7 offset:640\n\t" \
"ds_read_b128 v[20:23], v7 offset:672\n\t" \
"ds_read_b128 v[24:27], v7 offset:704\n\t" \
"ds_read_b128 v[28:31], v7 offset:736\n\t" \
"s_sleep 32\n\t" \
"v_min_u32 v14, 15, v0\n\t" \
"v_lshlrev_b32 v14, 7, v14\n\t" \
"s_mov_b32 s65, 0x40000\n\t" \
"Lpoll9_%=:\n\t" \
"buffer_load_dwordx2 v[128:129], v14, s[28:31], s57 offen sc1\n\t" \
"s_waitcnt vmcnt(0)\n\t" \
"v_cmp_ne_u64 vcc, s[44:45], v[128:129]\n\t" \
"s_nop 0\n\t" \
"s_cmp_eq_u64 vcc, 0\n\t" \
"s_cbranch_scc1 Lpdone9_%=\n\t" \
"s_sleep 8\n\t" \
"s_sub_u32 s65, s65, 1\n\t" \
"s_cmp_lg_u32 s65, 0\n\t" \
"s_cbranch_scc1 Lpoll9_%=\n\t" \
"Lpdone9_%=:\n\t" \
"s_setprio 2\n\t" \
"s_barrier\n\t" \
"buffer_load_dwordx4 v[32:35], v1, s[28:31], s59 offen offset:0 sc1\n\t" \
"buffer_load_dwordx4 v[36:39], v1, s[28:31], s59 offen offset:1024 sc1\n\t" \
"buffer_load_dwordx4 v[40:43], v1, s[28:31], s59 offen offset:2048 sc1\n\t" \
"buffer_load_dwordx4 v[44:47], v1, s[28:31], s59 offen offset:3072 sc1\n\t" \
"buffer_load_dwordx4 v[128:131], v1, s[28:31], s58 offen offset:0 sc1\n\t" \
"buffer_load_dwordx4 v[132:135], v1, s[28:31], s58 offen offset:1024 sc1\n\t" \
"buffer_load_dwordx4 v[136:139], v1, s[28:31], s58 offen offset:2048 sc1\n\t" \
"buffer_load_dwordx4 v[140:143], v1, s[28:31], s58 offen offset:3072 sc1\n\t" \
"s_add_u32 s58, s58, 4096\n\t" \
"buffer_load_dwordx4 v[144:147], v1, s[28:31], s58 offen offset:0 sc1\n\t" \
"buffer_load_dwordx4 v[148:151], v1, s[28:31], s58 offen offset:1024 sc1\n\t" \
"buffer_load_dwordx4 v[152:155], v1, s[28:31], s58 offen offset:2048 sc1\n\t" \
"buffer_load_dwordx4 v[156:159], v1, s[28:31], s58 offen offset:3072 sc1\n\t" \
"s_add_u32 s58, s58, 4096\n\t" \
"buffer_load_dwordx4 v[160:163], v1, s[28:31], s58 offen offset:0 sc1\n\t" \
"buffer_load_dwordx4 v[164:167], v1, s[28:31], s58 offen offset:1024 sc1\n\t" \
"s_waitcnt vmcnt(10)\n\t" \
"v_add_f32 v32, v32, v36\n\t" \
"v_add_f32 v33, v33, v37\n\t" \
"v_add_f32 v34, v34, v38\n\t" \
"v_add_f32 v35, v35, v39\n\t" \
"v_add_f32 v32, v32, v40\n\t" \
"v_add_f32 v33, v33, v41\n\t" \
"v_add_f32 v34, v34, v42\n\t" \
"v_add_f32 v35, v35, v43\n\t" \
"v_add_f32 v32, v32, v44\n\t" \
"v_add_f32 v33, v33, v45\n\t" \
"v_add_f32 v34, v34, v46\n\t" \
"v_add_f32 v35, v35, v47\n\t" \
"v_mov_b32 v48, v32\n\t" \
"v_mov_b32 v49, v33\n\t" \
"v_mov_b32 v50, v34\n\t" \
"v_mov_b32 v51, v35\n\t" \
"v_permlane16_swap_b32 v48, v32\n\t" \
"v_permlane16_swap_b32 v49, v33\n\t" \
"v_permlane16_swap_b32 v50, v34\n\t" \
"v_permlane16_swap_b32 v51, v35\n\t" \
"v_add_f32 v32, v32, v48\n\t" \
"v_add_f32 v33, v33, v49\n\t" \
"v_add_f32 v34, v34, v50\n\t" \
"v_add_f32 v35, v35, v51\n\t" \
"v_mov_b32 v48, v32\n\t" \
"v_mov_b32 v49, v33\n\t" \
"v_mov_b32 v50, v34\n\t" \
"v_mov_b32 v51, v35\n\t" \
"v_permlane32_swap_b32 v48, v32\n\t" \
"v_permlane32_swap_b32 v49, v33\n\t" \
"v_permlane32_swap_b32 v50, v34\n\t" \
"v_permlane32_swap_b32 v51, v35\n\t" \
"v_add_f32 v32, v32, v48\n\t" \
"v_add_f32 v33, v33, v49\n\t" \
"v_add_f32 v34, v34, v50\n\t" \
"v_add_f32 v35, v35, v51\n\t" \
"ds_write_b128 v52, v[32:35]\n\t" \
"ds_read_b64 v[36:37], v53\n\t" \
"s_waitcnt lgkmcnt(0)\n\t" \
"v_mul_f32 v36, 0x3b000000, v36\n\t" \
"v_mul_f32 v37, 0x3b000000, v37\n\t" \
"v_fma_f32 v37, -v36, v36, v37\n\t" \
"v_max_f32 v37, 0, v37\n\t" \
"v_add_f32 v38, 0x3727c5ac, v37\n\t" \
"v_rsq_f32 v38, v38\n\t" \
"s_nop 0\n\t" \
"v_mul_f32 v37, v37, v38\n\t" \
"v_mul_f32 v37, v37, v38\n\t" \
"v_add_f32 v37, 0x3727c5ac, v37\n\t" \
"v_rsq_f32 v37, v37\n\t" \
"s_nop 0\n\t" \
"v_mul_f32 v8, v37, v38\n\t" \
"v_mul_f32 v9, v36, v8\n\t" \
"v_sub_f32 v9, 0, v9\n\t" \
"s_waitcnt vmcnt(9)\n\t" \
"v_mfma_f32_32x32x16_bf16 v[32:47], v[96:99], v[128:131], 0\n\t" \
"ds_read_b64_tr_b16 v[120:121], v12 offset:6144\n\t" \
"ds_read_b64_tr_b16 v[122:123], v12 offset:7168\n\t" \
"v_mfma_f32_32x32x16_bf16 v[48:63], v[100:103], v[128:131], 0\n\t" \
"ds_read_b64_tr_b16 v[124:125], v13 offset:6144\n\t" \
"ds_read_b64_tr_b16 v[126:127], v13 offset:7168\n\t" \
"buffer_load_dwordx4 v[168:171], v1, s[28:31], s58 offen offset:2048 sc1\n\t" \
"s_waitcnt vmcnt(9)\n\t" \
"v_mfma_f32_32x32x16_bf16 v[32:47], v[104:107], v[132:135], v[32:47]\n\t" \
"ds_read_b64_tr_b16 v[96:97], v12 offset:8192\n\t" \
"ds_read_b64_tr_b16 v[98:99], v12 offset:9216\n\t" \
"ds_read_b128 v[172:175], v7 offset:0\n\t" \
"v_mfma_f32_32x32x16_bf16 v[48:63], v[108:111], v[132:135], v[48:63]\n\t" \
"ds_read_b64_tr_b16 v[100:101], v13 offset:8192\n\t" \
"ds_read_b64_tr_b16 v[102:103], v13 offset:9216\n\t" \
"buffer_load_dwordx4 v[128:131], v1, s[28:31], s58 offen offset:3072 sc1\n\t" \
"s_waitcnt vmcnt(9)\n\t" \
"v_mfma_f32_32x32x16_bf16 v[32:47], v[112:115], v[136:139], v[32:47]\n\t" \
"ds_read_b64_tr_b16 v[104:105], v12 offset:10240\n\t" \
"ds_read_b64_tr_b16 v[106:107], v12 offset:11264\n\t" \
"v_mfma_f32_32x32x16_bf16 v[48:63], v[116:119], v[136:139], v[48:63]\n\t" \
"ds_read_b64_tr_b16 v[108:109], v13 offset:10240\n\t" \
"ds_read_b64_tr_b16 v[110:111], v13 offset:11264\n\t" \
"s_add_u32 s58, s58, 4096\n\t" \
"buffer_load_dwordx4 v[132:135], v1, s[28:31], s58 offen offset:0 sc1\n\t" \
"s_waitcnt lgkmcnt(9) vmcnt(9)\n\t" \
"v_mfma_f32_32x32x16_bf16 v[32:47], v[120:123], v[140:143], v[32:47]\n\t" \
"ds_read_b64_tr_b16 v[112:113], v12 offset:12288\n\t" \
"ds_read_b64_tr_b16 v[114:115], v12 offset:13312\n\t" \
"s_waitcnt lgkmcnt(8)\n\t" \
"v_fmac_f32 v64, v9, v172\n\t" \
"v_fmac_f32 v65, v9, v173\n\t" \
"v_mfma_f32_32x32x16_bf16 v[48:63], v[124:127], v[140:143], v[48:63]\n\t" \
"ds_read_b64_tr_b16 v[116:117], v13 offset:12288\n\t" \
"ds_read_b64_tr_b16 v[118:119], v13 offset:13312\n\t" \
"buffer_load_dwordx4 v[136:139], v1, s[28:31], s58 offen offset:1024 sc1\n\t" \
"v_fmac_f32 v66, v9, v174\n\t" \
"v_fmac_f32 v67, v9, v175\n\t" \
"s_waitcnt lgkmcnt(8) vmcnt(9)\n\t" \
"v_mfma_f32_32x32x16_bf16 v[32:47], v[96:99], v[144:147], v[32:47]\n\t" \
"ds_read_b64_tr_b16 v[120:121], v12 offset:14336\n\t" \
"ds_read_b64_tr_b16 v[122:123], v12 offset:15360\n\t" \
"v_mfma_f32_32x32x16_bf16 v[48:63], v[100:103], v[144:147], v[48:63]\n\t" \
"ds_read_b64_tr_b16 v[124:125], v13 offset:14336\n\t" \
"ds_read_b64_tr_b16 v[126:127], v13 offset:15360\n\t" \
"buffer_load_dwordx4 v[140:143], v1, s[28:31], s58 offen offset:2048 sc1\n\t" \
"ds_read_b128 v[172:175], v7 offset:32\n\t" \
"s_waitcnt lgkmcnt(9) vmcnt(9)\n\t" \
"v_mfma_f32_32x32x16_bf16 v[32:47], v[104:107], v[148:151], v[32:47]\n\t" \
"ds_read_b64_tr_b16 v[96:97], v12 offset:16384\n\t" \
"ds_read_b64_tr_b16 v[98:99], v12 offset:17408\n\t" \
"v_mfma_f32_32x32x16_bf16 v[48:63], v[108:111], v[148:151], v[48:63]\n\t" \
"ds_read_b64_tr_b16 v[100:101], v13 offset:16384\n\t" \
"ds_read_b64_tr_b16 v[102:103], v13 offset:17408\n\t" \
"buffer_load_dwordx4 v[144:147], v1, s[28:31], s58 offen offset:3072 sc1\n\t" \
"s_waitcnt lgkmcnt(9) vmcnt(9)\n\t" \
"v_mfma_f32_32x32x16_bf16 v[32:47], v[112:115], v[152:155], v[32:47]\n\t" \
"ds_read_b64_tr_b16 v[104:105], v12 offset:18432\n\t" \
"ds_read_b64_tr_b16 v[106:107], v12 offset:19456\n\t" \
"v_mfma_f32_32x32x16_bf16 v[48:63], v[116:119], v[152:155], v[48:63]\n\t" \
"ds_read_b64_tr_b16 v[108:109], v13 offset:18432\n\t" \
"ds_read_b64_tr_b16 v[110:111], v13 offset:19456\n\t" \
"s_add_u32 s58, s58, 4096\n\t" \
"buffer_load_dwordx4 v[148:151], v1, s[28:31], s58 offen offset:0 sc1\n\t" \
"s_waitcnt lgkmcnt(8)\n\t" \
"v_fmac_f32 v68, v9, v172\n\t" \
"v_fmac_f32 v69, v9, v173\n\t" \
"s_waitcnt vmcnt(9)\n\t" \
"v_mfma_f32_32x32x16_bf16 v[32:47], v[120:123], v[156:159], v[32:47]\n\t" \
"ds_read_b64_tr_b16 v[112:113], v12 offset:20480\n\t" \
"ds_read_b64_tr_b16 v[114:115], v12 offset:21504\n\t" \
"v_mfma_f32_32x32x16_bf16 v[48:63], v[124:127], v[156:159], v[48:63]\n\t" \
"ds_read_b64_tr_b16 v[116:117], v13 offset:20480\n\t" \
"ds_read_b64_tr_b16 v[118:119], v13 offset:21504\n\t" \
"buffer_load_dwordx4 v[152:155], v1, s[28:31], s58 offen offset:1024 sc1\n\t" \
"v_fmac_f32 v70, v9, v174\n\t" \
"v_fmac_f32 v71, v9, v175\n\t" \
"s_waitcnt lgkmcnt(8) vmcnt(9)\n\t" \
"v_mfma_f32_32x32x16_bf16 v[32:47], v[96:99], v[160:163], v[32:47]\n\t" \
"ds_read_b64_tr_b16 v[120:121], v12 offset:22528\n\t" \
"ds_read_b64_tr_b16 v[122:123], v12 offset:23552\n\t" \
"ds_read_b128 v[172:175], v7 offset:64\n\t" \
"v_mfma_f32_32x32x16_bf16 v[48:63], v[100:103], v[160:163], v[48:63]\n\t" \
"ds_read_b64_tr_b16 v[124:125], v13 offset:22528\n\t" \
"ds_read_b64_tr_b16 v[126:127], v13 offset:23552\n\t" \
"buffer_load_dwordx4 v[156:159], v1, s[28:31], s58 offen offset:2048 sc1\n\t" \
"s_waitcnt lgkmcnt(9) vmcnt(9)\n\t" \
"v_mfma_f32_32x32x16_bf16 v[32:47], v[104:107], v[164:167], v[32:47]\n\t" \
"ds_read_b64_tr_b16 v[96:97], v12 offset:24576\n\t" \
"ds_read_b64_tr_b16 v[98:99], v12 offset:25600\n\t" \
"v_mfma_f32_32x32x16_bf16 v[48:63], v[108:111], v[164:167], v[48:63]\n\t" \
"ds_read_b64_tr_b16 v[100:101], v13 offset:24576\n\t" \
"ds_read_b64_tr_b16 v[102:103], v13 offset:25600\n\t" \
"buffer_load_dwordx4 v[160:163], v1, s[28:31], s58 offen offset:3072 sc1\n\t" \
"s_waitcnt lgkmcnt(9) vmcnt(9)\n\t" \
"v_mfma_f32_32x32x16_bf16 v[32:47], v[112:115], v[168:171], v[32:47]\n\t" \
"ds_read_b64_tr_b16 v[104:105], v12 offset:26624\n\t" \
"ds_read_b64_tr_b16 v[106:107], v12 offset:27648\n\t" \
"s_waitcnt lgkmcnt(8)\n\t" \
"v_fmac_f32 v72, v9, v172\n\t" \
"v_fmac_f32 v73, v9, v173\n\t" \
"v_mfma_f32_32x32x16_bf16 v[48:63], v[116:119], v[168:171], v[48:63]\n\t" \
"ds_read_b64_tr_b16 v[108:109], v13 offset:26624\n\t" \
"ds_read_b64_tr_b16 v[110:111], v13 offset:27648\n\t" \
"s_add_u32 s58, s58, 4096\n\t" \
"buffer_load_dwordx4 v[164:167], v1, s[28:31], s58 offen offset:0 sc1\n\t" \
"s_waitcnt lgkmcnt(8) vmcnt(9)\n\t" \
"v_mfma_f32_32x32x16_bf16 v[32:47], v[120:123], v[128:131], v[32:47]\n\t" \
"ds_read_b64_tr_b16 v[112:113], v12 offset:28672\n\t" \
"ds_read_b64_tr_b16 v[114:115], v12 offset:29696\n\t" \
"v_fmac_f32 v74, v9, v174\n\t" \
"v_fmac_f32 v75, v9, v175\n\t" \
"v_mfma_f32_32x32x16_bf16 v[48:63], v[124:127], v[128:131], v[48:63]\n\t" \
"ds_read_b64_tr_b16 v[116:117], v13 offset:28672\n\t" \
"ds_read_b64_tr_b16 v[118:119], v13 offset:29696\n\t" \
"buffer_load_dwordx4 v[168:171], v1, s[28:31], s58 offen offset:1024 sc1\n\t" \
"ds_read_b128 v[172:175], v7 offset:96\n\t" \
"s_waitcnt lgkmcnt(9) vmcnt(9)\n\t" \
"v_mfma_f32_32x32x16_bf16 v[32:47], v[96:99], v[132:135], v[32:47]\n\t" \
"ds_read_b64_tr_b16 v[120:121], v12 offset:30720\n\t" \
"ds_read_b64_tr_b16 v[122:123], v12 offset:31744\n\t" \
"v_mfma_f32_32x32x16_bf16 v[48:63], v[100:103], v[132:135], v[48:63]\n\t" \
"ds_read_b64_tr_b16 v[124:125], v13 offset:30720\n\t" \
"ds_read_b64_tr_b16 v[126:127], v13 offset:31744\n\t" \
"buffer_load_dwordx4 v[128:131], v1, s[28:31], s58 offen offset:2048 sc1\n\t" \
"s_waitcnt lgkmcnt(9) vmcnt(9)\n\t" \
"v_mfma_f32_32x32x16_bf16 v[32:47], v[104:107], v[136:139], v[32:47]\n\t" \
"ds_read_b64_tr_b16 v[96:97], v12 offset:32768\n\t" \
"ds_read_b64_tr_b16 v[98:99], v12 offset:33792\n\t" \
"v_mfma_f32_32x32x16_bf16 v[48:63], v[108:111], v[136:139], v[48:63]\n\t" \
"ds_read_b64_tr_b16 v[100:101], v13 offset:32768\n\t" \
"ds_read_b64_tr_b16 v[102:103], v13 offset:33792\n\t" \
"buffer_load_dwordx4 v[132:135], v1, s[28:31], s58 offen offset:3072 sc1\n\t" \
"s_waitcnt lgkmcnt(9) vmcnt(9)\n\t" \
"v_mfma_f32_32x32x16_bf16 v[32:47], v[112:115], v[140:143], v[32:47]\n\t" \
"ds_read_b64_tr_b16 v[104:105], v12 offset:34816\n\t" \
"ds_read_b64_tr_b16 v[106:107], v12 offset:35840\n\t" \
"s_waitcnt lgkmcnt(10)\n\t" \
"v_fmac_f32 v76, v9, v172\n\t" \
"v_fmac_f32 v77, v9, v173\n\t" \
"v_mfma_f32_32x32x16_bf16 v[48:63], v[116:119], v[140:143], v[48:63]\n\t" \
"ds_read_b64_tr_b16 v[108:109], v13 offset:34816\n\t" \
"ds_read_b64_tr_b16 v[110:111], v13 offset:35840\n\t" \
"s_add_u32 s58, s58, 4096\n\t" \
"buffer_load_dwordx4 v[136:139], v1, s[28:31], s58 offen offset:0 sc1\n\t" \
"v_fmac_f32 v78, v9, v174\n\t" \
"v_fmac_f32 v79, v9, v175\n\t" \
"s_waitcnt lgkmcnt(8) vmcnt(9)\n\t" \
"v_mfma_f32_32x32x16_bf16 v[32:47], v[120:123], v[144:147], v[32:47]\n\t" \
"ds_read_b64_tr_b16 v[112:113], v12 offset:36864\n\t" \
"ds_read_b64_tr_b16 v[114:115], v12 offset:37888\n\t" \
"v_mfma_f32_32x32x16_bf16 v[48:63], v[124:127], v[144:147], v[48:63]\n\t" \
"ds_read_b64_tr_b16 v[116:117], v13 offset:36864\n\t" \
"ds_read_b64_tr_b16 v[118:119], v13 offset:37888\n\t" \
"buffer_load_dwordx4 v[140:143], v1, s[28:31], s58 offen offset:1024 sc1\n\t" \
"ds_read_b128 v[172:175], v7 offset:128\n\t" \
"s_waitcnt lgkmcnt(9) vmcnt(9)\n\t" \
"v_mfma_f32_32x32x16_bf16 v[32:47], v[96:99], v[148:151], v[32:47]\n\t" \
"ds_read_b64_tr_b16 v[120:121], v12 offset:38912\n\t" \
"ds_read_b64_tr_b16 v[122:123], v12 offset:39936\n\t" \
"v_mfma_f32_32x32x16_bf16 v[48:63], v[100:103], v[148:151], v[48:63]\n\t" \
"ds_read_b64_tr_b16 v[124:125], v13 offset:38912\n\t" \
"ds_read_b64_tr_b16 v[126:127], v13 offset:39936\n\t" \
"buffer_load_dwordx4 v[144:147], v1, s[28:31], s58 offen offset:2048 sc1\n\t" \
"s_waitcnt lgkmcnt(9) vmcnt(9)\n\t" \
"v_mfma_f32_32x32x16_bf16 v[32:47], v[104:107], v[152:155], v[32:47]\n\t" \
"ds_read_b64_tr_b16 v[96:97], v12 offset:40960\n\t" \
"ds_read_b64_tr_b16 v[98:99], v12 offset:41984\n\t" \
"v_mfma_f32_32x32x16_bf16 v[48:63], v[108:111], v[152:155], v[48:63]\n\t" \
"ds_read_b64_tr_b16 v[100:101], v13 offset:40960\n\t" \
"ds_read_b64_tr_b16 v[102:103], v13 offset:41984\n\t" \
"buffer_load_dwordx4 v[148:151], v1, s[28:31], s58 offen offset:3072 sc1\n\t" \
"s_waitcnt lgkmcnt(8)\n\t" \
"v_fmac_f32 v16, v9, v172\n\t" \
"v_fmac_f32 v17, v9, v173\n\t" \
"s_waitcnt vmcnt(9)\n\t" \
"v_mfma_f32_32x32x16_bf16 v[32:47], v[112:115], v[156:159], v[32:47]\n\t" \
"ds_read_b64_tr_b16 v[104:105], v12 offset:43008\n\t" \
"ds_read_b64_tr_b16 v[106:107], v12 offset:44032\n\t" \
"v_fmac_f32 v18, v9, v174\n\t" \
"v_fmac_f32 v19, v9, v175\n\t" \
"v_mfma_f32_32x32x16_bf16 v[48:63], v[116:119], v[156:159], v[48:63]\n\t" \
"ds_read_b64_tr_b16 v[108:109], v13 offset:43008\n\t" \
"ds_read_b64_tr_b16 v[110:111], v13 offset:44032\n\t" \
"s_add_u32 s58, s58, 4096\n\t" \
"buffer_load_dwordx4 v[152:155], v1, s[28:31], s58 offen offset:0 sc1\n\t" \
"s_waitcnt lgkmcnt(8) vmcnt(9)\n\t" \
"v_mfma_f32_32x32x16_bf16 v[32:47], v[120:123], v[160:163], v[32:47]\n\t" \
"ds_read_b64_tr_b16 v[112:113], v12 offset:45056\n\t" \
"ds_read_b64_tr_b16 v[114:115], v12 offset:46080\n\t" \
"ds_read_b128 v[172:175], v7 offset:160\n\t" \
"v_mfma_f32_32x32x16_bf16 v[48:63], v[124:127], v[160:163], v[48:63]\n\t" \
"ds_read_b64_tr_b16 v[116:117], v13 offset:45056\n\t" \
"ds_read_b64_tr_b16 v[118:119], v13 offset:46080\n\t" \
"buffer_load_dwordx4 v[156:159], v1, s[28:31], s58 offen offset:1024 sc1\n\t" \
"s_waitcnt lgkmcnt(9) vmcnt(9)\n\t" \
"v_mfma_f32_32x32x16_bf16 v[32:47], v[96:99], v[164:167], v[32:47]\n\t" \
"ds_read_b64_tr_b16 v[120:121], v12 offset:47104\n\t" \
"ds_read_b64_tr_b16 v[122:123], v12 offset:48128\n\t" \
"v_mfma_f32_32x32x16_bf16 v[48:63], v[100:103], v[164:167], v[48:63]\n\t" \
"ds_read_b64_tr_b16 v[124:125], v13 offset:47104\n\t" \
"ds_read_b64_tr_b16 v[126:127], v13 offset:48128\n\t" \
"buffer_load_dwordx4 v[160:163], v1, s[28:31], s58 offen offset:2048 sc1\n\t" \
"s_waitcnt lgkmcnt(9) vmcnt(9)\n\t" \
"v_mfma_f32_32x32x16_bf16 v[32:47], v[104:107], v[168:171], v[32:47]\n\t" \
"ds_read_b64_tr_b16 v[96:97], v12 offset:49152\n\t" \
"ds_read_b64_tr_b16 v[98:99], v12 offset:50176\n\t" \
"s_waitcnt lgkmcnt(8)\n\t" \
"v_fmac_f32 v20, v9, v172\n\t" \
"v_fmac_f32 v21, v9, v173\n\t" \
"v_mfma_f32_32x32x16_bf16 v[48:63], v[108:111], v[168:171], v[48:63]\n\t" \
"ds_read_b64_tr_b16 v[100:101], v13 offset:49152\n\t" \
"ds_read_b64_tr_b16 v[102:103], v13 offset:50176\n\t" \
"buffer_load_dwordx4 v[164:167], v1, s[28:31], s58 offen offset:3072 sc1\n\t" \
"s_waitcnt lgkmcnt(8) vmcnt(9)\n\t" \
"v_mfma_f32_32x32x16_bf16 v[32:47], v[112:115], v[128:131], v[32:47]\n\t" \
"ds_read_b64_tr_b16 v[104:105], v12 offset:51200\n\t" \
"ds_read_b64_tr_b16 v[106:107], v12 offset:52224\n\t" \
"v_fmac_f32 v22, v9, v174\n\t" \
"v_fmac_f32 v23, v9, v175\n\t" \
"v_mfma_f32_32x32x16_bf16 v[48:63], v[116:119], v[128:131], v[48:63]\n\t" \
"ds_read_b64_tr_b16 v[108:109], v13 offset:51200\n\t" \
"ds_read_b64_tr_b16 v[110:111], v13 offset:52224\n\t" \
"ds_read_b128 v[172:175], v7 offset:192\n\t" \
"s_waitcnt lgkmcnt(9) vmcnt(8)\n\t" \
"v_mfma_f32_32x32x16_bf16 v[32:47], v[120:123], v[132:135], v[32:47]\n\t" \
"ds_read_b64_tr_b16 v[112:113], v12 offset:53248\n\t" \
"ds_read_b64_tr_b16 v[114:115], v12 offset:54272\n\t" \
"v_mfma_f32_32x32x16_bf16 v[48:63], v[124:127], v[132:135], v[48:63]\n\t" \
"ds_read_b64_tr_b16 v[116:117], v13 offset:53248\n\t" \
"ds_read_b64_tr_b16 v[118:119], v13 offset:54272\n\t" \
"s_waitcnt lgkmcnt(9) vmcnt(7)\n\t" \
"v_mfma_f32_32x32x16_bf16 v[32:47], v[96:99], v[136:139], v[32:47]\n\t" \
"ds_read_b64_tr_b16 v[120:121], v12 offset:55296\n\t" \
"ds_read_b64_tr_b16 v[122:123], v12 offset:56320\n\t" \
"v_mfma_f32_32x32x16_bf16 v[48:63], v[100:103], v[136:139], v[48:63]\n\t" \
"ds_read_b64_tr_b16 v[124:125], v13 offset:55296\n\t" \
"ds_read_b64_tr_b16 v[126:127], v13 offset:56320\n\t" \
"s_waitcnt lgkmcnt(8)\n\t" \
"v_fmac_f32 v24, v9, v172\n\t" \
"v_fmac_f32 v25, v9, v173\n\t" \
"s_waitcnt vmcnt(6)\n\t" \
"v_mfma_f32_32x32x16_bf16 v[32:47], v[104:107], v[140:143], v[32:47]\n\t" \
"ds_read_b64_tr_b16 v[96:97], v12 offset:57344\n\t" \
"ds_read_b64_tr_b16 v[98:99], v12 offset:58368\n\t" \
"v_mfma_f32_32x32x16_bf16 v[48:63], v[108:111], v[140:143], v[48:63]\n\t" \
"ds_read_b64_tr_b16 v[100:101], v13 offset:57344\n\t" \
"ds_read_b64_tr_b16 v[102:103], v13 offset:58368\n\t" \
"v_fmac_f32 v26, v9, v174\n\t" \
"v_fmac_f32 v27, v9, v175\n\t" \
"s_waitcnt lgkmcnt(8) vmcnt(5)\n\t" \
"v_mfma_f32_32x32x16_bf16 v[32:47], v[112:115], v[144:147], v[32:47]\n\t" \
"ds_read_b64_tr_b16 v[104:105], v12 offset:59392\n\t" \
"ds_read_b64_tr_b16 v[106:107], v12 offset:60416\n\t" \
"ds_read_b128 v[172:175], v7 offset:224\n\t" \
"v_mfma_f32_32x32x16_bf16 v[48:63], v[116:119], v[144:147], v[48:63]\n\t" \
"ds_read_b64_tr_b16 v[108:109], v13 offset:59392\n\t" \
"ds_read_b64_tr_b16 v[110:111], v13 offset:60416\n\t" \
"s_waitcnt lgkmcnt(9) vmcnt(4)\n\t" \
"v_mfma_f32_32x32x16_bf16 v[32:47], v[120:123], v[148:151], v[32:47]\n\t" \
"ds_read_b64_tr_b16 v[112:113], v12 offset:61440\n\t" \
"ds_read_b64_tr_b16 v[114:115], v12 offset:62464\n\t" \
"v_mfma_f32_32x32x16_bf16 v[48:63], v[124:127], v[148:151], v[48:63]\n\t" \
"ds_read_b64_tr_b16 v[116:117], v13 offset:61440\n\t" \
"ds_read_b64_tr_b16 v[118:119], v13 offset:62464\n\t" \
"s_waitcnt lgkmcnt(9) vmcnt(3)\n\t" \
"v_mfma_f32_32x32x16_bf16 v[32:47], v[96:99], v[152:155], v[32:47]\n\t" \
"ds_read_b64_tr_b16 v[120:121], v12 offset:63488\n\t" \
"ds_read_b64_tr_b16 v[122:123], v12 offset:64512\n\t" \
"v_mfma_f32_32x32x16_bf16 v[48:63], v[100:103], v[152:155], v[48:63]\n\t" \
"ds_read_b64_tr_b16 v[124:125], v13 offset:63488\n\t" \
"ds_read_b64_tr_b16 v[126:127], v13 offset:64512\n\t" \
"s_waitcnt lgkmcnt(10)\n\t" \
"v_fmac_f32 v28, v9, v172\n\t" \
"v_fmac_f32 v29, v9, v173\n\t" \
"s_waitcnt lgkmcnt(8) vmcnt(2)\n\t" \
"v_mfma_f32_32x32x16_bf16 v[32:47], v[104:107], v[156:159], v[32:47]\n\t" \
"v_fmac_f32 v30, v9, v174\n\t" \
"v_fmac_f32 v31, v9, v175\n\t" \
"v_mfma_f32_32x32x16_bf16 v[48:63], v[108:111], v[156:159], v[48:63]\n\t" \
"s_waitcnt lgkmcnt(4) vmcnt(1)\n\t" \
"v_mfma_f32_32x32x16_bf16 v[32:47], v[112:115], v[160:163], v[32:47]\n\t" \
"v_mfma_f32_32x32x16_bf16 v[48:63], v[116:119], v[160:163], v[48:63]\n\t" \
"s_waitcnt lgkmcnt(0) vmcnt(0)\n\t" \
"v_mfma_f32_32x32x16_bf16 v[32:47], v[120:123], v[164:167], v[32:47]\n\t" \
"v_mfma_f32_32x32x16_bf16 v[48:63], v[124:127], v[164:167], v[48:63]\n\t" \
"s_barrier\n\t" \
"s_nop 9\n\t" \
"v_pk_fma_f32 v[64:65], v[8:9], v[32:33], v[64:65] op_sel_hi:[0,1,1]\n\t" \
"v_pk_fma_f32 v[66:67], v[8:9], v[34:35], v[66:67] op_sel_hi:[0,1,1]\n\t" \
"v_pk_fma_f32 v[68:69], v[8:9], v[36:37], v[68:69] op_sel_hi:[0,1,1]\n\t" \
"v_pk_fma_f32 v[70:71], v[8:9], v[38:39], v[70:71] op_sel_hi:[0,1,1]\n\t" \
"v_pk_mul_f32 v[32:33], v[64:65], v[64:65]\n\t" \
"v_pk_mul_f32 v[34:35], v[66:67], v[66:67]\n\t" \
"v_pk_mul_f32 v[36:37], v[68:69], v[68:69]\n\t" \
"v_pk_mul_f32 v[38:39], v[70:71], v[70:71]\n\t" \
"v_pk_fma_f32 v[32:33], v[32:33], s[68:69], v[10:11] op_sel_hi:[1,1,0]\n\t" \
"v_pk_fma_f32 v[34:35], v[34:35], s[68:69], v[10:11] op_sel_hi:[1,1,0]\n\t" \
"v_pk_fma_f32 v[36:37], v[36:37], s[68:69], v[10:11] op_sel_hi:[1,1,0]\n\t" \
"v_pk_fma_f32 v[38:39], v[38:39], s[68:69], v[10:11] op_sel_hi:[1,1,0]\n\t" \
"v_pk_mul_f32 v[32:33], v[32:33], v[64:65]\n\t" \
"v_pk_mul_f32 v[34:35], v[34:35], v[66:67]\n\t" \
"v_pk_mul_f32 v[36:37], v[36:37], v[68:69]\n\t" \
"v_pk_mul_f32 v[38:39], v[38:39], v[70:71]\n\t" \
"v_exp_f32 v32, v32\n\t" \
"v_exp_f32 v33, v33\n\t" \
"v_exp_f32 v34, v34\n\t" \
"v_exp_f32 v35, v35\n\t" \
"v_exp_f32 v36, v36\n\t" \
"v_exp_f32 v37, v37\n\t" \
"v_exp_f32 v38, v38\n\t" \
"v_exp_f32 v39, v39\n\t" \
"v_pk_add_f32 v[32:33], v[32:33], s[70:71]\n\t" \
"v_pk_add_f32 v[34:35], v[34:35], s[70:71]\n\t" \
"v_pk_add_f32 v[36:37], v[36:37], s[70:71]\n\t" \
"v_pk_add_f32 v[38:39], v[38:39], s[70:71]\n\t" \
"v_rcp_f32 v32, v32\n\t" \
"v_rcp_f32 v33, v33\n\t" \
"v_rcp_f32 v34, v34\n\t" \
"v_rcp_f32 v35, v35\n\t" \
"v_rcp_f32 v36, v36\n\t" \
"v_rcp_f32 v37, v37\n\t" \
"v_rcp_f32 v38, v38\n\t" \
"v_rcp_f32 v39, v39\n\t" \
"v_pk_mul_f32 v[64:65], v[64:65], v[32:33]\n\t" \
"v_pk_mul_f32 v[66:67], v[66:67], v[34:35]\n\t" \
"v_pk_mul_f32 v[68:69], v[68:69], v[36:37]\n\t" \
"v_pk_mul_f32 v[70:71], v[70:71], v[38:39]\n\t" \
"v_cvt_pk_bf16_f32 v32, v64, v65\n\t" \
"v_cvt_pk_bf16_f32 v33, v66, v67\n\t" \
"v_cvt_pk_bf16_f32 v34, v68, v69\n\t" \
"v_cvt_pk_bf16_f32 v35, v70, v71\n\t" \
"buffer_store_dwordx4 v[32:35], v1, s[28:31], s77 offen offset:0 sc1\n\t" \
"v_pk_fma_f32 v[72:73], v[8:9], v[40:41], v[72:73] op_sel_hi:[0,1,1]\n\t" \
"v_pk_fma_f32 v[74:75], v[8:9], v[42:43], v[74:75] op_sel_hi:[0,1,1]\n\t" \
"v_pk_fma_f32 v[76:77], v[8:9], v[44:45], v[76:77] op_sel_hi:[0,1,1]\n\t" \
"v_pk_fma_f32 v[78:79], v[8:9], v[46:47], v[78:79] op_sel_hi:[0,1,1]\n\t" \
"v_pk_mul_f32 v[40:41], v[72:73], v[72:73]\n\t" \
"v_pk_mul_f32 v[42:43], v[74:75], v[74:75]\n\t" \
"v_pk_mul_f32 v[44:45], v[76:77], v[76:77]\n\t" \
"v_pk_mul_f32 v[46:47], v[78:79], v[78:79]\n\t" \
"v_pk_fma_f32 v[40:41], v[40:41], s[68:69], v[10:11] op_sel_hi:[1,1,0]\n\t" \
"v_pk_fma_f32 v[42:43], v[42:43], s[68:69], v[10:11] op_sel_hi:[1,1,0]\n\t" \
"v_pk_fma_f32 v[44:45], v[44:45], s[68:69], v[10:11] op_sel_hi:[1,1,0]\n\t" \
"v_pk_fma_f32 v[46:47], v[46:47], s[68:69], v[10:11] op_sel_hi:[1,1,0]\n\t" \
"v_pk_mul_f32 v[40:41], v[40:41], v[72:73]\n\t" \
"v_pk_mul_f32 v[42:43], v[42:43], v[74:75]\n\t" \
"v_pk_mul_f32 v[44:45], v[44:45], v[76:77]\n\t" \
"v_pk_mul_f32 v[46:47], v[46:47], v[78:79]\n\t" \
"v_exp_f32 v40, v40\n\t" \
"v_exp_f32 v41, v41\n\t" \
"v_exp_f32 v42, v42\n\t" \
"v_exp_f32 v43, v43\n\t" \
"v_exp_f32 v44, v44\n\t" \
"v_exp_f32 v45, v45\n\t" \
"v_exp_f32 v46, v46\n\t" \
"v_exp_f32 v47, v47\n\t" \
"v_pk_add_f32 v[40:41], v[40:41], s[70:71]\n\t" \
"v_pk_add_f32 v[42:43], v[42:43], s[70:71]\n\t" \
"v_pk_add_f32 v[44:45], v[44:45], s[70:71]\n\t" \
"v_pk_add_f32 v[46:47], v[46:47], s[70:71]\n\t" \
"v_rcp_f32 v40, v40\n\t" \
"v_rcp_f32 v41, v41\n\t" \
"v_rcp_f32 v42, v42\n\t" \
"v_rcp_f32 v43, v43\n\t" \
"v_rcp_f32 v44, v44\n\t" \
"v_rcp_f32 v45, v45\n\t" \
"v_rcp_f32 v46, v46\n\t" \
"v_rcp_f32 v47, v47\n\t" \
"v_pk_mul_f32 v[72:73], v[72:73], v[40:41]\n\t" \
"v_pk_mul_f32 v[74:75], v[74:75], v[42:43]\n\t" \
"v_pk_mul_f32 v[76:77], v[76:77], v[44:45]\n\t" \
"v_pk_mul_f32 v[78:79], v[78:79], v[46:47]\n\t" \
"v_cvt_pk_bf16_f32 v40, v72, v73\n\t" \
"v_cvt_pk_bf16_f32 v41, v74, v75\n\t" \
"v_cvt_pk_bf16_f32 v42, v76, v77\n\t" \
"v_cvt_pk_bf16_f32 v43, v78, v79\n\t" \
"buffer_store_dwordx4 v[40:43], v1, s[28:31], s77 offen offset:1024 sc1\n\t" \
"v_pk_fma_f32 v[16:17], v[8:9], v[48:49], v[16:17] op_sel_hi:[0,1,1]\n\t" \
"v_pk_fma_f32 v[18:19], v[8:9], v[50:51], v[18:19] op_sel_hi:[0,1,1]\n\t" \
"v_pk_fma_f32 v[20:21], v[8:9], v[52:53], v[20:21] op_sel_hi:[0,1,1]\n\t" \
"v_pk_fma_f32 v[22:23], v[8:9], v[54:55], v[22:23] op_sel_hi:[0,1,1]\n\t" \
"v_pk_mul_f32 v[48:49], v[16:17], v[16:17]\n\t" \
"v_pk_mul_f32 v[50:51], v[18:19], v[18:19]\n\t" \
"v_pk_mul_f32 v[52:53], v[20:21], v[20:21]\n\t" \
"v_pk_mul_f32 v[54:55], v[22:23], v[22:23]\n\t" \
"v_pk_fma_f32 v[48:49], v[48:49], s[68:69], v[10:11] op_sel_hi:[1,1,0]\n\t" \
"v_pk_fma_f32 v[50:51], v[50:51], s[68:69], v[10:11] op_sel_hi:[1,1,0]\n\t" \
"v_pk_fma_f32 v[52:53], v[52:53], s[68:69], v[10:11] op_sel_hi:[1,1,0]\n\t" \
"v_pk_fma_f32 v[54:55], v[54:55], s[68:69], v[10:11] op_sel_hi:[1,1,0]\n\t" \
"v_pk_mul_f32 v[48:49], v[48:49], v[16:17]\n\t" \
"v_pk_mul_f32 v[50:51], v[50:51], v[18:19]\n\t" \
"v_pk_mul_f32 v[52:53], v[52:53], v[20:21]\n\t" \
"v_pk_mul_f32 v[54:55], v[54:55], v[22:23]\n\t" \
"v_exp_f32 v48, v48\n\t" \
"v_exp_f32 v49, v49\n\t" \
"v_exp_f32 v50, v50\n\t" \
"v_exp_f32 v51, v51\n\t" \
"v_exp_f32 v52, v52\n\t" \
"v_exp_f32 v53, v53\n\t" \
"v_exp_f32 v54, v54\n\t" \
"v_exp_f32 v55, v55\n\t" \
"v_pk_add_f32 v[48:49], v[48:49], s[70:71]\n\t" \
"v_pk_add_f32 v[50:51], v[50:51], s[70:71]\n\t" \
"v_pk_add_f32 v[52:53], v[52:53], s[70:71]\n\t" \
"v_pk_add_f32 v[54:55], v[54:55], s[70:71]\n\t" \
"v_rcp_f32 v48, v48\n\t" \
"v_rcp_f32 v49, v49\n\t" \
"v_rcp_f32 v50, v50\n\t" \
"v_rcp_f32 v51, v51\n\t" \
"v_rcp_f32 v52, v52\n\t" \
"v_rcp_f32 v53, v53\n\t" \
"v_rcp_f32 v54, v54\n\t" \
"v_rcp_f32 v55, v55\n\t" \
"v_pk_mul_f32 v[16:17], v[16:17], v[48:49]\n\t" \
"v_pk_mul_f32 v[18:19], v[18:19], v[50:51]\n\t" \
"v_pk_mul_f32 v[20:21], v[20:21], v[52:53]\n\t" \
"v_pk_mul_f32 v[22:23], v[22:23], v[54:55]\n\t" \
"v_cvt_pk_bf16_f32 v48, v16, v17\n\t" \
"v_cvt_pk_bf16_f32 v49, v18, v19\n\t" \
"v_cvt_pk_bf16_f32 v50, v20, v21\n\t" \
"v_cvt_pk_bf16_f32 v51, v22, v23\n\t" \
"buffer_store_dwordx4 v[48:51], v1, s[28:31], s77 offen offset:2048 sc1\n\t" \
"v_pk_fma_f32 v[24:25], v[8:9], v[56:57], v[24:25] op_sel_hi:[0,1,1]\n\t" \
"v_pk_fma_f32 v[26:27], v[8:9], v[58:59], v[26:27] op_sel_hi:[0,1,1]\n\t" \
"v_pk_fma_f32 v[28:29], v[8:9], v[60:61], v[28:29] op_sel_hi:[0,1,1]\n\t" \
"v_pk_fma_f32 v[30:31], v[8:9], v[62:63], v[30:31] op_sel_hi:[0,1,1]\n\t" \
"v_pk_mul_f32 v[56:57], v[24:25], v[24:25]\n\t" \
"v_pk_mul_f32 v[58:59], v[26:27], v[26:27]\n\t" \
"v_pk_mul_f32 v[60:61], v[28:29], v[28:29]\n\t" \
"v_pk_mul_f32 v[62:63], v[30:31], v[30:31]\n\t" \
"v_pk_fma_f32 v[56:57], v[56:57], s[68:69], v[10:11] op_sel_hi:[1,1,0]\n\t" \
"v_pk_fma_f32 v[58:59], v[58:59], s[68:69], v[10:11] op_sel_hi:[1,1,0]\n\t" \
"v_pk_fma_f32 v[60:61], v[60:61], s[68:69], v[10:11] op_sel_hi:[1,1,0]\n\t" \
"v_pk_fma_f32 v[62:63], v[62:63], s[68:69], v[10:11] op_sel_hi:[1,1,0]\n\t" \
"v_pk_mul_f32 v[56:57], v[56:57], v[24:25]\n\t" \
"v_pk_mul_f32 v[58:59], v[58:59], v[26:27]\n\t" \
"v_pk_mul_f32 v[60:61], v[60:61], v[28:29]\n\t" \
"v_pk_mul_f32 v[62:63], v[62:63], v[30:31]\n\t" \
"v_exp_f32 v56, v56\n\t" \
"v_exp_f32 v57, v57\n\t" \
"v_exp_f32 v58, v58\n\t" \
"v_exp_f32 v59, v59\n\t" \
"v_exp_f32 v60, v60\n\t" \
"v_exp_f32 v61, v61\n\t" \
"v_exp_f32 v62, v62\n\t" \
"v_exp_f32 v63, v63\n\t" \
"v_pk_add_f32 v[56:57], v[56:57], s[70:71]\n\t" \
"v_pk_add_f32 v[58:59], v[58:59], s[70:71]\n\t" \
"v_pk_add_f32 v[60:61], v[60:61], s[70:71]\n\t" \
"v_pk_add_f32 v[62:63], v[62:63], s[70:71]\n\t" \
"v_rcp_f32 v56, v56\n\t" \
"v_rcp_f32 v57, v57\n\t" \
"v_rcp_f32 v58, v58\n\t" \
"v_rcp_f32 v59, v59\n\t" \
"v_rcp_f32 v60, v60\n\t" \
"v_rcp_f32 v61, v61\n\t" \
"v_rcp_f32 v62, v62\n\t" \
"v_rcp_f32 v63, v63\n\t" \
"v_pk_mul_f32 v[24:25], v[24:25], v[56:57]\n\t" \
"v_pk_mul_f32 v[26:27], v[26:27], v[58:59]\n\t" \
"v_pk_mul_f32 v[28:29], v[28:29], v[60:61]\n\t" \
"v_pk_mul_f32 v[30:31], v[30:31], v[62:63]\n\t" \
"v_cvt_pk_bf16_f32 v56, v24, v25\n\t" \
"v_cvt_pk_bf16_f32 v57, v26, v27\n\t" \
"v_cvt_pk_bf16_f32 v58, v28, v29\n\t" \
"v_cvt_pk_bf16_f32 v59, v30, v31\n\t" \
"buffer_store_dwordx4 v[56:59], v1, s[28:31], s77 offen offset:3072 sc1\n\t" \
"s_waitcnt vmcnt(2)\n\t" \
"v_mov_b32 v12, s44\n\t" \
"v_mov_b32 v13, s45\n\t" \
"v_mov_b32 v14, s44\n\t" \
"v_mov_b32 v15, s45\n\t" \
"s_mov_b64 exec, 0xff\n\t" \
"buffer_store_dwordx4 v[12:15], v1, s[28:31], s76 offen sc1\n\t" \
"s_mov_b64 exec, -1\n\t" \
"s_add_u32 s76, s76, 2048\n\t" \
"ds_write_b128 v3, v[176:179] offset:16384\n\t" \
"ds_write_b128 v3, v[180:183] offset:17408\n\t" \
"ds_write_b128 v3, v[184:187] offset:18432\n\t" \
"ds_write_b128 v3, v[188:191] offset:19456\n\t" \
"s_waitcnt vmcnt(1)\n\t" \
"v_mov_b32 v64, s44\n\t" \
"v_mov_b32 v65, s45\n\t" \
"v_mov_b32 v66, s44\n\t" \
"v_mov_b32 v67, s45\n\t" \
"s_mov_b64 exec, 0xff\n\t" \
"buffer_store_dwordx4 v[64:67], v1, s[28:31], s76 offen sc1\n\t" \
"s_mov_b64 exec, -1\n\t" \
"s_add_u32 s76, s76, 2048\n\t" \
"s_setprio 0\n\t" \
"s_branch Lbm1done_%=\n\t" \
"Lbnom1_%=:\n\t" \
"s_mov_b32 s38, 1835008\n\t" \
"s_add_u32 s63, s53, 1114112\n\t" \
"buffer_load_dwordx4 v[176:179], v11, s[36:39], s63 offen\n\t" \
"s_add_u32 s63, s53, 1122304\n\t" \
"buffer_load_dwordx4 v[180:183], v11, s[36:39], s63 offen\n\t" \
"s_add_u32 s63, s53, 1130496\n\t" \
"buffer_load_dwordx4 v[184:187], v11, s[36:39], s63 offen\n\t" \
"s_add_u32 s63, s53, 1138688\n\t" \
"buffer_load_dwordx4 v[188:191], v11, s[36:39], s63 offen\n\t" \
"s_add_u32 s63, s83, 1638400\n\t" \
"buffer_load_dwordx4 v[192:195], v11, s[36:39], s63 offen\n\t" \
"s_add_u32 s63, s83, 1646592\n\t" \
"buffer_load_dwordx4 v[196:199], v11, s[36:39], s63 offen\n\t" \
"s_add_u32 s63, s83, 1654784\n\t" \
"buffer_load_dwordx4 v[200:203], v11, s[36:39], s63 offen\n\t" \
"s_add_u32 s63, s83, 1662976\n\t" \
"buffer_load_dwordx4 v[204:207], v11, s[36:39], s63 offen\n\t" \
"s_add_u32 s63, s53, 360448\n\t" \
"buffer_load_dwordx4 v[208:211], v11, s[36:39], s63 offen\n\t" \
"s_add_u32 s63, s53, 368640\n\t" \
"buffer_load_dwordx4 v[212:215], v11, s[36:39], s63 offen\n\t" \
"s_add_u32 s63, s53, 376832\n\t" \
"buffer_load_dwordx4 v[216:219], v11, s[36:39], s63 offen\n\t" \
"s_add_u32 s63, s53, 385024\n\t" \
"buffer_load_dwordx4 v[220:223], v11, s[36:39], s63 offen\n\t" \
"s_add_u32 s63, s53, 884736\n\t" \
"buffer_load_dwordx4 v[224:227], v11, s[36:39], s63 offen\n\t" \
"s_add_u32 s63, s53, 892928\n\t" \
"buffer_load_dwordx4 v[228:231], v11, s[36:39], s63 offen\n\t" \
"s_add_u32 s63, s53, 901120\n\t" \
"buffer_load_dwordx4 v[232:235], v11, s[36:39], s63 offen\n\t" \
"s_add_u32 s63, s53, 909312\n\t" \
"buffer_load_dwordx4 v[236:239], v11, s[36:39], s63 offen\n\t" \
"s_add_u32 s63, s53, 1409024\n\t" \
"buffer_load_dwordx4 v[240:243], v11, s[36:39], s63 offen\n\t" \
"s_add_u32 s63, s53, 1417216\n\t" \
"buffer_load_dwordx4 v[244:247], v11, s[36:39], s63 offen\n\t" \
"s_add_u32 s63, s53, 1425408\n\t" \
"buffer_load_dwordx4 v[248:251], v11, s[36:39], s63 offen\n\t" \
"s_add_u32 s63, s53, 1433600\n\t" \
"buffer_load_dwordx4 v[252:255], v11, s[36:39], s63 offen\n\t" \
"s_waitcnt vmcnt(0)\n\t" \
"s_barrier\n\t" \
"s_barrier\n\t" \
"s_barrier\n\t" \
"ds_write_b128 v3, v[176:179] offset:16384\n\t" \
"ds_write_b128 v3, v[180:183] offset:17408\n\t" \
"ds_write_b128 v3, v[184:187] offset:18432\n\t" \
"ds_write_b128 v3, v[188:191] offset:19456\n\t" \
"Lbm1done_%=:\n\t" \
"s_barrier\n\t" \
"ds_write_b128 v3, v[192:195] offset:0\n\t" \
"s_nop 7\n\t" \
"ds_write_b128 v3, v[196:199] offset:1024\n\t" \
"s_nop 7\n\t" \
"ds_write_b128 v3, v[200:203] offset:2048\n\t" \
"s_nop 7\n\t" \
"ds_write_b128 v3, v[204:207] offset:3072\n\t" \
"s_nop 7\n\t" \
"ds_write_b128 v6, v[208:211] offset:0\n\t" \
"s_nop 7\n\t" \
"ds_write_b128 v6, v[212:215] offset:1024\n\t" \
"s_nop 7\n\t" \
"ds_write_b128 v6, v[216:219] offset:2048\n\t" \
"s_nop 7\n\t" \
"ds_write_b128 v6, v[220:223] offset:3072\n\t" \
"s_nop 7\n\t" \
"ds_write_b128 v6, v[224:227] offset:16384\n\t" \
"s_nop 7\n\t" \
"ds_write_b128 v6, v[228:231] offset:17408\n\t" \
"s_nop 7\n\t" \
"ds_write_b128 v6, v[232:235] offset:18432\n\t" \
"s_nop 7\n\t" \
"ds_write_b128 v6, v[236:239] offset:19456\n\t" \
"s_nop 7\n\t" \
"ds_write_b128 v6, v[240:243] offset:32768\n\t" \
"s_nop 7\n\t" \
"ds_write_b128 v6, v[244:247] offset:33792\n\t" \
"s_nop 7\n\t" \
"ds_write_b128 v6, v[248:251] offset:34816\n\t" \
"s_nop 7\n\t" \
"ds_write_b128 v6, v[252:255] offset:35840\n\t" \
"s_nop 7\n\t" \
"s_lshl_b32 s58, s55, 7\n\t" \
"s_add_u32 s58, s58, 81920\n\t" \
"v_and_b32 v12, 31, v0\n\t" \
"v_lshrrev_b32 v13, 5, v0\n\t" \
"v_and_b32 v18, 7, v0\n\t" \
"v_xor_b32 v13, v13, v18\n\t" \
"v_lshlrev_b32 v13, 4, v13\n\t" \
"v_lshl_add_u32 v13, v12, 8, v13\n\t" \
"s_and_b32 s57, s56, 1\n\t" \
"s_lshl_b32 s57, s57, 13\n\t" \
"s_add_u32 s57, s57, s58\n\t" \
"v_add_u32 v18, s57, v13\n\t" \
"v_xor_b32 v19, 32, v18\n\t" \
"v_xor_b32 v20, 64, v18\n\t" \
"v_xor_b32 v21, 96, v18\n\t" \
"v_lshrrev_b32 v14, 3, v0\n\t" \
"v_and_b32 v15, 7, v0\n\t" \
"v_lshlrev_b32 v16, 1, v15\n\t" \
"v_xor_b32 v16, v16, v14\n\t" \
"v_lshlrev_b32 v16, 4, v16\n\t" \
"v_lshl_add_u32 v16, v14, 8, v16\n\t" \
"s_lshl_b32 s57, s26, 11\n\t" \
"s_add_u32 s57, s57, 49152\n\t" \
"v_add_u32 v16, s57, v16\n\t" \
"v_xor_b32 v17, 16, v16\n\t" \
"v_lshlrev_b32 v15, 4, v15\n\t" \
"v_lshl_add_u32 v14, v14, 10, v15\n\t" \
"s_lshl_b32 s57, s51, 16\n\t" \
"s_lshl_b32 s58, s26, 13\n\t" \
"s_add_u32 s57, s57, s58\n\t" \
"s_lshl_b32 s58, s50, 7\n\t" \
"s_add_u32 s63, s57, s58\n\t" \
"s_mov_b32 s40, s22\n\t" \
"s_and_b32 s41, s23, 0xffff\n\t" \
"s_mov_b32 s42, 2097152\n\t" \
"s_mov_b32 s43, 0x20000\n\t" \
"s_waitcnt lgkmcnt(0)\n\t" \
"s_barrier\n\t" \
"ds_write_b128 v18, v[80:83]\n\t" \
"ds_write_b128 v19, v[84:87]\n\t" \
"ds_write_b128 v20, v[88:91]\n\t" \
"ds_write_b128 v21, v[92:95]\n\t" \
"Lfinal_%=:\n\t" \
"s_waitcnt lgkmcnt(0)\n\t" \
"s_barrier\n\t" \
"ds_read_b128 v[20:23], v16 offset:0\n\t" \
"ds_read_b128 v[28:31], v16 offset:16384\n\t" \
"ds_read_b128 v[36:39], v16 offset:32768\n\t" \
"ds_read_b128 v[24:27], v17 offset:0\n\t" \
"ds_read_b128 v[32:35], v17 offset:16384\n\t" \
"ds_read_b128 v[40:43], v17 offset:32768\n\t" \
"s_waitcnt lgkmcnt(3)\n\t" \
"v_add_f32 v20, v20, v28\n\t" \
"v_add_f32 v21, v21, v29\n\t" \
"v_add_f32 v22, v22, v30\n\t" \
"v_add_f32 v23, v23, v31\n\t" \
"v_add_f32 v20, v20, v36\n\t" \
"v_add_f32 v21, v21, v37\n\t" \
"v_add_f32 v22, v22, v38\n\t" \
"v_add_f32 v23, v23, v39\n\t" \
"v_cvt_pk_bf16_f32 v20, v20, v21\n\t" \
"v_cvt_pk_bf16_f32 v21, v22, v23\n\t" \
"s_waitcnt lgkmcnt(0)\n\t" \
"v_add_f32 v24, v24, v32\n\t" \
"v_add_f32 v25, v25, v33\n\t" \
"v_add_f32 v26, v26, v34\n\t" \
"v_add_f32 v27, v27, v35\n\t" \
"v_add_f32 v24, v24, v40\n\t" \
"v_add_f32 v25, v25, v41\n\t" \
"v_add_f32 v26, v26, v42\n\t" \
"v_add_f32 v27, v27, v43\n\t" \
"v_cvt_pk_bf16_f32 v22, v24, v25\n\t" \
"v_cvt_pk_bf16_f32 v23, v26, v27\n\t" \
"buffer_store_dwordx4 v[20:23], v14, s[40:43], s63 offen sc1\n\t" \
"s_or_b32 s57, s27, s26\n\t" \
"s_cmp_lg_u32 s57, 0\n\t" \
"s_cbranch_scc1 Lend_%=\n\t" \
"v_mov_b32 v24, s44\n\t" \
"v_mov_b32 v25, s45\n\t" \
"v_mov_b32 v26, s44\n\t" \
"v_mov_b32 v27, s45\n\t" \
"s_mov_b64 exec, 0xff\n\t" \
"buffer_store_dwordx4 v[24:27], v1, s[28:31], s57 offen sc1\n\t" \
"s_mov_b64 exec, -1\n\t" \
"Lend_%=:\n\t" \
"s_endpgm\n\t" \
"Lspinf_%=:\n\t" \
"s_mov_b32 s65, 0x40000\n\t" \
"Lpoll2_%=:\n\t" \
"buffer_load_dwordx2 v[16:17], v19, s[28:31], s88 offen sc1\n\t" \
"s_waitcnt vmcnt(0)\n\t" \
"v_cmp_ne_u64 vcc, s[44:45], v[16:17]\n\t" \
"s_nop 0\n\t" \
"s_cmp_eq_u64 vcc, 0\n\t" \
"s_cbranch_scc1 Lpdone2_%=\n\t" \
"s_sleep 1\n\t" \
"s_sub_u32 s65, s65, 1\n\t" \
"s_cmp_lg_u32 s65, 0\n\t" \
"s_cbranch_scc1 Lpoll2_%=\n\t" \
"Lpdone2_%=:\n\t" \
"s_branch Lspunf_%=\n\t" \
"Lspin0_%=:\n\t" \
"s_mov_b32 s65, 0x40000\n\t" \
"Lpoll4_%=:\n\t" \
"buffer_load_dwordx2 v[14:15], v13, s[28:31], s57 offen sc1\n\t" \
"s_waitcnt vmcnt(0)\n\t" \
"v_cmp_ne_u64 vcc, s[44:45], v[14:15]\n\t" \
"s_nop 0\n\t" \
"s_cmp_eq_u64 vcc, 0\n\t" \
"s_cbranch_scc1 Lpdone4_%=\n\t" \
"s_sleep 6\n\t" \
"s_sub_u32 s65, s65, 1\n\t" \
"s_cmp_lg_u32 s65, 0\n\t" \
"s_cbranch_scc1 Lpoll4_%=\n\t" \
"Lpdone4_%=:\n\t" \
"s_branch Lspun0_%=\n\t" \
"Lspin1_%=:\n\t" \
"s_mov_b32 s65, 0x40000\n\t" \
"Lpoll5_%=:\n\t" \
"buffer_load_dwordx2 v[14:15], v13, s[28:31], s57 offen sc1\n\t" \
"s_waitcnt vmcnt(0)\n\t" \
"v_cmp_ne_u64 vcc, s[44:45], v[14:15]\n\t" \
"s_nop 0\n\t" \
"s_cmp_eq_u64 vcc, 0\n\t" \
"s_cbranch_scc1 Lpdone5_%=\n\t" \
"s_sleep 6\n\t" \
"s_sub_u32 s65, s65, 1\n\t" \
"s_cmp_lg_u32 s65, 0\n\t" \
"s_cbranch_scc1 Lpoll5_%=\n\t" \
"Lpdone5_%=:\n\t" \
"s_branch Lspun1_%=\n\t" \
"Lspin2_%=:\n\t" \
"s_mov_b32 s65, 0x40000\n\t" \
"Lpoll6_%=:\n\t" \
"buffer_load_dwordx2 v[14:15], v13, s[28:31], s57 offen sc1\n\t" \
"s_waitcnt vmcnt(0)\n\t" \
"v_cmp_ne_u64 vcc, s[44:45], v[14:15]\n\t" \
"s_nop 0\n\t" \
"s_cmp_eq_u64 vcc, 0\n\t" \
"s_cbranch_scc1 Lpdone6_%=\n\t" \
"s_sleep 6\n\t" \
"s_sub_u32 s65, s65, 1\n\t" \
"s_cmp_lg_u32 s65, 0\n\t" \
"s_cbranch_scc1 Lpoll6_%=\n\t" \
"Lpdone6_%=:\n\t" \
"s_branch Lspun2_%=\n\t" \
"Lspin3_%=:\n\t" \
"s_mov_b32 s65, 0x40000\n\t" \
"Lpoll7_%=:\n\t" \
"buffer_load_dwordx2 v[14:15], v13, s[28:31], s57 offen sc1\n\t" \
"s_waitcnt vmcnt(0)\n\t" \
"v_cmp_ne_u64 vcc, s[44:45], v[14:15]\n\t" \
"s_nop 0\n\t" \
"s_cmp_eq_u64 vcc, 0\n\t" \
"s_cbranch_scc1 Lpdone7_%=\n\t" \
"s_sleep 6\n\t" \
"s_sub_u32 s65, s65, 1\n\t" \
"s_cmp_lg_u32 s65, 0\n\t" \
"s_cbranch_scc1 Lpoll7_%=\n\t" \
"Lpdone7_%=:\n\t" \
"s_branch Lspun3_%=\n\t" \
"Lslow_%=:\n\t" \
"s_waitcnt lgkmcnt(0)\n\t" \
"v_mov_b32 v32, 0\n\t" \
"v_mov_b32 v33, 0\n\t" \
"v_mov_b32 v34, 0\n\t" \
"v_mov_b32 v35, 0\n\t" \
"v_mov_b32 v36, 0\n\t" \
"v_mov_b32 v37, 0\n\t" \
"v_mov_b32 v38, 0\n\t" \
"v_mov_b32 v39, 0\n\t" \
"v_mov_b32 v40, 0\n\t" \
"v_mov_b32 v41, 0\n\t" \
"v_mov_b32 v42, 0\n\t" \
"v_mov_b32 v43, 0\n\t" \
"v_mov_b32 v44, 0\n\t" \
"v_mov_b32 v45, 0\n\t" \
"v_mov_b32 v46, 0\n\t" \
"v_mov_b32 v47, 0\n\t" \
"v_mov_b32 v48, 0\n\t" \
"v_mov_b32 v49, 0\n\t" \
"v_mov_b32 v50, 0\n\t" \
"v_mov_b32 v51, 0\n\t" \
"v_mov_b32 v52, 0\n\t" \
"v_mov_b32 v53, 0\n\t" \
"v_mov_b32 v54, 0\n\t" \
"v_mov_b32 v55, 0\n\t" \
"v_mov_b32 v56, 0\n\t" \
"v_mov_b32 v57, 0\n\t" \
"v_mov_b32 v58, 0\n\t" \
"v_mov_b32 v59, 0\n\t" \
"v_mov_b32 v60, 0\n\t" \
"v_mov_b32 v61, 0\n\t" \
"v_mov_b32 v62, 0\n\t" \
"v_mov_b32 v63, 0\n\t" \
"v_mov_b32 v8, 0xff800000\n\t" \
"v_mov_b32 v9, 0xff800000\n\t" \
"s_mov_b32 s59, s78\n\t" \
"s_mov_b32 s65, s77\n\t" \
"Lslowl_%=:\n\t" \
"s_cmp_eq_u32 s65, 32\n\t" \
"s_cselect_b32 s59, s76, s59\n\t" \
"buffer_load_dwordx4 v[128:131], v1, s[28:31], s59 offen sc1\n\t" \
"s_mov_b64 exec, s[94:95]\n\t" \
"buffer_load_dwordx4 v[144:147], v6, s[28:31], s59 offen offset:1024 sc1\n\t" \
"buffer_load_dwordx4 v[160:163], v6, s[28:31], s59 offen offset:1536 sc1\n\t" \
"s_mov_b64 exec, -1\n\t" \
"s_add_u32 s59, s59, 2048\n\t" \
"s_waitcnt vmcnt(0)\n\t" \
"v_mfma_f32_32x32x16_bf16 v[64:79], v[128:131], v[24:27], 0\n\t" \
"s_nop 15\n\t" \
"v_max3_f32 v12, v64, v65, v66\n\t" \
"v_max3_f32 v12, v12, v67, v68\n\t" \
"v_max3_f32 v12, v12, v69, v70\n\t" \
"v_max3_f32 v12, v12, v71, v72\n\t" \
"v_max3_f32 v12, v12, v73, v74\n\t" \
"v_max3_f32 v12, v12, v75, v76\n\t" \
"v_max3_f32 v12, v12, v77, v78\n\t" \
"v_max_f32 v12, v12, v79\n\t" \
"v_mov_b32 v10, v12\n\t" \
"s_nop 1\n\t" \
"v_permlane32_swap_b32 v10, v12\n\t" \
"v_max_f32 v12, v12, v10\n\t" \
"v_max_f32 v12, v12, v8\n\t" \
"v_sub_f32 v10, v8, v12\n\t" \
"v_exp_f32 v10, v10\n\t" \
"v_mov_b32 v8, v12\n\t" \
"v_sub_f32 v64, v64, v12\n\t" \
"v_sub_f32 v65, v65, v12\n\t" \
"v_sub_f32 v66, v66, v12\n\t" \
"v_sub_f32 v67, v67, v12\n\t" \
"v_sub_f32 v68, v68, v12\n\t" \
"v_sub_f32 v69, v69, v12\n\t" \
"v_sub_f32 v70, v70, v12\n\t" \
"v_sub_f32 v71, v71, v12\n\t" \
"v_sub_f32 v72, v72, v12\n\t" \
"v_sub_f32 v73, v73, v12\n\t" \
"v_sub_f32 v74, v74, v12\n\t" \
"v_sub_f32 v75, v75, v12\n\t" \
"v_sub_f32 v76, v76, v12\n\t" \
"v_sub_f32 v77, v77, v12\n\t" \
"v_sub_f32 v78, v78, v12\n\t" \
"v_sub_f32 v79, v79, v12\n\t" \
"v_exp_f32 v64, v64\n\t" \
"v_exp_f32 v65, v65\n\t" \
"v_exp_f32 v66, v66\n\t" \
"v_exp_f32 v67, v67\n\t" \
"v_exp_f32 v68, v68\n\t" \
"v_exp_f32 v69, v69\n\t" \
"v_exp_f32 v70, v70\n\t" \
"v_exp_f32 v71, v71\n\t" \
"v_exp_f32 v72, v72\n\t" \
"v_exp_f32 v73, v73\n\t" \
"v_exp_f32 v74, v74\n\t" \
"v_exp_f32 v75, v75\n\t" \
"v_exp_f32 v76, v76\n\t" \
"v_exp_f32 v77, v77\n\t" \
"v_exp_f32 v78, v78\n\t" \
"v_exp_f32 v79, v79\n\t" \
"v_mul_f32 v32, v32, v10\n\t" \
"v_mul_f32 v33, v33, v10\n\t" \
"v_mul_f32 v34, v34, v10\n\t" \
"v_mul_f32 v35, v35, v10\n\t" \
"v_mul_f32 v36, v36, v10\n\t" \
"v_mul_f32 v37, v37, v10\n\t" \
"v_mul_f32 v38, v38, v10\n\t" \
"v_mul_f32 v39, v39, v10\n\t" \
"v_mul_f32 v40, v40, v10\n\t" \
"s_nop 1\n\t" \
"v_smfmac_f32_32x32x32_bf16 v[32:47], v[144:147], v[64:71], v7\n\t" \
"v_smfmac_f32_32x32x32_bf16 v[32:47], v[160:163], v[72:79], v7\n\t" \
"v_mfma_f32_32x32x16_bf16 v[64:79], v[128:131], v[28:31], 0\n\t" \
"s_nop 15\n\t" \
"v_max3_f32 v12, v64, v65, v66\n\t" \
"v_max3_f32 v12, v12, v67, v68\n\t" \
"v_max3_f32 v12, v12, v69, v70\n\t" \
"v_max3_f32 v12, v12, v71, v72\n\t" \
"v_max3_f32 v12, v12, v73, v74\n\t" \
"v_max3_f32 v12, v12, v75, v76\n\t" \
"v_max3_f32 v12, v12, v77, v78\n\t" \
"v_max_f32 v12, v12, v79\n\t" \
"v_mov_b32 v10, v12\n\t" \
"s_nop 1\n\t" \
"v_permlane32_swap_b32 v10, v12\n\t" \
"v_max_f32 v12, v12, v10\n\t" \
"v_max_f32 v12, v12, v9\n\t" \
"v_sub_f32 v10, v9, v12\n\t" \
"v_exp_f32 v10, v10\n\t" \
"v_mov_b32 v9, v12\n\t" \
"v_sub_f32 v64, v64, v12\n\t" \
"v_sub_f32 v65, v65, v12\n\t" \
"v_sub_f32 v66, v66, v12\n\t" \
"v_sub_f32 v67, v67, v12\n\t" \
"v_sub_f32 v68, v68, v12\n\t" \
"v_sub_f32 v69, v69, v12\n\t" \
"v_sub_f32 v70, v70, v12\n\t" \
"v_sub_f32 v71, v71, v12\n\t" \
"v_sub_f32 v72, v72, v12\n\t" \
"v_sub_f32 v73, v73, v12\n\t" \
"v_sub_f32 v74, v74, v12\n\t" \
"v_sub_f32 v75, v75, v12\n\t" \
"v_sub_f32 v76, v76, v12\n\t" \
"v_sub_f32 v77, v77, v12\n\t" \
"v_sub_f32 v78, v78, v12\n\t" \
"v_sub_f32 v79, v79, v12\n\t" \
"v_exp_f32 v64, v64\n\t" \
"v_exp_f32 v65, v65\n\t" \
"v_exp_f32 v66, v66\n\t" \
"v_exp_f32 v67, v67\n\t" \
"v_exp_f32 v68, v68\n\t" \
"v_exp_f32 v69, v69\n\t" \
"v_exp_f32 v70, v70\n\t" \
"v_exp_f32 v71, v71\n\t" \
"v_exp_f32 v72, v72\n\t" \
"v_exp_f32 v73, v73\n\t" \
"v_exp_f32 v74, v74\n\t" \
"v_exp_f32 v75, v75\n\t" \
"v_exp_f32 v76, v76\n\t" \
"v_exp_f32 v77, v77\n\t" \
"v_exp_f32 v78, v78\n\t" \
"v_exp_f32 v79, v79\n\t" \
"v_mul_f32 v48, v48, v10\n\t" \
"v_mul_f32 v49, v49, v10\n\t" \
"v_mul_f32 v50, v50, v10\n\t" \
"v_mul_f32 v51, v51, v10\n\t" \
"v_mul_f32 v52, v52, v10\n\t" \
"v_mul_f32 v53, v53, v10\n\t" \
"v_mul_f32 v54, v54, v10\n\t" \
"v_mul_f32 v55, v55, v10\n\t" \
"v_mul_f32 v56, v56, v10\n\t" \
"s_nop 1\n\t" \
"v_smfmac_f32_32x32x32_bf16 v[48:63], v[144:147], v[64:71], v7\n\t" \
"v_smfmac_f32_32x32x32_bf16 v[48:63], v[160:163], v[72:79], v7\n\t" \
"s_nop 15\n\t" \
"s_sub_u32 s65, s65, 1\n\t" \
"s_cmp_lg_u32 s65, 0\n\t" \
"s_cbranch_scc1 Lslowl_%=\n\t" \
"v_mov_b32 v41, v8\n\t" \
"ds_write_b128 v14, v[32:35]\n\t" \
"ds_write_b128 v14, v[36:39] offset:1024\n\t" \
"ds_write_b64 v15, v[40:41] offset:2048\n\t" \
"s_branch Lback_%=\n\t"
#define CLOB "v0","v1","v2","v3","v4","v5","v6","v7","v8","v9","v10","v11","v12","v13","v14","v15","v16","v17","v18","v19","v20","v21","v22","v23","v24","v25","v26","v27","v28","v29","v30","v31","v32","v33","v34","v35","v36","v37","v38","v39","v40","v41","v42","v43","v44","v45","v46","v47","v48","v49","v50","v51","v52","v53","v54","v55","v56","v57","v58","v59","v60","v61","v62","v63","v64","v65","v66","v67","v68","v69","v70","v71","v72","v73","v74","v75","v76","v77","v78","v79","v80","v81","v82","v83","v84","v85","v86","v87","v88","v89","v90","v91","v92","v93","v94","v95","v96","v97","v98","v99","v100","v101","v102","v103","v104","v105","v106","v107","v108","v109","v110","v111","v112","v113","v114","v115","v116","v117","v118","v119","v120","v121","v122","v123","v124","v125","v126","v127","v128","v129","v130","v131","v132","v133","v134","v135","v136","v137","v138","v139","v140","v141","v142","v143","v144","v145","v146","v147","v148","v149","v150","v151","v152","v153","v154","v155","v156","v157","v158","v159","v160","v161","v162","v163","v164","v165","v166","v167","v168","v169","v170","v171","v172","v173","v174","v175","v176","v177","v178","v179","v180","v181","v182","v183","v184","v185","v186","v187","v188","v189","v190","v191","v192","v193","v194","v195","v196","v197","v198","v199","v200","v201","v202","v203","v204","v205","v206","v207","v208","v209","v210","v211","v212","v213","v214","v215","v216","v217","v218","v219","v220","v221","v222","v223","v224","v225","v226","v227","v228","v229","v230","v231","v232","v233","v234","v235","v236","v237","v238","v239","v240","v241","v242","v243","v244","v245","v246","v247","v248","v249","v250","v251","v252","v253","v254","v255","s8","s9","s10","s11","s12","s13","s14","s15","s16","s17","s18","s19","s20","s21","s22","s23","s24","s25","s26","s27","s28","s29","s30","s31","s33","s34","s35","s36","s37","s38","s39","s40","s41","s42","s43","s44","s45","s46","s47","s48","s49","s50","s51","s52","s53","s54","s55","s56","s57","s58","s59","s60","s61","s62","s63","s64","s65","s66","s67","s68","s69","s70","s71","s72","s73","s74","s75","s76","s77","s78","s79","s80","s81","s82","s83","s84","s85","s86","s87","s88","s89","s90","s91","s92","s93","s94","s95","s96","s97"

constexpr unsigned LDS_BYTES = 163840;
struct Params { const void* x; const void* wqkv; const void* wo; const void* w1; const void* b1; const void* w2; const void* b2; void* out; void* ws; };

__global__ __launch_bounds__(512, 2) void fused_block(Params p) {
    const int wid = __builtin_amdgcn_readfirstlane(threadIdx.x >> 6);
    asm volatile(BODY :: [ka] "s"(__builtin_amdgcn_kernarg_segment_ptr()), [wid] "s"(wid), [bid] "s"(blockIdx.x) : "memory", "scc", "vcc", CLOB);
}

extern "C" void kernel_launch(void* const* d_in, const int* in_sizes, int n_in, void* d_out, int out_size, void* d_ws, size_t ws_size, hipStream_t stream) {
    Params p;
    p.x = d_in[0]; p.wqkv = d_in[1]; p.wo = d_in[2]; p.w1 = d_in[4]; p.b1 = d_in[5]; p.w2 = d_in[6]; p.b2 = d_in[7]; p.out = d_out; p.ws = d_ws;
    (void)hipFuncSetAttribute((const void*)fused_block, hipFuncAttributeMaxDynamicSharedMemorySize, LDS_BYTES);
    fused_block<<<256, 512, LDS_BYTES, stream>>>(p);
}
```
